```python
import torch

def _softmax(x, axis=-1):
    return torch.softmax(x, dim=axis)
def _gelu(x):
    return torch.nn.functional.gelu(x, approximate="tanh")
def _silu(x):
    return torch.nn.functional.silu(x)
def _sigmoid(x):
    return torch.sigmoid(x)
def _layernorm(x, axis=-1, eps=1e-5):
    m = x.mean(dim=axis, keepdim=True)
    v = x.var(dim=axis, keepdim=True, unbiased=False)
    return (x - m) / torch.sqrt(v + eps)
def _scatter_add(tgt, idx, src, axis=0):
    return tgt.clone().index_add_(axis, idx, src)
def _segment_sum(src, seg, n):
    out = torch.zeros((n,) + src.shape[1:], dtype=src.dtype)
    return out.index_add_(0, seg, src)
def _pad(x, pw):
    flat = tuple(v for pair in reversed(pw) for v in pair)
    return torch.nn.functional.pad(x, flat)

kernel_name = 'standalone__gqa_block_cached__near_ridge'

def setup_inputs():

    _w = (lambda a: (lambda t: t.to(torch.bfloat16) if t.is_floating_point() else t)(torch.as_tensor(a)))
    B, Sq, D = 8, 224, 512
    Hq, Hkv, Sk, Dh = 32, 32, 1024, 128
    assert Hq % Hkv == 0, (Hq, Hkv)
    return {
        "x": _w((lambda *s: torch.randn(*s, dtype=torch.bfloat16) * 0.8)(B, Sq, D)),
        "k_cache": _w((lambda *s: torch.randn(*s, dtype=torch.bfloat16) * 0.8)(B, Hkv, Sk, Dh)),
        "v_cache": _w((lambda *s: torch.randn(*s, dtype=torch.bfloat16) * 0.05)(B, Hkv, Sk, Dh)),
        "wq": _w((lambda *s: torch.randn(*s, dtype=torch.bfloat16) * 0.8)(D, Hq * Dh) * (D ** -0.5)),
        "wo": _w((lambda *s: torch.randn(*s, dtype=torch.bfloat16) * 0.05)(Hq * Dh, D) * ((Hq * Dh) ** -0.5)),
    }

def reference(x, k_cache, v_cache, wq, wo):
    B, Sq, D = x.shape
    Hq, Dh = 32, 128
    Hkv = k_cache.shape[1]
    rep = Hq // Hkv
    q = (lambda x, *d: x.permute(*d))((x @ wq).reshape(B, Sq, Hq, Dh), 0, 2, 1, 3)
    k = (lambda x, n, axis: torch.repeat_interleave(x, n, dim=axis))(k_cache, rep, axis=1)
    v = (lambda x, n, axis: torch.repeat_interleave(x, n, dim=axis))(v_cache, rep, axis=1)
    scores = torch.einsum("bhqd,bhkd->bhqk", q, k) * (Dh ** -0.5)
    p = _softmax(scores, axis=-1)
    o = torch.einsum("bhqk,bhkd->bhqd", p, v)
    return (lambda x, *d: x.permute(*d))(o, 0, 2, 1, 3).reshape(B, Sq, Hq * Dh) @ wo
```

```cpp
#include <hip/hip_runtime.h>
#include <hip/hip_bf16.h>
typedef __attribute__((ext_vector_type(8))) short bf16x8;
typedef __attribute__((ext_vector_type(16))) float f32x16;
typedef unsigned short u16;
__device__ __forceinline__ float bf2f(u16 x){ return __uint_as_float(((unsigned)x)<<16); }
__device__ __forceinline__ u16 f2bf(float f){ unsigned u=__float_as_uint(f); u += 0x7FFF + ((u>>16)&1); return (u16)(u>>16); }
__device__ __forceinline__ int crow(int r,int hi){ return (r&3)+8*(r>>2)+4*hi; }
__global__ __launch_bounds__(64) void gemm_naive(const u16* A,const u16* B,u16* C,int M,int N,int K){
  int lane=threadIdx.x, r=lane&31, hi=lane>>5;
  int m0=blockIdx.y*32, n0=blockIdx.x*32;
  f32x16 acc={};
  for(int k=0;k<K;k+=16){
    bf16x8 a=*(const bf16x8*)(A+(size_t)(m0+r)*K+k+hi*8);
    bf16x8 b; for(int j=0;j<8;j++) b[j]=(short)B[(size_t)(k+hi*8+j)*N+n0+r];
    acc=__builtin_amdgcn_mfma_f32_32x32x16_bf16(a,b,acc,0,0,0);
  }
  for(int i=0;i<16;i++) C[(size_t)(m0+crow(i,hi))*N+n0+r]=f2bf(acc[i]);
}
__global__ __launch_bounds__(64) void attn_naive(const u16* Q,const u16* Kc,const u16* Vc,u16* O){
  int lane=threadIdx.x, r=lane&31, hi=lane>>5;
  int qb=blockIdx.x, h=blockIdx.y, b=blockIdx.z;
  const u16* Qp=Q+((size_t)(b*224+qb*32+r))*4096+h*128;
  const u16* Kp=Kc+((size_t)(b*32+h))*1024*128;
  const u16* Vp=Vc+((size_t)(b*32+h))*1024*128;
  bf16x8 q[8];
  #pragma unroll
  for(int d=0;d<8;d++) q[d]=*(const bf16x8*)(Qp+d*16+hi*8);
  f32x16 o[4]={};
  float m=-1e30f,l=0; const float C=0.088388347648318440f*1.4426950408889634f;
  for(int t=0;t<1024;t+=32){
    f32x16 s={};
    #pragma unroll
    for(int d=0;d<8;d++){ bf16x8 a=*(const bf16x8*)(Kp+(size_t)(t+r)*128+d*16+hi*8); s=__builtin_amdgcn_mfma_f32_32x32x16_bf16(a,q[d],s,0,0,0);}
    float mx=s[0]; for(int i=1;i<16;i++) mx=fmaxf(mx,s[i]);
    mx=fmaxf(mx,__shfl_xor(mx,32));
    float mn=fmaxf(m,mx), al=exp2f((m-mn)*C); m=mn;
    float ps=0; for(int i=0;i<16;i++){ s[i]=exp2f((s[i]-mn)*C); ps+=s[i]; }
    ps+=__shfl_xor(ps,32); l=l*al+ps;
    #pragma unroll
    for(int db=0;db<4;db++) for(int i=0;i<16;i++) o[db][i]*=al;
    #pragma unroll
    for(int ks=0;ks<2;ks++){
      bf16x8 p; for(int j=0;j<8;j++) p[j]=(short)f2bf(s[8*ks+j]);
      #pragma unroll
      for(int db=0;db<4;db++){
        bf16x8 a; for(int j=0;j<8;j++) a[j]=(short)Vp[(size_t)(t+16*ks+8*(j>>2)+4*hi+(j&3))*128+db*32+r];
        o[db]=__builtin_amdgcn_mfma_f32_32x32x16_bf16(a,p,o[db],0,0,0);
      }
    }
  }
  float il=1.f/l;
  u16* Op=O+((size_t)(b*224+qb*32+r))*4096+h*128;
  #pragma unroll
  for(int db=0;db<4;db++) for(int i=0;i<16;i++) Op[db*32+crow(i,hi)]=f2bf(o[db][i]*il);
}
extern "C" void kernel_launch(void* const* d_in, const int* in_sizes, int n_in, void* d_out, int out_size, void* d_ws, size_t ws_size, hipStream_t stream) {
  const u16* x=(const u16*)d_in[0]; const u16* k=(const u16*)d_in[1]; const u16* v=(const u16*)d_in[2]; const u16* wq=(const u16*)d_in[3]; const u16* wo=(const u16*)d_in[4];
  u16* Q=(u16*)d_ws; u16* O=Q+(size_t)1792*4096;
  gemm_naive<<<dim3(4096/32,1792/32),64,0,stream>>>(x,wq,Q,1792,4096,512);
  attn_naive<<<dim3(7,32,8),64,0,stream>>>(Q,k,v,O);
  gemm_naive<<<dim3(512/32,1792/32),64,0,stream>>>(O,wo,(u16*)d_out,1792,512,4096);
}
```

```cpp
#include <hip/hip_runtime.h>
__global__ __launch_bounds__(256,1) __attribute__((aligned(4096))) void gqa_fused(const void* x,const void* k,const void* v,const void* wq,const void* wo,void* out,void* ws){
  __shared__ __attribute__((aligned(16))) char lds[163840];
  unsigned tid=threadIdx.x;
  asm volatile(
"s_load_dwordx8 s[4:11], %[ka], 0\n"
"s_load_dwordx4 s[12:15], %[ka], 0x20\n"
"s_load_dwordx2 s[16:17], %[ka], 0x30\n"
"v_lshrrev_b32 v1, 6, v0\n"
"v_and_b32 v0, 63, v0\n"
"v_readfirstlane_b32 s52, v1\n"
"s_and_b32 s61, %[bid], 7\n"
"s_lshr_b32 s60, %[bid], 3\n"
"s_lshr_b32 s57, s61, 1\n"
"s_lshr_b32 s58, s60, 2\n"
"s_and_b32 s60, s60, 3\n"
"s_and_b32 s61, s61, 1\n"
"s_lshl2_add_u32 s55, s61, s60\n"
"s_lshl3_add_u32 s56, s57, s58\n"
"s_mov_b32 s59, 0\n"
"s_mov_b32 s35, %[bid]\n"
"s_mov_b32 s69, 0\n"
"s_and_b32 s53, s52, 1\n"
"s_lshr_b32 s54, s52, 1\n"
"v_bfe_u32 v56, v0, 2, 3\n"
"v_lshrrev_b32 v57, 5, v0\n"
"v_and_b32 v58, 3, v0\n"
"v_lshrrev_b32 v59, 2, v56\n"
"s_lshl_b32 s60, s53, 1\n"
"v_add_u32 v59, s60, v59\n"
"v_sub_u32 v59, 4, v59\n"
"v_and_b32 v59, 3, v59\n"
"v_xor_b32 v59, v59, v58\n"
"v_lshlrev_b32 v59, 4, v59\n"
"v_lshl_add_u32 v59, v57, 6, v59\n"
"v_lshl_add_u32 v44, v56, 10, v59\n"
"v_lshlrev_b32 v60, 4, v58\n"
"v_lshl_add_u32 v60, v57, 6, v60\n"
"v_lshl_add_u32 v45, v56, 13, v60\n"
"s_mul_i32 s60, s54, 14\n"
"s_add_u32 s60, s60, s53\n"
"s_lshl_b32 s64, s60, 13\n"
"s_lshl_b32 s65, s60, 10\n"
"s_lshl_b32 s66, s52, 17\n"
"s_lshl_b32 s67, s52, 12\n"
"s_getpc_b64 s[92:93]\n"
"s_and_b32 s92, s92, 0xfffff000\n"
"s_lshr_b32 s60, %[bid], 3\n"
"s_lshl2_add_u32 s60, s60, s52\n"
"s_mul_i32 s60, s60, 448\n"
"v_min_u32 v64, 6, v0\n"
"v_lshlrev_b32 v64, 6, v64\n"
"v_add_u32 v64, s60, v64\n"
"v_min_u32 v64, 57280, v64\n"
"global_load_dword v65, v64, s[92:93]\n"
"s_add_u32 s62, s59, 0\n"
"s_and_b32 s62, s62, 7\n"
"s_lshl_b32 s63, s62, 7\n"
"s_add_u32 s71, s64, s63\n"
"s_lshl_b32 s63, s62, 19\n"
"s_add_u32 s72, s66, s63\n"
"v_mov_b32 v82, v56\n"
"v_mov_b32 v83, v57\n"
"v_mov_b32 v84, v58\n"
"v_and_b32 v56, 15, v0\n"
"v_lshrrev_b32 v57, 4, v0\n"
"v_bfe_u32 v58, v0, 2, 2\n"
"v_and_b32 v59, 3, v0\n"
"v_sub_u32 v60, 4, v58\n"
"v_and_b32 v60, 3, v60\n"
"v_xor_b32 v60, v60, v57\n"
"v_lshlrev_b32 v60, 4, v60\n"
"v_and_b32 v61, 7, v56\n"
"v_lshl_add_u32 v60, v61, 6, v60\n"
"v_lshrrev_b32 v61, 3, v56\n"
"v_lshl_add_u32 v62, v61, 10, v60\n"
"s_mul_i32 s60, s54, 14336\n"
"s_lshl_b32 s61, s53, 13\n"
"s_add_u32 s62, s60, s61\n"
"s_add_u32 s60, s60, 6144\n"
"v_add_u32 v63, s62, v62\n"
"v_add_u32 v62, s60, v62\n"
"v_add_u32 v32, 32768, v63\n"
"v_add_u32 v35, 32768, v62\n"
"v_add_u32 v33, 131072, v63\n"
"v_add_u32 v36, 131072, v62\n"
"v_add_u32 v34, 65536, v63\n"
"v_add_u32 v37, 65536, v62\n"
"v_lshlrev_b32 v62, 4, v59\n"
"v_lshl_add_u32 v62, v58, 6, v62\n"
"v_lshl_add_u32 v62, v57, 11, v62\n"
"s_lshl_b32 s60, s53, 10\n"
"v_add_u32 v62, s60, v62\n"
"v_add_u32 v38, 98304, v62\n"
"v_xor_b32 v39, 1024, v38\n"
"v_add_u32 v40, 114688, v62\n"
"v_xor_b32 v41, 1024, v40\n"
"v_add_u32 v42, 16384, v62\n"
"v_xor_b32 v43, 1024, v42\n"
"v_sub_u32 v60, 4, v58\n"
"v_and_b32 v60, 3, v60\n"
"v_xor_b32 v60, v60, v57\n"
"v_lshlrev_b32 v60, 4, v60\n"
"v_lshl_add_u32 v60, v59, 6, v60\n"
"v_lshl_add_u32 v60, v58, 10, v60\n"
"s_mul_i32 s62, s53, 8704\n"
"v_add_u32 v1, s62, v60\n"
"v_xor_b32 v2, 0x200, v1\n"
"v_xor_b32 v3, 0x2000, v1\n"
"v_xor_b32 v4, 0x2200, v1\n"
"v_and_b32 v61, 1, v57\n"
"v_lshlrev_b32 v60, 3, v59\n"
"v_lshl_add_u32 v60, v61, 5, v60\n"
"v_lshl_add_u32 v60, v58, 6, v60\n"
"v_lshl_add_u32 v62, v57, 10, v60\n"
"s_add_u32 s62, s62, 65536\n"
"v_add_u32 v5, s62, v62\n"
"v_xor_b32 v6, 0x20, v5\n"
"v_xor_b32 v248, 0x200, v5\n"
"v_xor_b32 v249, 0x220, v5\n"
"v_xor_b32 v7, 0x2000, v5\n"
"v_xor_b32 v8, 0x2020, v5\n"
"v_xor_b32 v250, 0x2200, v5\n"
"v_xor_b32 v251, 0x2220, v5\n"
"v_lshl_add_u32 v60, v61, 8, v60\n"
"v_lshrrev_b32 v61, 1, v57\n"
"s_lshl_b32 s82, s52, 13\n"
"s_add_u32 s82, s82, 131072\n"
"s_lshl_b32 s83, s52, 12\n"
"v_lshl_add_u32 v62, v61, 10, v60\n"
"v_add_u32 v13, s82, v62\n"
"v_xor_b32 v14, 32, v13\n"
"v_lshlrev_b32 v11, 4, v0\n"
"v_add_u32 v46, s82, v11\n"
"v_xor_b32 v47, 0x2000, v46\n"
"s_add_u32 s60, s83, 49152\n"
"v_add_u32 v19, s60, v11\n"
"v_xor_b32 v18, 0x1000, v19\n"
"s_lshl_b32 s81, s52, 20\n"
"s_lshl_b32 s60, s58, 7\n"
"s_add_u32 s81, s81, s60\n"
"s_mul_i32 s60, s55, 14\n"
"s_mul_i32 s61, s54, 7\n"
"s_add_u32 s60, s60, s61\n"
"s_lshl_b32 s60, s60, 17\n"
"s_lshl_b32 s61, s56, 12\n"
"s_add_u32 s60, s60, s61\n"
"s_lshl_b32 s61, s53, 11\n"
"s_add_u32 s60, s60, s61\n"
"s_add_u32 s80, s60, 393216\n"
"s_lshl_b32 s61, s53, 19\n"
"s_add_u32 s78, s60, s61\n"
"s_xor_b32 s79, s78, 2048\n"
"s_mov_b32 s74, 0x3e0293ee\n"
"v_mov_b32 v16, 0\n"
"v_mov_b32 v17, 0\n"
"v_mov_b32 v252, 1.0\n"
"v_mov_b32 v20, 0x3f803f80\n"
"v_mov_b32 v21, 0x3f803f80\n"
"v_mov_b32 v22, 0x3f803f80\n"
"v_mov_b32 v23, 0x3f803f80\n"
"v_mov_b32 v56, v82\n"
"v_mov_b32 v57, v83\n"
"v_mov_b32 v58, v84\n"
"s_waitcnt lgkmcnt(0)\n"
"s_mul_i32 s60, s55, 229376\n"
"s_add_u32 s20, s4, s60\n"
"s_addc_u32 s21, s5, 0\n"
"s_and_b32 s21, s21, 0xffff\n"
"s_mov_b32 s22, 229376\n"
"s_mov_b32 s23, 0x20000\n"
"s_lshl_b32 s60, s56, 8\n"
"s_add_u32 s24, s10, s60\n"
"s_addc_u32 s25, s11, 0\n"
"s_and_b32 s25, s25, 0xffff\n"
"s_mov_b32 s26, 0\n"
"s_mov_b32 s27, 0x20000\n"
"s_sub_u32 s26, 4194304, s60\n"
"s_add_u32 m0, s67, 98304\n"
"s_add_u32 s76, s72, 0\n"
"buffer_load_dwordx4 v45, s[24:27], s76 offen lds\n"
"s_add_u32 m0, s67, 99328\n"
"s_add_u32 s77, s72, 128\n"
"buffer_load_dwordx4 v45, s[24:27], s77 offen lds\n"
"s_add_u32 m0, s67, 100352\n"
"s_add_u32 s76, s72, 65536\n"
"buffer_load_dwordx4 v45, s[24:27], s76 offen lds\n"
"s_add_u32 m0, s67, 101376\n"
"s_add_u32 s77, s72, 65664\n"
"buffer_load_dwordx4 v45, s[24:27], s77 offen lds\n"
"s_add_u32 m0, s65, 32768\n"
"s_add_u32 s76, s71, 0\n"
"buffer_load_dwordx4 v44, s[20:23], s76 offen lds\n"
"s_add_u32 m0, s65, 34816\n"
"s_add_u32 s77, s71, 16384\n"
"buffer_load_dwordx4 v44, s[20:23], s77 offen lds\n"
"s_add_u32 m0, s65, 36864\n"
"s_add_u32 s76, s71, 32768\n"
"buffer_load_dwordx4 v44, s[20:23], s76 offen lds\n"
"s_add_u32 m0, s65, 38912\n"
"s_add_u32 s77, s71, 49152\n"
"buffer_load_dwordx4 v44, s[20:23], s77 offen lds\n"
"s_add_u32 m0, s65, 40960\n"
"s_add_u32 s76, s71, 65536\n"
"buffer_load_dwordx4 v44, s[20:23], s76 offen lds\n"
"s_add_u32 m0, s65, 43008\n"
"s_add_u32 s77, s71, 81920\n"
"buffer_load_dwordx4 v44, s[20:23], s77 offen lds\n"
"s_add_u32 m0, s65, 45056\n"
"s_add_u32 s76, s71, 98304\n"
"buffer_load_dwordx4 v44, s[20:23], s76 offen lds\n"
"v_and_b32 v66, 7, v0\n"
"v_lshrrev_b32 v67, 3, v0\n"
"s_mul_i32 s60, s58, 28\n"
"s_mul_i32 s61, s52, 7\n"
"s_add_u32 s60, s60, s61\n"
"v_min_u32 v68, 6, v66\n"
"v_add_lshl_u32 v68, v68, s60, 10\n"
"v_lshl_add_u32 v68, v67, 7, v68\n"
"s_and_b32 s60, s55, 3\n"
"s_lshl2_add_u32 s60, s60, s52\n"
"s_lshl_b32 s60, s60, 2\n"
"v_lshrrev_b32 v69, 1, v66\n"
"v_lshl_add_u32 v69, v67, 6, v69\n"
"v_add_lshl_u32 v69, v69, s60, 13\n"
"v_and_b32 v66, 1, v66\n"
"v_lshl_add_u32 v69, v66, 7, v69\n"
"v_mov_b32 v66, 0x7ff00000\n"
"v_cmp_gt_u32 vcc, 5, v67\n"
"v_lshlrev_b32 v54, 7, v0\n"
"s_lshl_b32 s60, s52, 13\n"
"v_cndmask_b32 v70, v66, v68, vcc\n"
"v_cndmask_b32 v71, v66, v69, vcc\n"
"v_cndmask_b32 v52, v68, v66, vcc\n"
"v_cndmask_b32 v53, v69, v66, vcc\n"
"v_add_u32 v54, s60, v54\n"
"v_cmp_gt_u32 vcc, 2, v67\n"
"s_nop 1\n"
"v_cndmask_b32 v70, v70, v66, vcc\n"
"v_cndmask_b32 v71, v71, v66, vcc\n"
"v_cmp_gt_u32 vcc, 16, v0\n"
"s_lshl_b32 s60, s52, 11\n"
"v_lshlrev_b32 v74, 7, v0\n"
"v_add_u32 v74, s60, v74\n"
"v_cndmask_b32 v74, v66, v74, vcc\n"
"s_lshl_b32 s60, s55, 5\n"
"s_add_u32 s60, s60, s56\n"
"s_lshl_b32 s60, s60, 18\n"
"s_add_u32 s28, s6, s60\n"
"s_addc_u32 s29, s7, 0\n"
"s_and_b32 s29, s29, 0xffff\n"
"s_mov_b32 s30, 262144\n"
"s_mov_b32 s31, 0x20000\n"
"s_add_u32 s88, s8, s60\n"
"s_addc_u32 s89, s9, 0\n"
"s_and_b32 s89, s89, 0xffff\n"
"s_mov_b32 s90, 262144\n"
"s_mov_b32 s91, 0x20000\n"
"s_lshl_b32 s68, s52, 10\n"
"s_mov_b32 s75, 0\n"
"v_bfe_u32 v60, v0, 2, 2\n"
"v_lshrrev_b32 v61, 4, v0\n"
"v_lshlrev_b32 v61, 6, v61\n"
"v_lshl_add_u32 v60, v60, 8, v61\n"
"s_sub_u32 s60, 4, s52\n"
"s_and_b32 s60, s60, 3\n"
"v_xor_b32 v61, s60, v58\n"
"v_lshl_add_u32 v9, v61, 4, v60\n"
"s_lshl_b32 s60, s53, 1\n"
"v_xor_b32 v61, s60, v58\n"
"v_lshl_add_u32 v10, v61, 4, v60\n"
"v_lshrrev_b32 v59, 2, v56\n"
"v_lshlrev_b32 v59, 1, v59\n"
"v_xor_b32 v59, v59, v58\n"
"v_lshlrev_b32 v59, 4, v59\n"
"v_lshl_add_u32 v59, v57, 6, v59\n"
"v_lshl_add_u32 v12, v56, 10, v59\n"
"s_add_u32 s48, s16, 0\n"
"s_addc_u32 s49, s17, 0\n"
"s_and_b32 s49, s49, 0xffff\n"
"s_mov_b32 s50, 65536\n"
"s_mov_b32 s51, 0x20000\n"
"s_lshl_b32 s60, s55, 5\n"
"s_add_u32 s60, s60, s56\n"
"s_lshl_b32 s61, s60, 7\n"
"s_lshl2_add_u32 s60, s60, 32768\n"
"v_lshlrev_b32 v60, 2, v0\n"
"v_add_u32 v60, s61, v60\n"
"v_mov_b32 v61, s60\n"
"v_cmp_gt_u32 vcc, 32, v0\n"
"s_nop 1\n"
"v_cndmask_b32 v49, v61, v60, vcc\n"
"buffer_load_dword v48, v49, s[48:51], 0 offen sc1\n"
"s_add_u32 s36, s16, 65536\n"
"s_addc_u32 s37, s17, 0\n"
"s_and_b32 s37, s37, 0xffff\n"
"s_mov_b32 s38, 14680064\n"
"s_mov_b32 s39, 0x20000\n"
"s_add_u32 s40, s12, 0\n"
"s_addc_u32 s41, s13, 0\n"
"s_and_b32 s41, s41, 0xffff\n"
"s_mov_b32 s42, 4194304\n"
"s_mov_b32 s43, 0x20000\n"
"s_add_u32 s62, s59, 1\n"
"s_and_b32 s62, s62, 7\n"
"s_lshl_b32 s63, s62, 7\n"
"s_add_u32 s71, s64, s63\n"
"s_lshl_b32 s63, s62, 19\n"
"s_add_u32 s72, s66, s63\n"
"s_add_u32 m0, s67, 114688\n"
"s_add_u32 s77, s72, 0\n"
"buffer_load_dwordx4 v45, s[24:27], s77 offen lds\n"
"s_add_u32 m0, s67, 115712\n"
"s_add_u32 s76, s72, 128\n"
"buffer_load_dwordx4 v45, s[24:27], s76 offen lds\n"
"s_add_u32 m0, s67, 116736\n"
"s_add_u32 s77, s72, 65536\n"
"buffer_load_dwordx4 v45, s[24:27], s77 offen lds\n"
"s_add_u32 m0, s67, 117760\n"
"s_add_u32 s76, s72, 65664\n"
"buffer_load_dwordx4 v45, s[24:27], s76 offen lds\n"
"s_add_u32 m0, s65, 131072\n"
"s_add_u32 s77, s71, 0\n"
"buffer_load_dwordx4 v44, s[20:23], s77 offen lds\n"
"s_add_u32 m0, s65, 133120\n"
"s_add_u32 s76, s71, 16384\n"
"buffer_load_dwordx4 v44, s[20:23], s76 offen lds\n"
"s_add_u32 m0, s65, 135168\n"
"s_add_u32 s77, s71, 32768\n"
"buffer_load_dwordx4 v44, s[20:23], s77 offen lds\n"
"s_add_u32 m0, s65, 137216\n"
"s_add_u32 s76, s71, 49152\n"
"buffer_load_dwordx4 v44, s[20:23], s76 offen lds\n"
"s_add_u32 m0, s65, 139264\n"
"s_add_u32 s77, s71, 65536\n"
"buffer_load_dwordx4 v44, s[20:23], s77 offen lds\n"
"s_add_u32 m0, s65, 141312\n"
"s_add_u32 s76, s71, 81920\n"
"buffer_load_dwordx4 v44, s[20:23], s76 offen lds\n"
"s_add_u32 m0, s65, 143360\n"
"s_add_u32 s77, s71, 98304\n"
"buffer_load_dwordx4 v44, s[20:23], s77 offen lds\n"
"s_waitcnt vmcnt(12)\n"
"s_barrier\n"
"s_add_u32 s61, s75, 0\n"
"s_and_b32 s61, s61, 15\n"
"s_lshl_b32 s61, s61, 14\n"
"s_add_u32 s84, s68, s61\n"
"ds_read_b128 a[64:67], v32 offset:0\n"
"ds_read_b128 a[68:71], v32 offset:512\n"
"ds_read_b64_tr_b16 a[96:97], v38 offset:0\n"
"ds_read_b64_tr_b16 a[98:99], v38 offset:256\n"
"ds_read_b64_tr_b16 a[100:101], v38 offset:8192\n"
"ds_read_b64_tr_b16 a[102:103], v38 offset:8448\n"
"ds_read_b128 a[72:75], v32 offset:2048\n"
"ds_read_b128 a[76:79], v32 offset:2560\n"
"ds_read_b64_tr_b16 a[104:105], v38 offset:8\n"
"ds_read_b64_tr_b16 a[106:107], v38 offset:264\n"
"ds_read_b64_tr_b16 a[108:109], v38 offset:8200\n"
"ds_read_b64_tr_b16 a[110:111], v38 offset:8456\n"
"ds_read_b128 a[80:83], v32 offset:4096\n"
"ds_read_b128 a[84:87], v32 offset:4608\n"
"ds_read_b64_tr_b16 a[112:113], v38 offset:512\n"
"ds_read_b64_tr_b16 a[114:115], v38 offset:768\n"
"ds_read_b64_tr_b16 a[116:117], v38 offset:8704\n"
"ds_read_b64_tr_b16 a[118:119], v38 offset:8960\n"
"s_waitcnt lgkmcnt(12)\n"
"v_mfma_f32_16x16x32_bf16 v[120:123], a[96:99], a[64:67], 0\n"
"s_add_u32 s62, s59, 2\n"
"ds_read_b128 a[88:91], v35 offset:0\n"
"ds_read_b128 a[92:95], v35 offset:512\n"
"v_mfma_f32_16x16x32_bf16 v[120:123], a[100:103], a[68:71], v[120:123]\n"
"s_and_b32 s62, s62, 7\n"
"s_waitcnt lgkmcnt(8)\n"
"ds_read_b64_tr_b16 a[120:121], v38 offset:520\n"
"ds_read_b64_tr_b16 a[122:123], v38 offset:776\n"
"v_mfma_f32_16x16x32_bf16 v[124:127], a[104:107], a[64:67], 0\n"
"s_lshl_b32 s63, s62, 7\n"
"ds_read_b64_tr_b16 a[124:125], v38 offset:8712\n"
"ds_read_b64_tr_b16 a[126:127], v38 offset:8968\n"
"v_mfma_f32_16x16x32_bf16 v[124:127], a[108:111], a[68:71], v[124:127]\n"
"s_add_u32 s71, s64, s63\n"
"s_waitcnt lgkmcnt(6)\n"
"ds_read_b64_tr_b16 a[128:129], v39 offset:0\n"
"ds_read_b64_tr_b16 a[130:131], v39 offset:256\n"
"v_mfma_f32_16x16x32_bf16 v[128:131], a[112:115], a[64:67], 0\n"
"s_lshl_b32 s63, s62, 19\n"
"ds_read_b64_tr_b16 a[132:133], v39 offset:8192\n"
"ds_read_b64_tr_b16 a[134:135], v39 offset:8448\n"
"v_mfma_f32_16x16x32_bf16 v[128:131], a[116:119], a[68:71], v[128:131]\n"
"s_add_u32 s72, s66, s63\n"
"s_waitcnt lgkmcnt(4)\n"
"ds_read_b64_tr_b16 a[136:137], v39 offset:8\n"
"v_mfma_f32_16x16x32_bf16 v[132:135], a[120:123], a[64:67], 0\n"
"s_add_u32 s76, s72, 0\n"
"ds_read_b64_tr_b16 a[138:139], v39 offset:264\n"
"ds_read_b64_tr_b16 a[140:141], v39 offset:8200\n"
"v_mfma_f32_16x16x32_bf16 v[132:135], a[124:127], a[68:71], v[132:135]\n"
"s_add_u32 m0, s67, 16384\n"
"s_waitcnt lgkmcnt(3)\n"
"ds_read_b64_tr_b16 a[142:143], v39 offset:8456\n"
"ds_read_b64_tr_b16 a[144:145], v39 offset:512\n"
"v_mfma_f32_16x16x32_bf16 v[136:139], a[128:131], a[64:67], 0\n"
"buffer_load_dwordx4 v45, s[24:27], s76 offen lds\n"
"ds_read_b64_tr_b16 a[146:147], v39 offset:768\n"
"v_mfma_f32_16x16x32_bf16 v[136:139], a[132:135], a[68:71], v[136:139]\n"
"s_waitcnt lgkmcnt(2)\n"
"ds_read_b64_tr_b16 a[148:149], v39 offset:8704\n"
"ds_read_b64_tr_b16 a[150:151], v39 offset:8960\n"
"v_mfma_f32_16x16x32_bf16 v[140:143], a[136:139], a[64:67], 0\n"
"s_add_u32 s77, s72, 128\n"
"ds_read_b64_tr_b16 a[152:153], v39 offset:520\n"
"ds_read_b64_tr_b16 a[154:155], v39 offset:776\n"
"v_mfma_f32_16x16x32_bf16 v[140:143], a[140:143], a[68:71], v[140:143]\n"
"s_add_u32 m0, s67, 17408\n"
"s_waitcnt lgkmcnt(2)\n"
"ds_read_b64_tr_b16 a[156:157], v39 offset:8712\n"
"ds_read_b64_tr_b16 a[158:159], v39 offset:8968\n"
"v_mfma_f32_16x16x32_bf16 v[144:147], a[144:147], a[64:67], 0\n"
"buffer_load_dwordx4 v45, s[24:27], s77 offen lds\n"
"v_mfma_f32_16x16x32_bf16 v[144:147], a[148:151], a[68:71], v[144:147]\n"
"s_waitcnt lgkmcnt(0)\n"
"v_mfma_f32_16x16x32_bf16 v[148:151], a[152:155], a[64:67], 0\n"
"v_mfma_f32_16x16x32_bf16 v[148:151], a[156:159], a[68:71], v[148:151]\n"
"s_add_u32 s76, s72, 65536\n"
"v_mfma_f32_16x16x32_bf16 v[152:155], a[96:99], a[72:75], 0\n"
"s_add_u32 m0, s67, 18432\n"
"v_mfma_f32_16x16x32_bf16 v[152:155], a[100:103], a[76:79], v[152:155]\n"
"buffer_load_dwordx4 v45, s[24:27], s76 offen lds\n"
"v_mfma_f32_16x16x32_bf16 v[156:159], a[104:107], a[72:75], 0\n"
"v_mfma_f32_16x16x32_bf16 v[156:159], a[108:111], a[76:79], v[156:159]\n"
"s_add_u32 s77, s72, 65664\n"
"v_mfma_f32_16x16x32_bf16 v[160:163], a[112:115], a[72:75], 0\n"
"s_add_u32 m0, s67, 19456\n"
"v_mfma_f32_16x16x32_bf16 v[160:163], a[116:119], a[76:79], v[160:163]\n"
"buffer_load_dwordx4 v45, s[24:27], s77 offen lds\n"
"v_mfma_f32_16x16x32_bf16 v[164:167], a[120:123], a[72:75], 0\n"
"v_mfma_f32_16x16x32_bf16 v[164:167], a[124:127], a[76:79], v[164:167]\n"
"v_mfma_f32_16x16x32_bf16 v[168:171], a[128:131], a[72:75], 0\n"
"s_add_u32 s76, s71, 0\n"
"v_mfma_f32_16x16x32_bf16 v[168:171], a[132:135], a[76:79], v[168:171]\n"
"s_add_u32 m0, s65, 65536\n"
"v_mfma_f32_16x16x32_bf16 v[172:175], a[136:139], a[72:75], 0\n"
"buffer_load_dwordx4 v44, s[20:23], s76 offen lds\n"
"v_mfma_f32_16x16x32_bf16 v[172:175], a[140:143], a[76:79], v[172:175]\n"
"s_waitcnt vmcnt(5)\n"
"s_barrier\n"
"v_mfma_f32_16x16x32_bf16 v[176:179], a[144:147], a[72:75], 0\n"
"s_add_u32 s77, s71, 16384\n"
"ds_read_b128 a[160:163], v33 offset:0\n"
"ds_read_b128 a[164:167], v33 offset:512\n"
"v_mfma_f32_16x16x32_bf16 v[176:179], a[148:151], a[76:79], v[176:179]\n"
"s_add_u32 m0, s65, 67584\n"
"ds_read_b64_tr_b16 a[192:193], v40 offset:0\n"
"ds_read_b64_tr_b16 a[194:195], v40 offset:256\n"
"v_mfma_f32_16x16x32_bf16 v[180:183], a[152:155], a[72:75], 0\n"
"buffer_load_dwordx4 v44, s[20:23], s77 offen lds\n"
"ds_read_b64_tr_b16 a[196:197], v40 offset:8192\n"
"v_mfma_f32_16x16x32_bf16 v[180:183], a[156:159], a[76:79], v[180:183]\n"
"ds_read_b64_tr_b16 a[198:199], v40 offset:8448\n"
"v_mfma_f32_16x16x32_bf16 v[184:187], a[96:99], a[80:83], 0\n"
"ds_read_b128 a[168:171], v33 offset:2048\n"
"ds_read_b128 a[172:175], v33 offset:2560\n"
"v_mfma_f32_16x16x32_bf16 v[184:187], a[100:103], a[84:87], v[184:187]\n"
"s_add_u32 s76, s71, 32768\n"
"ds_read_b64_tr_b16 a[200:201], v40 offset:8\n"
"ds_read_b64_tr_b16 a[202:203], v40 offset:264\n"
"v_mfma_f32_16x16x32_bf16 v[188:191], a[104:107], a[80:83], 0\n"
"s_add_u32 m0, s65, 69632\n"
"ds_read_b64_tr_b16 a[204:205], v40 offset:8200\n"
"v_mfma_f32_16x16x32_bf16 v[188:191], a[108:111], a[84:87], v[188:191]\n"
"buffer_load_dwordx4 v44, s[20:23], s76 offen lds\n"
"ds_read_b64_tr_b16 a[206:207], v40 offset:8456\n"
"v_mfma_f32_16x16x32_bf16 v[192:195], a[112:115], a[80:83], 0\n"
"ds_read_b128 a[176:179], v33 offset:4096\n"
"ds_read_b128 a[180:183], v33 offset:4608\n"
"v_mfma_f32_16x16x32_bf16 v[192:195], a[116:119], a[84:87], v[192:195]\n"
"s_add_u32 s77, s71, 49152\n"
"ds_read_b64_tr_b16 a[208:209], v40 offset:512\n"
"v_mfma_f32_16x16x32_bf16 v[196:199], a[120:123], a[80:83], 0\n"
"s_add_u32 m0, s65, 71680\n"
"ds_read_b64_tr_b16 a[210:211], v40 offset:768\n"
"ds_read_b64_tr_b16 a[212:213], v40 offset:8704\n"
"v_mfma_f32_16x16x32_bf16 v[196:199], a[124:127], a[84:87], v[196:199]\n"
"buffer_load_dwordx4 v44, s[20:23], s77 offen lds\n"
"ds_read_b64_tr_b16 a[214:215], v40 offset:8960\n"
"v_mfma_f32_16x16x32_bf16 v[200:203], a[128:131], a[80:83], 0\n"
"ds_read_b128 a[184:187], v36 offset:0\n"
"ds_read_b128 a[188:191], v36 offset:512\n"
"v_mfma_f32_16x16x32_bf16 v[200:203], a[132:135], a[84:87], v[200:203]\n"
"ds_read_b64_tr_b16 a[216:217], v40 offset:520\n"
"v_mfma_f32_16x16x32_bf16 v[204:207], a[136:139], a[80:83], 0\n"
"s_add_u32 s76, s71, 65536\n"
"ds_read_b64_tr_b16 a[218:219], v40 offset:776\n"
"ds_read_b64_tr_b16 a[220:221], v40 offset:8712\n"
"v_mfma_f32_16x16x32_bf16 v[204:207], a[140:143], a[84:87], v[204:207]\n"
"s_add_u32 m0, s65, 73728\n"
"ds_read_b64_tr_b16 a[222:223], v40 offset:8968\n"
"ds_read_b64_tr_b16 a[224:225], v41 offset:0\n"
"v_mfma_f32_16x16x32_bf16 v[208:211], a[144:147], a[80:83], 0\n"
"buffer_load_dwordx4 v44, s[20:23], s76 offen lds\n"
"v_mfma_f32_16x16x32_bf16 v[208:211], a[148:151], a[84:87], v[208:211]\n"
"ds_read_b64_tr_b16 a[226:227], v41 offset:256\n"
"ds_read_b64_tr_b16 a[228:229], v41 offset:8192\n"
"v_mfma_f32_16x16x32_bf16 v[212:215], a[152:155], a[80:83], 0\n"
"s_add_u32 s77, s71, 81920\n"
"ds_read_b64_tr_b16 a[230:231], v41 offset:8448\n"
"ds_read_b64_tr_b16 a[232:233], v41 offset:8\n"
"v_mfma_f32_16x16x32_bf16 v[212:215], a[156:159], a[84:87], v[212:215]\n"
"s_add_u32 m0, s65, 75776\n"
"ds_read_b64_tr_b16 a[234:235], v41 offset:264\n"
"v_mfma_f32_16x16x32_bf16 v[216:219], a[96:99], a[88:91], 0\n"
"buffer_load_dwordx4 v44, s[20:23], s77 offen lds\n"
"ds_read_b64_tr_b16 a[236:237], v41 offset:8200\n"
"v_mfma_f32_16x16x32_bf16 v[216:219], a[100:103], a[92:95], v[216:219]\n"
"ds_read_b64_tr_b16 a[238:239], v41 offset:8456\n"
"ds_read_b64_tr_b16 a[240:241], v41 offset:512\n"
"v_mfma_f32_16x16x32_bf16 v[220:223], a[104:107], a[88:91], 0\n"
"ds_read_b64_tr_b16 a[242:243], v41 offset:768\n"
"ds_read_b64_tr_b16 a[244:245], v41 offset:8704\n"
"v_mfma_f32_16x16x32_bf16 v[220:223], a[108:111], a[92:95], v[220:223]\n"
"s_add_u32 s76, s71, 98304\n"
"ds_read_b64_tr_b16 a[246:247], v41 offset:8960\n"
"v_mfma_f32_16x16x32_bf16 v[224:227], a[112:115], a[88:91], 0\n"
"s_add_u32 m0, s65, 77824\n"
"ds_read_b64_tr_b16 a[248:249], v41 offset:520\n"
"ds_read_b64_tr_b16 a[250:251], v41 offset:776\n"
"v_mfma_f32_16x16x32_bf16 v[224:227], a[116:119], a[92:95], v[224:227]\n"
"s_add_u32 s62, s59, 3\n"
"buffer_load_dwordx4 v44, s[20:23], s76 offen lds\n"
"ds_read_b64_tr_b16 a[252:253], v41 offset:8712\n"
"v_mfma_f32_16x16x32_bf16 v[228:231], a[120:123], a[88:91], 0\n"
"s_and_b32 s62, s62, 7\n"
"ds_read_b64_tr_b16 a[254:255], v41 offset:8968\n"
"v_mfma_f32_16x16x32_bf16 v[228:231], a[124:127], a[92:95], v[228:231]\n"
"s_lshl_b32 s63, s62, 7\n"
"s_waitcnt lgkmcnt(15)\n"
"v_mfma_f32_16x16x32_bf16 v[120:123], a[192:195], a[160:163], v[120:123]\n"
"s_add_u32 s71, s64, s63\n"
"v_mfma_f32_16x16x32_bf16 v[120:123], a[196:199], a[164:167], v[120:123]\n"
"s_lshl_b32 s63, s62, 19\n"
"v_mfma_f32_16x16x32_bf16 v[124:127], a[200:203], a[160:163], v[124:127]\n"
"s_add_u32 s72, s66, s63\n"
"s_add_u32 s77, s72, 0\n"
"v_mfma_f32_16x16x32_bf16 v[124:127], a[204:207], a[164:167], v[124:127]\n"
"s_add_u32 m0, s67, 98304\n"
"v_mfma_f32_16x16x32_bf16 v[128:131], a[208:211], a[160:163], v[128:131]\n"
"buffer_load_dwordx4 v45, s[24:27], s77 offen lds\n"
"v_mfma_f32_16x16x32_bf16 v[128:131], a[212:215], a[164:167], v[128:131]\n"
"v_mfma_f32_16x16x32_bf16 v[132:135], a[216:219], a[160:163], v[132:135]\n"
"s_add_u32 s76, s72, 128\n"
"v_mfma_f32_16x16x32_bf16 v[132:135], a[220:223], a[164:167], v[132:135]\n"
"s_add_u32 m0, s67, 99328\n"
"s_waitcnt lgkmcnt(12)\n"
"v_mfma_f32_16x16x32_bf16 v[136:139], a[224:227], a[160:163], v[136:139]\n"
"buffer_load_dwordx4 v45, s[24:27], s76 offen lds\n"
"v_mfma_f32_16x16x32_bf16 v[136:139], a[228:231], a[164:167], v[136:139]\n"
"s_waitcnt lgkmcnt(8)\n"
"v_mfma_f32_16x16x32_bf16 v[140:143], a[232:235], a[160:163], v[140:143]\n"
"v_mfma_f32_16x16x32_bf16 v[140:143], a[236:239], a[164:167], v[140:143]\n"
"s_add_u32 s77, s72, 65536\n"
"s_waitcnt lgkmcnt(4)\n"
"v_mfma_f32_16x16x32_bf16 v[144:147], a[240:243], a[160:163], v[144:147]\n"
"s_add_u32 m0, s67, 100352\n"
"v_mfma_f32_16x16x32_bf16 v[144:147], a[244:247], a[164:167], v[144:147]\n"
"s_waitcnt lgkmcnt(0)\n"
"buffer_load_dwordx4 v45, s[24:27], s77 offen lds\n"
"v_mfma_f32_16x16x32_bf16 v[148:151], a[248:251], a[160:163], v[148:151]\n"
"v_mfma_f32_16x16x32_bf16 v[148:151], a[252:255], a[164:167], v[148:151]\n"
"s_add_u32 s76, s72, 65664\n"
"v_mfma_f32_16x16x32_bf16 v[152:155], a[192:195], a[168:171], v[152:155]\n"
"s_add_u32 m0, s67, 101376\n"
"v_mfma_f32_16x16x32_bf16 v[152:155], a[196:199], a[172:175], v[152:155]\n"
"buffer_load_dwordx4 v45, s[24:27], s76 offen lds\n"
"v_mfma_f32_16x16x32_bf16 v[156:159], a[200:203], a[168:171], v[156:159]\n"
"v_mfma_f32_16x16x32_bf16 v[156:159], a[204:207], a[172:175], v[156:159]\n"
"v_mfma_f32_16x16x32_bf16 v[160:163], a[208:211], a[168:171], v[160:163]\n"
"s_add_u32 s77, s71, 0\n"
"v_mfma_f32_16x16x32_bf16 v[160:163], a[212:215], a[172:175], v[160:163]\n"
"s_add_u32 m0, s65, 32768\n"
"v_mfma_f32_16x16x32_bf16 v[164:167], a[216:219], a[168:171], v[164:167]\n"
"buffer_load_dwordx4 v44, s[20:23], s77 offen lds\n"
"v_mfma_f32_16x16x32_bf16 v[164:167], a[220:223], a[172:175], v[164:167]\n"
"v_mfma_f32_16x16x32_bf16 v[168:171], a[224:227], a[168:171], v[168:171]\n"
"s_add_u32 s76, s71, 16384\n"
"v_mfma_f32_16x16x32_bf16 v[168:171], a[228:231], a[172:175], v[168:171]\n"
"s_add_u32 m0, s65, 34816\n"
"v_mfma_f32_16x16x32_bf16 v[172:175], a[232:235], a[168:171], v[172:175]\n"
"buffer_load_dwordx4 v44, s[20:23], s76 offen lds\n"
"v_mfma_f32_16x16x32_bf16 v[172:175], a[236:239], a[172:175], v[172:175]\n"
"s_waitcnt vmcnt(6)\n"
"s_barrier\n"
"v_mfma_f32_16x16x32_bf16 v[176:179], a[240:243], a[168:171], v[176:179]\n"
"ds_read_b128 a[64:67], v34 offset:0\n"
"ds_read_b128 a[68:71], v34 offset:512\n"
"v_mfma_f32_16x16x32_bf16 v[176:179], a[244:247], a[172:175], v[176:179]\n"
"s_add_u32 s77, s71, 32768\n"
"ds_read_b64_tr_b16 a[96:97], v42 offset:0\n"
"ds_read_b64_tr_b16 a[98:99], v42 offset:256\n"
"v_mfma_f32_16x16x32_bf16 v[180:183], a[248:251], a[168:171], v[180:183]\n"
"s_add_u32 m0, s65, 36864\n"
"ds_read_b64_tr_b16 a[100:101], v42 offset:8192\n"
"v_mfma_f32_16x16x32_bf16 v[180:183], a[252:255], a[172:175], v[180:183]\n"
"buffer_load_dwordx4 v44, s[20:23], s77 offen lds\n"
"ds_read_b64_tr_b16 a[102:103], v42 offset:8448\n"
"v_mfma_f32_16x16x32_bf16 v[184:187], a[192:195], a[176:179], v[184:187]\n"
"ds_read_b128 a[72:75], v34 offset:2048\n"
"ds_read_b128 a[76:79], v34 offset:2560\n"
"v_mfma_f32_16x16x32_bf16 v[184:187], a[196:199], a[180:183], v[184:187]\n"
"s_add_u32 s76, s71, 49152\n"
"ds_read_b64_tr_b16 a[104:105], v42 offset:8\n"
"ds_read_b64_tr_b16 a[106:107], v42 offset:264\n"
"v_mfma_f32_16x16x32_bf16 v[188:191], a[200:203], a[176:179], v[188:191]\n"
"s_add_u32 m0, s65, 38912\n"
"ds_read_b64_tr_b16 a[108:109], v42 offset:8200\n"
"v_mfma_f32_16x16x32_bf16 v[188:191], a[204:207], a[180:183], v[188:191]\n"
"buffer_load_dwordx4 v44, s[20:23], s76 offen lds\n"
"ds_read_b64_tr_b16 a[110:111], v42 offset:8456\n"
"v_mfma_f32_16x16x32_bf16 v[192:195], a[208:211], a[176:179], v[192:195]\n"
"ds_read_b128 a[80:83], v34 offset:4096\n"
"ds_read_b128 a[84:87], v34 offset:4608\n"
"v_mfma_f32_16x16x32_bf16 v[192:195], a[212:215], a[180:183], v[192:195]\n"
"ds_read_b64_tr_b16 a[112:113], v42 offset:512\n"
"v_mfma_f32_16x16x32_bf16 v[196:199], a[216:219], a[176:179], v[196:199]\n"
"s_add_u32 s77, s71, 65536\n"
"ds_read_b64_tr_b16 a[114:115], v42 offset:768\n"
"ds_read_b64_tr_b16 a[116:117], v42 offset:8704\n"
"v_mfma_f32_16x16x32_bf16 v[196:199], a[220:223], a[180:183], v[196:199]\n"
"s_add_u32 m0, s65, 40960\n"
"ds_read_b64_tr_b16 a[118:119], v42 offset:8960\n"
"ds_read_b128 a[88:91], v37 offset:0\n"
"v_mfma_f32_16x16x32_bf16 v[200:203], a[224:227], a[176:179], v[200:203]\n"
"buffer_load_dwordx4 v44, s[20:23], s77 offen lds\n"
"ds_read_b128 a[92:95], v37 offset:512\n"
"v_mfma_f32_16x16x32_bf16 v[200:203], a[228:231], a[180:183], v[200:203]\n"
"ds_read_b64_tr_b16 a[120:121], v42 offset:520\n"
"v_mfma_f32_16x16x32_bf16 v[204:207], a[232:235], a[176:179], v[204:207]\n"
"s_add_u32 s76, s71, 81920\n"
"ds_read_b64_tr_b16 a[122:123], v42 offset:776\n"
"ds_read_b64_tr_b16 a[124:125], v42 offset:8712\n"
"v_mfma_f32_16x16x32_bf16 v[204:207], a[236:239], a[180:183], v[204:207]\n"
"s_add_u32 m0, s65, 43008\n"
"ds_read_b64_tr_b16 a[126:127], v42 offset:8968\n"
"ds_read_b64_tr_b16 a[128:129], v43 offset:0\n"
"v_mfma_f32_16x16x32_bf16 v[208:211], a[240:243], a[176:179], v[208:211]\n"
"buffer_load_dwordx4 v44, s[20:23], s76 offen lds\n"
"v_mfma_f32_16x16x32_bf16 v[208:211], a[244:247], a[180:183], v[208:211]\n"
"ds_read_b64_tr_b16 a[130:131], v43 offset:256\n"
"ds_read_b64_tr_b16 a[132:133], v43 offset:8192\n"
"v_mfma_f32_16x16x32_bf16 v[212:215], a[248:251], a[176:179], v[212:215]\n"
"ds_read_b64_tr_b16 a[134:135], v43 offset:8448\n"
"ds_read_b64_tr_b16 a[136:137], v43 offset:8\n"
"v_mfma_f32_16x16x32_bf16 v[212:215], a[252:255], a[180:183], v[212:215]\n"
"s_add_u32 s77, s71, 98304\n"
"ds_read_b64_tr_b16 a[138:139], v43 offset:264\n"
"v_mfma_f32_16x16x32_bf16 v[216:219], a[192:195], a[184:187], v[216:219]\n"
"s_add_u32 m0, s65, 45056\n"
"ds_read_b64_tr_b16 a[140:141], v43 offset:8200\n"
"ds_read_b64_tr_b16 a[142:143], v43 offset:8456\n"
"v_mfma_f32_16x16x32_bf16 v[216:219], a[196:199], a[188:191], v[216:219]\n"
"buffer_load_dwordx4 v44, s[20:23], s77 offen lds\n"
"ds_read_b64_tr_b16 a[144:145], v43 offset:512\n"
"v_mfma_f32_16x16x32_bf16 v[220:223], a[200:203], a[184:187], v[220:223]\n"
"ds_read_b64_tr_b16 a[146:147], v43 offset:768\n"
"ds_read_b64_tr_b16 a[148:149], v43 offset:8704\n"
"v_mfma_f32_16x16x32_bf16 v[220:223], a[204:207], a[188:191], v[220:223]\n"
"s_add_u32 s76, s84, 0\n"
"ds_read_b64_tr_b16 a[150:151], v43 offset:8960\n"
"v_mfma_f32_16x16x32_bf16 v[224:227], a[208:211], a[184:187], v[224:227]\n"
"s_add_u32 m0, s68, 0\n"
"ds_read_b64_tr_b16 a[152:153], v43 offset:520\n"
"ds_read_b64_tr_b16 a[154:155], v43 offset:776\n"
"v_mfma_f32_16x16x32_bf16 v[224:227], a[212:215], a[188:191], v[224:227]\n"
"s_add_u32 s62, s59, 4\n"
"buffer_load_dwordx4 v9, s[28:31], s76 offen nt lds\n"
"ds_read_b64_tr_b16 a[156:157], v43 offset:8712\n"
"v_mfma_f32_16x16x32_bf16 v[228:231], a[216:219], a[184:187], v[228:231]\n"
"s_and_b32 s62, s62, 7\n"
"ds_read_b64_tr_b16 a[158:159], v43 offset:8968\n"
"v_mfma_f32_16x16x32_bf16 v[228:231], a[220:223], a[188:191], v[228:231]\n"
"s_lshl_b32 s63, s62, 7\n"
"s_waitcnt lgkmcnt(15)\n"
"v_mfma_f32_16x16x32_bf16 v[120:123], a[96:99], a[64:67], v[120:123]\n"
"s_add_u32 s71, s64, s63\n"
"v_mfma_f32_16x16x32_bf16 v[120:123], a[100:103], a[68:71], v[120:123]\n"
"s_lshl_b32 s63, s62, 19\n"
"v_mfma_f32_16x16x32_bf16 v[124:127], a[104:107], a[64:67], v[124:127]\n"
"s_add_u32 s72, s66, s63\n"
"s_add_u32 s77, s72, 0\n"
"v_mfma_f32_16x16x32_bf16 v[124:127], a[108:111], a[68:71], v[124:127]\n"
"s_add_u32 m0, s67, 114688\n"
"v_mfma_f32_16x16x32_bf16 v[128:131], a[112:115], a[64:67], v[128:131]\n"
"buffer_load_dwordx4 v45, s[24:27], s77 offen lds\n"
"v_mfma_f32_16x16x32_bf16 v[128:131], a[116:119], a[68:71], v[128:131]\n"
"v_mfma_f32_16x16x32_bf16 v[132:135], a[120:123], a[64:67], v[132:135]\n"
"s_add_u32 s76, s72, 128\n"
"v_mfma_f32_16x16x32_bf16 v[132:135], a[124:127], a[68:71], v[132:135]\n"
"s_add_u32 m0, s67, 115712\n"
"s_waitcnt lgkmcnt(12)\n"
"v_mfma_f32_16x16x32_bf16 v[136:139], a[128:131], a[64:67], v[136:139]\n"
"buffer_load_dwordx4 v45, s[24:27], s76 offen lds\n"
"v_mfma_f32_16x16x32_bf16 v[136:139], a[132:135], a[68:71], v[136:139]\n"
"s_waitcnt lgkmcnt(8)\n"
"v_mfma_f32_16x16x32_bf16 v[140:143], a[136:139], a[64:67], v[140:143]\n"
"v_mfma_f32_16x16x32_bf16 v[140:143], a[140:143], a[68:71], v[140:143]\n"
"s_add_u32 s77, s72, 65536\n"
"s_waitcnt lgkmcnt(4)\n"
"v_mfma_f32_16x16x32_bf16 v[144:147], a[144:147], a[64:67], v[144:147]\n"
"s_add_u32 m0, s67, 116736\n"
"v_mfma_f32_16x16x32_bf16 v[144:147], a[148:151], a[68:71], v[144:147]\n"
"s_waitcnt lgkmcnt(0)\n"
"buffer_load_dwordx4 v45, s[24:27], s77 offen lds\n"
"v_mfma_f32_16x16x32_bf16 v[148:151], a[152:155], a[64:67], v[148:151]\n"
"v_mfma_f32_16x16x32_bf16 v[148:151], a[156:159], a[68:71], v[148:151]\n"
"s_add_u32 s76, s72, 65664\n"
"v_mfma_f32_16x16x32_bf16 v[152:155], a[96:99], a[72:75], v[152:155]\n"
"s_add_u32 m0, s67, 117760\n"
"v_mfma_f32_16x16x32_bf16 v[152:155], a[100:103], a[76:79], v[152:155]\n"
"buffer_load_dwordx4 v45, s[24:27], s76 offen lds\n"
"v_mfma_f32_16x16x32_bf16 v[156:159], a[104:107], a[72:75], v[156:159]\n"
"v_mfma_f32_16x16x32_bf16 v[156:159], a[108:111], a[76:79], v[156:159]\n"
"v_mfma_f32_16x16x32_bf16 v[160:163], a[112:115], a[72:75], v[160:163]\n"
"s_add_u32 s77, s71, 0\n"
"v_mfma_f32_16x16x32_bf16 v[160:163], a[116:119], a[76:79], v[160:163]\n"
"s_add_u32 m0, s65, 131072\n"
"v_mfma_f32_16x16x32_bf16 v[164:167], a[120:123], a[72:75], v[164:167]\n"
"buffer_load_dwordx4 v44, s[20:23], s77 offen lds\n"
"v_mfma_f32_16x16x32_bf16 v[164:167], a[124:127], a[76:79], v[164:167]\n"
"v_mfma_f32_16x16x32_bf16 v[168:171], a[128:131], a[72:75], v[168:171]\n"
"s_add_u32 s76, s71, 16384\n"
"v_mfma_f32_16x16x32_bf16 v[168:171], a[132:135], a[76:79], v[168:171]\n"
"s_add_u32 m0, s65, 133120\n"
"v_mfma_f32_16x16x32_bf16 v[172:175], a[136:139], a[72:75], v[172:175]\n"
"buffer_load_dwordx4 v44, s[20:23], s76 offen lds\n"
"v_mfma_f32_16x16x32_bf16 v[172:175], a[140:143], a[76:79], v[172:175]\n"
"v_readlane_b32 s73, v48, 32\n"
"v_readlane_b32 s60, v49, 32\n"
"s_lshl2_add_u32 s61, s52, 61440\n"
"v_mov_b32 v51, s61\n"
"L_nl_%=:\n"
"s_add_u32 s73, s73, 1\n"
"v_cmp_eq_u32 vcc, s73, v48\n"
"s_nop 1\n"
"s_cbranch_vccnz L_nl_%=\n"
"s_cmp_eq_u32 s52, 0\n"
"s_cselect_b32 s60, s60, 0x100000\n"
"v_mov_b32 v50, s73\n"
"s_mov_b64 exec, 1\n"
"ds_write_b32 v51, v50\n"
"buffer_store_dword v50, off, s[48:51], s60 sc1\n"
"s_mov_b64 exec, -1\n"
"v_mov_b32 v51, 61440\n"
"s_waitcnt vmcnt(8) lgkmcnt(0)\n"
"s_barrier\n"
"v_mfma_f32_16x16x32_bf16 v[176:179], a[144:147], a[72:75], v[176:179]\n"
"ds_read_b128 a[160:163], v32 offset:0\n"
"ds_read_b128 a[164:167], v32 offset:512\n"
"v_mfma_f32_16x16x32_bf16 v[176:179], a[148:151], a[76:79], v[176:179]\n"
"s_add_u32 s77, s71, 32768\n"
"ds_read_b64_tr_b16 a[192:193], v38 offset:0\n"
"ds_read_b64_tr_b16 a[194:195], v38 offset:256\n"
"v_mfma_f32_16x16x32_bf16 v[180:183], a[152:155], a[72:75], v[180:183]\n"
"s_add_u32 m0, s65, 135168\n"
"ds_read_b64_tr_b16 a[196:197], v38 offset:8192\n"
"v_mfma_f32_16x16x32_bf16 v[180:183], a[156:159], a[76:79], v[180:183]\n"
"buffer_load_dwordx4 v44, s[20:23], s77 offen lds\n"
"ds_read_b64_tr_b16 a[198:199], v38 offset:8448\n"
"v_mfma_f32_16x16x32_bf16 v[184:187], a[96:99], a[80:83], v[184:187]\n"
"ds_read_b128 a[168:171], v32 offset:2048\n"
"ds_read_b128 a[172:175], v32 offset:2560\n"
"v_mfma_f32_16x16x32_bf16 v[184:187], a[100:103], a[84:87], v[184:187]\n"
"s_add_u32 s76, s71, 49152\n"
"ds_read_b64_tr_b16 a[200:201], v38 offset:8\n"
"ds_read_b64_tr_b16 a[202:203], v38 offset:264\n"
"v_mfma_f32_16x16x32_bf16 v[188:191], a[104:107], a[80:83], v[188:191]\n"
"s_add_u32 m0, s65, 137216\n"
"ds_read_b64_tr_b16 a[204:205], v38 offset:8200\n"
"v_mfma_f32_16x16x32_bf16 v[188:191], a[108:111], a[84:87], v[188:191]\n"
"buffer_load_dwordx4 v44, s[20:23], s76 offen lds\n"
"ds_read_b64_tr_b16 a[206:207], v38 offset:8456\n"
"v_mfma_f32_16x16x32_bf16 v[192:195], a[112:115], a[80:83], v[192:195]\n"
"ds_read_b128 a[176:179], v32 offset:4096\n"
"ds_read_b128 a[180:183], v32 offset:4608\n"
"v_mfma_f32_16x16x32_bf16 v[192:195], a[116:119], a[84:87], v[192:195]\n"
"ds_read_b64_tr_b16 a[208:209], v38 offset:512\n"
"v_mfma_f32_16x16x32_bf16 v[196:199], a[120:123], a[80:83], v[196:199]\n"
"s_add_u32 s77, s71, 65536\n"
"ds_read_b64_tr_b16 a[210:211], v38 offset:768\n"
"ds_read_b64_tr_b16 a[212:213], v38 offset:8704\n"
"v_mfma_f32_16x16x32_bf16 v[196:199], a[124:127], a[84:87], v[196:199]\n"
"s_add_u32 m0, s65, 139264\n"
"ds_read_b64_tr_b16 a[214:215], v38 offset:8960\n"
"ds_read_b128 a[184:187], v35 offset:0\n"
"v_mfma_f32_16x16x32_bf16 v[200:203], a[128:131], a[80:83], v[200:203]\n"
"buffer_load_dwordx4 v44, s[20:23], s77 offen lds\n"
"ds_read_b128 a[188:191], v35 offset:512\n"
"v_mfma_f32_16x16x32_bf16 v[200:203], a[132:135], a[84:87], v[200:203]\n"
"ds_read_b64_tr_b16 a[216:217], v38 offset:520\n"
"v_mfma_f32_16x16x32_bf16 v[204:207], a[136:139], a[80:83], v[204:207]\n"
"s_add_u32 s76, s71, 81920\n"
"ds_read_b64_tr_b16 a[218:219], v38 offset:776\n"
"ds_read_b64_tr_b16 a[220:221], v38 offset:8712\n"
"v_mfma_f32_16x16x32_bf16 v[204:207], a[140:143], a[84:87], v[204:207]\n"
"s_add_u32 m0, s65, 141312\n"
"ds_read_b64_tr_b16 a[222:223], v38 offset:8968\n"
"ds_read_b64_tr_b16 a[224:225], v39 offset:0\n"
"v_mfma_f32_16x16x32_bf16 v[208:211], a[144:147], a[80:83], v[208:211]\n"
"buffer_load_dwordx4 v44, s[20:23], s76 offen lds\n"
"v_mfma_f32_16x16x32_bf16 v[208:211], a[148:151], a[84:87], v[208:211]\n"
"ds_read_b64_tr_b16 a[226:227], v39 offset:256\n"
"ds_read_b64_tr_b16 a[228:229], v39 offset:8192\n"
"v_mfma_f32_16x16x32_bf16 v[212:215], a[152:155], a[80:83], v[212:215]\n"
"ds_read_b64_tr_b16 a[230:231], v39 offset:8448\n"
"ds_read_b64_tr_b16 a[232:233], v39 offset:8\n"
"v_mfma_f32_16x16x32_bf16 v[212:215], a[156:159], a[84:87], v[212:215]\n"
"s_add_u32 s77, s71, 98304\n"
"ds_read_b64_tr_b16 a[234:235], v39 offset:264\n"
"v_mfma_f32_16x16x32_bf16 v[216:219], a[96:99], a[88:91], v[216:219]\n"
"s_add_u32 m0, s65, 143360\n"
"ds_read_b64_tr_b16 a[236:237], v39 offset:8200\n"
"ds_read_b64_tr_b16 a[238:239], v39 offset:8456\n"
"v_mfma_f32_16x16x32_bf16 v[216:219], a[100:103], a[92:95], v[216:219]\n"
"buffer_load_dwordx4 v44, s[20:23], s77 offen lds\n"
"ds_read_b64_tr_b16 a[240:241], v39 offset:512\n"
"v_mfma_f32_16x16x32_bf16 v[220:223], a[104:107], a[88:91], v[220:223]\n"
"ds_read_b64_tr_b16 a[242:243], v39 offset:768\n"
"ds_read_b64_tr_b16 a[244:245], v39 offset:8704\n"
"v_mfma_f32_16x16x32_bf16 v[220:223], a[108:111], a[92:95], v[220:223]\n"
"s_add_u32 s76, s84, 4096\n"
"ds_read_b64_tr_b16 a[246:247], v39 offset:8960\n"
"v_mfma_f32_16x16x32_bf16 v[224:227], a[112:115], a[88:91], v[224:227]\n"
"s_add_u32 m0, s68, 4096\n"
"ds_read_b64_tr_b16 a[248:249], v39 offset:520\n"
"ds_read_b64_tr_b16 a[250:251], v39 offset:776\n"
"v_mfma_f32_16x16x32_bf16 v[224:227], a[116:119], a[92:95], v[224:227]\n"
"s_add_u32 s62, s59, 5\n"
"buffer_load_dwordx4 v9, s[28:31], s76 offen nt lds\n"
"ds_read_b64_tr_b16 a[252:253], v39 offset:8712\n"
"v_mfma_f32_16x16x32_bf16 v[228:231], a[120:123], a[88:91], v[228:231]\n"
"s_and_b32 s62, s62, 7\n"
"ds_read_b64_tr_b16 a[254:255], v39 offset:8968\n"
"v_mfma_f32_16x16x32_bf16 v[228:231], a[124:127], a[92:95], v[228:231]\n"
"s_lshl_b32 s63, s62, 7\n"
"s_waitcnt lgkmcnt(15)\n"
"v_mfma_f32_16x16x32_bf16 v[120:123], a[192:195], a[160:163], v[120:123]\n"
"s_add_u32 s71, s64, s63\n"
"v_mfma_f32_16x16x32_bf16 v[120:123], a[196:199], a[164:167], v[120:123]\n"
"s_lshl_b32 s63, s62, 19\n"
"v_mfma_f32_16x16x32_bf16 v[124:127], a[200:203], a[160:163], v[124:127]\n"
"s_add_u32 s72, s66, s63\n"
"s_add_u32 s77, s72, 0\n"
"v_mfma_f32_16x16x32_bf16 v[124:127], a[204:207], a[164:167], v[124:127]\n"
"s_add_u32 m0, s67, 16384\n"
"v_mfma_f32_16x16x32_bf16 v[128:131], a[208:211], a[160:163], v[128:131]\n"
"buffer_load_dwordx4 v45, s[24:27], s77 offen lds\n"
"v_mfma_f32_16x16x32_bf16 v[128:131], a[212:215], a[164:167], v[128:131]\n"
"v_mfma_f32_16x16x32_bf16 v[132:135], a[216:219], a[160:163], v[132:135]\n"
"s_add_u32 s76, s72, 128\n"
"v_mfma_f32_16x16x32_bf16 v[132:135], a[220:223], a[164:167], v[132:135]\n"
"s_add_u32 m0, s67, 17408\n"
"s_waitcnt lgkmcnt(12)\n"
"v_mfma_f32_16x16x32_bf16 v[136:139], a[224:227], a[160:163], v[136:139]\n"
"buffer_load_dwordx4 v45, s[24:27], s76 offen lds\n"
"v_mfma_f32_16x16x32_bf16 v[136:139], a[228:231], a[164:167], v[136:139]\n"
"s_waitcnt lgkmcnt(8)\n"
"v_mfma_f32_16x16x32_bf16 v[140:143], a[232:235], a[160:163], v[140:143]\n"
"v_mfma_f32_16x16x32_bf16 v[140:143], a[236:239], a[164:167], v[140:143]\n"
"s_add_u32 s77, s72, 65536\n"
"s_waitcnt lgkmcnt(4)\n"
"v_mfma_f32_16x16x32_bf16 v[144:147], a[240:243], a[160:163], v[144:147]\n"
"s_add_u32 m0, s67, 18432\n"
"v_mfma_f32_16x16x32_bf16 v[144:147], a[244:247], a[164:167], v[144:147]\n"
"s_waitcnt lgkmcnt(0)\n"
"buffer_load_dwordx4 v45, s[24:27], s77 offen lds\n"
"v_mfma_f32_16x16x32_bf16 v[148:151], a[248:251], a[160:163], v[148:151]\n"
"v_mfma_f32_16x16x32_bf16 v[148:151], a[252:255], a[164:167], v[148:151]\n"
"s_add_u32 s76, s72, 65664\n"
"v_mfma_f32_16x16x32_bf16 v[152:155], a[192:195], a[168:171], v[152:155]\n"
"s_add_u32 m0, s67, 19456\n"
"v_mfma_f32_16x16x32_bf16 v[152:155], a[196:199], a[172:175], v[152:155]\n"
"buffer_load_dwordx4 v45, s[24:27], s76 offen lds\n"
"v_mfma_f32_16x16x32_bf16 v[156:159], a[200:203], a[168:171], v[156:159]\n"
"v_mfma_f32_16x16x32_bf16 v[156:159], a[204:207], a[172:175], v[156:159]\n"
"v_mfma_f32_16x16x32_bf16 v[160:163], a[208:211], a[168:171], v[160:163]\n"
"s_add_u32 s77, s71, 0\n"
"v_mfma_f32_16x16x32_bf16 v[160:163], a[212:215], a[172:175], v[160:163]\n"
"s_add_u32 m0, s65, 65536\n"
"v_mfma_f32_16x16x32_bf16 v[164:167], a[216:219], a[168:171], v[164:167]\n"
"buffer_load_dwordx4 v44, s[20:23], s77 offen lds\n"
"v_mfma_f32_16x16x32_bf16 v[164:167], a[220:223], a[172:175], v[164:167]\n"
"v_mfma_f32_16x16x32_bf16 v[168:171], a[224:227], a[168:171], v[168:171]\n"
"s_add_u32 s76, s71, 16384\n"
"v_mfma_f32_16x16x32_bf16 v[168:171], a[228:231], a[172:175], v[168:171]\n"
"s_add_u32 m0, s65, 67584\n"
"v_mfma_f32_16x16x32_bf16 v[172:175], a[232:235], a[168:171], v[172:175]\n"
"buffer_load_dwordx4 v44, s[20:23], s76 offen lds\n"
"v_mfma_f32_16x16x32_bf16 v[172:175], a[236:239], a[172:175], v[172:175]\n"
"s_waitcnt vmcnt(7)\n"
"s_barrier\n"
"v_mfma_f32_16x16x32_bf16 v[176:179], a[240:243], a[168:171], v[176:179]\n"
"ds_read_b128 a[64:67], v33 offset:0\n"
"ds_read_b128 a[68:71], v33 offset:512\n"
"v_mfma_f32_16x16x32_bf16 v[176:179], a[244:247], a[172:175], v[176:179]\n"
"s_add_u32 s77, s71, 32768\n"
"ds_read_b64_tr_b16 a[96:97], v40 offset:0\n"
"ds_read_b64_tr_b16 a[98:99], v40 offset:256\n"
"ds_read_b32 v50, v51\n"
"v_mfma_f32_16x16x32_bf16 v[180:183], a[248:251], a[168:171], v[180:183]\n"
"s_add_u32 m0, s65, 69632\n"
"ds_read_b64_tr_b16 a[100:101], v40 offset:8192\n"
"v_mfma_f32_16x16x32_bf16 v[180:183], a[252:255], a[172:175], v[180:183]\n"
"buffer_load_dwordx4 v44, s[20:23], s77 offen lds\n"
"ds_read_b64_tr_b16 a[102:103], v40 offset:8448\n"
"v_mfma_f32_16x16x32_bf16 v[184:187], a[192:195], a[176:179], v[184:187]\n"
"ds_read_b128 a[72:75], v33 offset:2048\n"
"ds_read_b128 a[76:79], v33 offset:2560\n"
"v_mfma_f32_16x16x32_bf16 v[184:187], a[196:199], a[180:183], v[184:187]\n"
"s_add_u32 s76, s71, 49152\n"
"ds_read_b64_tr_b16 a[104:105], v40 offset:8\n"
"ds_read_b64_tr_b16 a[106:107], v40 offset:264\n"
"v_mfma_f32_16x16x32_bf16 v[188:191], a[200:203], a[176:179], v[188:191]\n"
"s_add_u32 m0, s65, 71680\n"
"ds_read_b64_tr_b16 a[108:109], v40 offset:8200\n"
"v_mfma_f32_16x16x32_bf16 v[188:191], a[204:207], a[180:183], v[188:191]\n"
"buffer_load_dwordx4 v44, s[20:23], s76 offen lds\n"
"ds_read_b64_tr_b16 a[110:111], v40 offset:8456\n"
"v_mfma_f32_16x16x32_bf16 v[192:195], a[208:211], a[176:179], v[192:195]\n"
"ds_read_b128 a[80:83], v33 offset:4096\n"
"ds_read_b128 a[84:87], v33 offset:4608\n"
"v_mfma_f32_16x16x32_bf16 v[192:195], a[212:215], a[180:183], v[192:195]\n"
"ds_read_b64_tr_b16 a[112:113], v40 offset:512\n"
"v_mfma_f32_16x16x32_bf16 v[196:199], a[216:219], a[176:179], v[196:199]\n"
"s_add_u32 s77, s71, 65536\n"
"ds_read_b64_tr_b16 a[114:115], v40 offset:768\n"
"ds_read_b64_tr_b16 a[116:117], v40 offset:8704\n"
"v_mfma_f32_16x16x32_bf16 v[196:199], a[220:223], a[180:183], v[196:199]\n"
"s_add_u32 m0, s65, 73728\n"
"ds_read_b64_tr_b16 a[118:119], v40 offset:8960\n"
"ds_read_b128 a[88:91], v36 offset:0\n"
"v_mfma_f32_16x16x32_bf16 v[200:203], a[224:227], a[176:179], v[200:203]\n"
"buffer_load_dwordx4 v44, s[20:23], s77 offen lds\n"
"ds_read_b128 a[92:95], v36 offset:512\n"
"v_mfma_f32_16x16x32_bf16 v[200:203], a[228:231], a[180:183], v[200:203]\n"
"ds_read_b64_tr_b16 a[120:121], v40 offset:520\n"
"v_mfma_f32_16x16x32_bf16 v[204:207], a[232:235], a[176:179], v[204:207]\n"
"s_add_u32 s76, s71, 81920\n"
"ds_read_b64_tr_b16 a[122:123], v40 offset:776\n"
"ds_read_b64_tr_b16 a[124:125], v40 offset:8712\n"
"v_mfma_f32_16x16x32_bf16 v[204:207], a[236:239], a[180:183], v[204:207]\n"
"s_add_u32 m0, s65, 75776\n"
"ds_read_b64_tr_b16 a[126:127], v40 offset:8968\n"
"ds_read_b64_tr_b16 a[128:129], v41 offset:0\n"
"v_mfma_f32_16x16x32_bf16 v[208:211], a[240:243], a[176:179], v[208:211]\n"
"buffer_load_dwordx4 v44, s[20:23], s76 offen lds\n"
"v_mfma_f32_16x16x32_bf16 v[208:211], a[244:247], a[180:183], v[208:211]\n"
"ds_read_b64_tr_b16 a[130:131], v41 offset:256\n"
"ds_read_b64_tr_b16 a[132:133], v41 offset:8192\n"
"v_mfma_f32_16x16x32_bf16 v[212:215], a[248:251], a[176:179], v[212:215]\n"
"ds_read_b64_tr_b16 a[134:135], v41 offset:8448\n"
"ds_read_b64_tr_b16 a[136:137], v41 offset:8\n"
"v_mfma_f32_16x16x32_bf16 v[212:215], a[252:255], a[180:183], v[212:215]\n"
"s_add_u32 s77, s71, 98304\n"
"ds_read_b64_tr_b16 a[138:139], v41 offset:264\n"
"v_mfma_f32_16x16x32_bf16 v[216:219], a[192:195], a[184:187], v[216:219]\n"
"s_add_u32 m0, s65, 77824\n"
"ds_read_b64_tr_b16 a[140:141], v41 offset:8200\n"
"ds_read_b64_tr_b16 a[142:143], v41 offset:8456\n"
"v_mfma_f32_16x16x32_bf16 v[216:219], a[196:199], a[188:191], v[216:219]\n"
"buffer_load_dwordx4 v44, s[20:23], s77 offen lds\n"
"ds_read_b64_tr_b16 a[144:145], v41 offset:512\n"
"v_mfma_f32_16x16x32_bf16 v[220:223], a[200:203], a[184:187], v[220:223]\n"
"ds_read_b64_tr_b16 a[146:147], v41 offset:768\n"
"ds_read_b64_tr_b16 a[148:149], v41 offset:8704\n"
"v_mfma_f32_16x16x32_bf16 v[220:223], a[204:207], a[188:191], v[220:223]\n"
"s_add_u32 s76, s84, 8192\n"
"ds_read_b64_tr_b16 a[150:151], v41 offset:8960\n"
"v_mfma_f32_16x16x32_bf16 v[224:227], a[208:211], a[184:187], v[224:227]\n"
"s_add_u32 m0, s68, 8192\n"
"ds_read_b64_tr_b16 a[152:153], v41 offset:520\n"
"ds_read_b64_tr_b16 a[154:155], v41 offset:776\n"
"v_mfma_f32_16x16x32_bf16 v[224:227], a[212:215], a[188:191], v[224:227]\n"
"s_add_u32 s62, s59, 6\n"
"buffer_load_dwordx4 v9, s[28:31], s76 offen nt lds\n"
"ds_read_b64_tr_b16 a[156:157], v41 offset:8712\n"
"v_mfma_f32_16x16x32_bf16 v[228:231], a[216:219], a[184:187], v[228:231]\n"
"s_and_b32 s62, s62, 7\n"
"ds_read_b64_tr_b16 a[158:159], v41 offset:8968\n"
"v_mfma_f32_16x16x32_bf16 v[228:231], a[220:223], a[188:191], v[228:231]\n"
"s_lshl_b32 s63, s62, 7\n"
"s_waitcnt lgkmcnt(15)\n"
"v_mfma_f32_16x16x32_bf16 v[120:123], a[96:99], a[64:67], v[120:123]\n"
"s_add_u32 s71, s64, s63\n"
"v_mfma_f32_16x16x32_bf16 v[120:123], a[100:103], a[68:71], v[120:123]\n"
"s_lshl_b32 s63, s62, 19\n"
"v_mfma_f32_16x16x32_bf16 v[124:127], a[104:107], a[64:67], v[124:127]\n"
"s_add_u32 s72, s66, s63\n"
"s_add_u32 s77, s72, 0\n"
"v_mfma_f32_16x16x32_bf16 v[124:127], a[108:111], a[68:71], v[124:127]\n"
"s_add_u32 m0, s67, 98304\n"
"v_mfma_f32_16x16x32_bf16 v[128:131], a[112:115], a[64:67], v[128:131]\n"
"buffer_load_dwordx4 v45, s[24:27], s77 offen lds\n"
"v_mfma_f32_16x16x32_bf16 v[128:131], a[116:119], a[68:71], v[128:131]\n"
"v_mfma_f32_16x16x32_bf16 v[132:135], a[120:123], a[64:67], v[132:135]\n"
"s_add_u32 s76, s72, 128\n"
"v_mfma_f32_16x16x32_bf16 v[132:135], a[124:127], a[68:71], v[132:135]\n"
"s_add_u32 m0, s67, 99328\n"
"s_waitcnt lgkmcnt(12)\n"
"v_mfma_f32_16x16x32_bf16 v[136:139], a[128:131], a[64:67], v[136:139]\n"
"buffer_load_dwordx4 v45, s[24:27], s76 offen lds\n"
"v_mfma_f32_16x16x32_bf16 v[136:139], a[132:135], a[68:71], v[136:139]\n"
"s_waitcnt lgkmcnt(8)\n"
"v_mfma_f32_16x16x32_bf16 v[140:143], a[136:139], a[64:67], v[140:143]\n"
"v_mfma_f32_16x16x32_bf16 v[140:143], a[140:143], a[68:71], v[140:143]\n"
"s_add_u32 s77, s72, 65536\n"
"s_waitcnt lgkmcnt(4)\n"
"v_mfma_f32_16x16x32_bf16 v[144:147], a[144:147], a[64:67], v[144:147]\n"
"s_add_u32 m0, s67, 100352\n"
"v_mfma_f32_16x16x32_bf16 v[144:147], a[148:151], a[68:71], v[144:147]\n"
"s_waitcnt lgkmcnt(0)\n"
"buffer_load_dwordx4 v45, s[24:27], s77 offen lds\n"
"v_mfma_f32_16x16x32_bf16 v[148:151], a[152:155], a[64:67], v[148:151]\n"
"v_mfma_f32_16x16x32_bf16 v[148:151], a[156:159], a[68:71], v[148:151]\n"
"s_add_u32 s76, s72, 65664\n"
"v_mfma_f32_16x16x32_bf16 v[152:155], a[96:99], a[72:75], v[152:155]\n"
"s_add_u32 m0, s67, 101376\n"
"v_mfma_f32_16x16x32_bf16 v[152:155], a[100:103], a[76:79], v[152:155]\n"
"buffer_load_dwordx4 v45, s[24:27], s76 offen lds\n"
"v_mfma_f32_16x16x32_bf16 v[156:159], a[104:107], a[72:75], v[156:159]\n"
"v_mfma_f32_16x16x32_bf16 v[156:159], a[108:111], a[76:79], v[156:159]\n"
"v_mfma_f32_16x16x32_bf16 v[160:163], a[112:115], a[72:75], v[160:163]\n"
"s_add_u32 s77, s71, 0\n"
"v_mfma_f32_16x16x32_bf16 v[160:163], a[116:119], a[76:79], v[160:163]\n"
"s_add_u32 m0, s65, 32768\n"
"v_mfma_f32_16x16x32_bf16 v[164:167], a[120:123], a[72:75], v[164:167]\n"
"buffer_load_dwordx4 v44, s[20:23], s77 offen lds\n"
"v_mfma_f32_16x16x32_bf16 v[164:167], a[124:127], a[76:79], v[164:167]\n"
"v_mfma_f32_16x16x32_bf16 v[168:171], a[128:131], a[72:75], v[168:171]\n"
"s_add_u32 s76, s71, 16384\n"
"v_mfma_f32_16x16x32_bf16 v[168:171], a[132:135], a[76:79], v[168:171]\n"
"s_add_u32 m0, s65, 34816\n"
"v_mfma_f32_16x16x32_bf16 v[172:175], a[136:139], a[72:75], v[172:175]\n"
"buffer_load_dwordx4 v44, s[20:23], s76 offen lds\n"
"v_mfma_f32_16x16x32_bf16 v[172:175], a[140:143], a[76:79], v[172:175]\n"
"s_waitcnt vmcnt(7)\n"
"s_barrier\n"
"v_mfma_f32_16x16x32_bf16 v[176:179], a[144:147], a[72:75], v[176:179]\n"
"ds_read_b128 a[160:163], v34 offset:0\n"
"ds_read_b128 a[164:167], v34 offset:512\n"
"v_readfirstlane_b32 s73, v50\n"
"v_mfma_f32_16x16x32_bf16 v[176:179], a[148:151], a[76:79], v[176:179]\n"
"s_add_u32 s77, s71, 32768\n"
"ds_read_b64_tr_b16 a[192:193], v42 offset:0\n"
"ds_read_b64_tr_b16 a[194:195], v42 offset:256\n"
"v_mfma_f32_16x16x32_bf16 v[180:183], a[152:155], a[72:75], v[180:183]\n"
"s_add_u32 m0, s65, 36864\n"
"ds_read_b64_tr_b16 a[196:197], v42 offset:8192\n"
"v_mfma_f32_16x16x32_bf16 v[180:183], a[156:159], a[76:79], v[180:183]\n"
"buffer_load_dwordx4 v44, s[20:23], s77 offen lds\n"
"ds_read_b64_tr_b16 a[198:199], v42 offset:8448\n"
"v_mfma_f32_16x16x32_bf16 v[184:187], a[96:99], a[80:83], v[184:187]\n"
"ds_read_b128 a[168:171], v34 offset:2048\n"
"ds_read_b128 a[172:175], v34 offset:2560\n"
"v_mfma_f32_16x16x32_bf16 v[184:187], a[100:103], a[84:87], v[184:187]\n"
"s_add_u32 s76, s71, 49152\n"
"ds_read_b64_tr_b16 a[200:201], v42 offset:8\n"
"ds_read_b64_tr_b16 a[202:203], v42 offset:264\n"
"v_mfma_f32_16x16x32_bf16 v[188:191], a[104:107], a[80:83], v[188:191]\n"
"s_add_u32 m0, s65, 38912\n"
"ds_read_b64_tr_b16 a[204:205], v42 offset:8200\n"
"v_mfma_f32_16x16x32_bf16 v[188:191], a[108:111], a[84:87], v[188:191]\n"
"buffer_load_dwordx4 v44, s[20:23], s76 offen lds\n"
"ds_read_b64_tr_b16 a[206:207], v42 offset:8456\n"
"v_mfma_f32_16x16x32_bf16 v[192:195], a[112:115], a[80:83], v[192:195]\n"
"ds_read_b128 a[176:179], v34 offset:4096\n"
"ds_read_b128 a[180:183], v34 offset:4608\n"
"v_mfma_f32_16x16x32_bf16 v[192:195], a[116:119], a[84:87], v[192:195]\n"
"ds_read_b64_tr_b16 a[208:209], v42 offset:512\n"
"v_mfma_f32_16x16x32_bf16 v[196:199], a[120:123], a[80:83], v[196:199]\n"
"s_add_u32 s77, s71, 65536\n"
"ds_read_b64_tr_b16 a[210:211], v42 offset:768\n"
"ds_read_b64_tr_b16 a[212:213], v42 offset:8704\n"
"v_mfma_f32_16x16x32_bf16 v[196:199], a[124:127], a[84:87], v[196:199]\n"
"s_add_u32 m0, s65, 40960\n"
"ds_read_b64_tr_b16 a[214:215], v42 offset:8960\n"
"ds_read_b128 a[184:187], v37 offset:0\n"
"v_mfma_f32_16x16x32_bf16 v[200:203], a[128:131], a[80:83], v[200:203]\n"
"buffer_load_dwordx4 v44, s[20:23], s77 offen lds\n"
"ds_read_b128 a[188:191], v37 offset:512\n"
"v_mfma_f32_16x16x32_bf16 v[200:203], a[132:135], a[84:87], v[200:203]\n"
"ds_read_b64_tr_b16 a[216:217], v42 offset:520\n"
"v_mfma_f32_16x16x32_bf16 v[204:207], a[136:139], a[80:83], v[204:207]\n"
"s_add_u32 s76, s71, 81920\n"
"ds_read_b64_tr_b16 a[218:219], v42 offset:776\n"
"ds_read_b64_tr_b16 a[220:221], v42 offset:8712\n"
"v_mfma_f32_16x16x32_bf16 v[204:207], a[140:143], a[84:87], v[204:207]\n"
"s_add_u32 m0, s65, 43008\n"
"ds_read_b64_tr_b16 a[222:223], v42 offset:8968\n"
"ds_read_b64_tr_b16 a[224:225], v43 offset:0\n"
"v_mfma_f32_16x16x32_bf16 v[208:211], a[144:147], a[80:83], v[208:211]\n"
"buffer_load_dwordx4 v44, s[20:23], s76 offen lds\n"
"v_mfma_f32_16x16x32_bf16 v[208:211], a[148:151], a[84:87], v[208:211]\n"
"ds_read_b64_tr_b16 a[226:227], v43 offset:256\n"
"ds_read_b64_tr_b16 a[228:229], v43 offset:8192\n"
"v_mfma_f32_16x16x32_bf16 v[212:215], a[152:155], a[80:83], v[212:215]\n"
"ds_read_b64_tr_b16 a[230:231], v43 offset:8448\n"
"ds_read_b64_tr_b16 a[232:233], v43 offset:8\n"
"v_mfma_f32_16x16x32_bf16 v[212:215], a[156:159], a[84:87], v[212:215]\n"
"s_add_u32 s77, s71, 98304\n"
"ds_read_b64_tr_b16 a[234:235], v43 offset:264\n"
"v_mfma_f32_16x16x32_bf16 v[216:219], a[96:99], a[88:91], v[216:219]\n"
"s_add_u32 m0, s65, 45056\n"
"ds_read_b64_tr_b16 a[236:237], v43 offset:8200\n"
"ds_read_b64_tr_b16 a[238:239], v43 offset:8456\n"
"v_mfma_f32_16x16x32_bf16 v[216:219], a[100:103], a[92:95], v[216:219]\n"
"buffer_load_dwordx4 v44, s[20:23], s77 offen lds\n"
"ds_read_b64_tr_b16 a[240:241], v43 offset:512\n"
"v_mfma_f32_16x16x32_bf16 v[220:223], a[104:107], a[88:91], v[220:223]\n"
"ds_read_b64_tr_b16 a[242:243], v43 offset:768\n"
"ds_read_b64_tr_b16 a[244:245], v43 offset:8704\n"
"v_mfma_f32_16x16x32_bf16 v[220:223], a[108:111], a[92:95], v[220:223]\n"
"s_add_u32 s76, s84, 12288\n"
"ds_read_b64_tr_b16 a[246:247], v43 offset:8960\n"
"v_mfma_f32_16x16x32_bf16 v[224:227], a[112:115], a[88:91], v[224:227]\n"
"s_add_u32 m0, s68, 12288\n"
"ds_read_b64_tr_b16 a[248:249], v43 offset:520\n"
"ds_read_b64_tr_b16 a[250:251], v43 offset:776\n"
"v_mfma_f32_16x16x32_bf16 v[224:227], a[116:119], a[92:95], v[224:227]\n"
"s_add_u32 s62, s59, 7\n"
"buffer_load_dwordx4 v9, s[28:31], s76 offen nt lds\n"
"ds_read_b64_tr_b16 a[252:253], v43 offset:8712\n"
"v_mfma_f32_16x16x32_bf16 v[228:231], a[120:123], a[88:91], v[228:231]\n"
"s_and_b32 s62, s62, 7\n"
"ds_read_b64_tr_b16 a[254:255], v43 offset:8968\n"
"v_mfma_f32_16x16x32_bf16 v[228:231], a[124:127], a[92:95], v[228:231]\n"
"s_lshl_b32 s63, s62, 7\n"
"s_waitcnt lgkmcnt(15)\n"
"v_mfma_f32_16x16x32_bf16 v[120:123], a[192:195], a[160:163], v[120:123]\n"
"s_add_u32 s71, s64, s63\n"
"v_mfma_f32_16x16x32_bf16 v[120:123], a[196:199], a[164:167], v[120:123]\n"
"s_lshl_b32 s63, s62, 19\n"
"v_mfma_f32_16x16x32_bf16 v[124:127], a[200:203], a[160:163], v[124:127]\n"
"s_add_u32 s72, s66, s63\n"
"s_add_u32 s77, s72, 0\n"
"v_mfma_f32_16x16x32_bf16 v[124:127], a[204:207], a[164:167], v[124:127]\n"
"s_add_u32 m0, s67, 114688\n"
"v_mfma_f32_16x16x32_bf16 v[128:131], a[208:211], a[160:163], v[128:131]\n"
"buffer_load_dwordx4 v45, s[24:27], s77 offen lds\n"
"v_mfma_f32_16x16x32_bf16 v[128:131], a[212:215], a[164:167], v[128:131]\n"
"v_mfma_f32_16x16x32_bf16 v[132:135], a[216:219], a[160:163], v[132:135]\n"
"s_add_u32 s76, s72, 128\n"
"v_mfma_f32_16x16x32_bf16 v[132:135], a[220:223], a[164:167], v[132:135]\n"
"s_add_u32 m0, s67, 115712\n"
"s_waitcnt lgkmcnt(12)\n"
"v_mfma_f32_16x16x32_bf16 v[136:139], a[224:227], a[160:163], v[136:139]\n"
"buffer_load_dwordx4 v45, s[24:27], s76 offen lds\n"
"v_mfma_f32_16x16x32_bf16 v[136:139], a[228:231], a[164:167], v[136:139]\n"
"s_waitcnt lgkmcnt(8)\n"
"v_mfma_f32_16x16x32_bf16 v[140:143], a[232:235], a[160:163], v[140:143]\n"
"v_mfma_f32_16x16x32_bf16 v[140:143], a[236:239], a[164:167], v[140:143]\n"
"s_add_u32 s77, s72, 65536\n"
"s_waitcnt lgkmcnt(4)\n"
"v_mfma_f32_16x16x32_bf16 v[144:147], a[240:243], a[160:163], v[144:147]\n"
"s_add_u32 m0, s67, 116736\n"
"v_mfma_f32_16x16x32_bf16 v[144:147], a[244:247], a[164:167], v[144:147]\n"
"s_waitcnt lgkmcnt(0)\n"
"buffer_load_dwordx4 v45, s[24:27], s77 offen lds\n"
"v_mfma_f32_16x16x32_bf16 v[148:151], a[248:251], a[160:163], v[148:151]\n"
"v_mfma_f32_16x16x32_bf16 v[148:151], a[252:255], a[164:167], v[148:151]\n"
"s_add_u32 s76, s72, 65664\n"
"v_mfma_f32_16x16x32_bf16 v[152:155], a[192:195], a[168:171], v[152:155]\n"
"s_add_u32 m0, s67, 117760\n"
"v_mfma_f32_16x16x32_bf16 v[152:155], a[196:199], a[172:175], v[152:155]\n"
"buffer_load_dwordx4 v45, s[24:27], s76 offen lds\n"
"v_mfma_f32_16x16x32_bf16 v[156:159], a[200:203], a[168:171], v[156:159]\n"
"v_mfma_f32_16x16x32_bf16 v[156:159], a[204:207], a[172:175], v[156:159]\n"
"v_mfma_f32_16x16x32_bf16 v[160:163], a[208:211], a[168:171], v[160:163]\n"
"s_add_u32 s77, s71, 0\n"
"v_mfma_f32_16x16x32_bf16 v[160:163], a[212:215], a[172:175], v[160:163]\n"
"s_add_u32 m0, s65, 131072\n"
"v_mfma_f32_16x16x32_bf16 v[164:167], a[216:219], a[168:171], v[164:167]\n"
"buffer_load_dwordx4 v44, s[20:23], s77 offen lds\n"
"v_mfma_f32_16x16x32_bf16 v[164:167], a[220:223], a[172:175], v[164:167]\n"
"v_mfma_f32_16x16x32_bf16 v[168:171], a[224:227], a[168:171], v[168:171]\n"
"s_add_u32 s76, s71, 16384\n"
"v_mfma_f32_16x16x32_bf16 v[168:171], a[228:231], a[172:175], v[168:171]\n"
"s_add_u32 m0, s65, 133120\n"
"v_mfma_f32_16x16x32_bf16 v[172:175], a[232:235], a[168:171], v[172:175]\n"
"buffer_load_dwordx4 v44, s[20:23], s76 offen lds\n"
"v_mfma_f32_16x16x32_bf16 v[172:175], a[236:239], a[172:175], v[172:175]\n"
"s_waitcnt vmcnt(7)\n"
"s_barrier\n"
"v_mfma_f32_16x16x32_bf16 v[176:179], a[240:243], a[168:171], v[176:179]\n"
"ds_read_b128 a[64:67], v32 offset:0\n"
"ds_read_b128 a[68:71], v32 offset:512\n"
"v_mfma_f32_16x16x32_bf16 v[176:179], a[244:247], a[172:175], v[176:179]\n"
"s_add_u32 s77, s71, 32768\n"
"ds_read_b64_tr_b16 a[96:97], v38 offset:0\n"
"ds_read_b64_tr_b16 a[98:99], v38 offset:256\n"
"v_mfma_f32_16x16x32_bf16 v[180:183], a[248:251], a[168:171], v[180:183]\n"
"s_add_u32 m0, s65, 135168\n"
"ds_read_b64_tr_b16 a[100:101], v38 offset:8192\n"
"v_mfma_f32_16x16x32_bf16 v[180:183], a[252:255], a[172:175], v[180:183]\n"
"buffer_load_dwordx4 v44, s[20:23], s77 offen lds\n"
"ds_read_b64_tr_b16 a[102:103], v38 offset:8448\n"
"v_mfma_f32_16x16x32_bf16 v[184:187], a[192:195], a[176:179], v[184:187]\n"
"ds_read_b128 a[72:75], v32 offset:2048\n"
"ds_read_b128 a[76:79], v32 offset:2560\n"
"v_mfma_f32_16x16x32_bf16 v[184:187], a[196:199], a[180:183], v[184:187]\n"
"s_add_u32 s76, s71, 49152\n"
"ds_read_b64_tr_b16 a[104:105], v38 offset:8\n"
"ds_read_b64_tr_b16 a[106:107], v38 offset:264\n"
"v_mfma_f32_16x16x32_bf16 v[188:191], a[200:203], a[176:179], v[188:191]\n"
"s_add_u32 m0, s65, 137216\n"
"ds_read_b64_tr_b16 a[108:109], v38 offset:8200\n"
"v_mfma_f32_16x16x32_bf16 v[188:191], a[204:207], a[180:183], v[188:191]\n"
"buffer_load_dwordx4 v44, s[20:23], s76 offen lds\n"
"ds_read_b64_tr_b16 a[110:111], v38 offset:8456\n"
"v_mfma_f32_16x16x32_bf16 v[192:195], a[208:211], a[176:179], v[192:195]\n"
"ds_read_b128 a[80:83], v32 offset:4096\n"
"ds_read_b128 a[84:87], v32 offset:4608\n"
"v_mfma_f32_16x16x32_bf16 v[192:195], a[212:215], a[180:183], v[192:195]\n"
"ds_read_b64_tr_b16 a[112:113], v38 offset:512\n"
"v_mfma_f32_16x16x32_bf16 v[196:199], a[216:219], a[176:179], v[196:199]\n"
"s_add_u32 s77, s71, 65536\n"
"ds_read_b64_tr_b16 a[114:115], v38 offset:768\n"
"ds_read_b64_tr_b16 a[116:117], v38 offset:8704\n"
"v_mfma_f32_16x16x32_bf16 v[196:199], a[220:223], a[180:183], v[196:199]\n"
"s_add_u32 m0, s65, 139264\n"
"ds_read_b64_tr_b16 a[118:119], v38 offset:8960\n"
"ds_read_b128 a[88:91], v35 offset:0\n"
"v_mfma_f32_16x16x32_bf16 v[200:203], a[224:227], a[176:179], v[200:203]\n"
"buffer_load_dwordx4 v44, s[20:23], s77 offen lds\n"
"ds_read_b128 a[92:95], v35 offset:512\n"
"v_mfma_f32_16x16x32_bf16 v[200:203], a[228:231], a[180:183], v[200:203]\n"
"ds_read_b64_tr_b16 a[120:121], v38 offset:520\n"
"v_mfma_f32_16x16x32_bf16 v[204:207], a[232:235], a[176:179], v[204:207]\n"
"s_add_u32 s76, s71, 81920\n"
"ds_read_b64_tr_b16 a[122:123], v38 offset:776\n"
"ds_read_b64_tr_b16 a[124:125], v38 offset:8712\n"
"v_mfma_f32_16x16x32_bf16 v[204:207], a[236:239], a[180:183], v[204:207]\n"
"s_add_u32 m0, s65, 141312\n"
"ds_read_b64_tr_b16 a[126:127], v38 offset:8968\n"
"ds_read_b64_tr_b16 a[128:129], v39 offset:0\n"
"v_mfma_f32_16x16x32_bf16 v[208:211], a[240:243], a[176:179], v[208:211]\n"
"buffer_load_dwordx4 v44, s[20:23], s76 offen lds\n"
"v_mfma_f32_16x16x32_bf16 v[208:211], a[244:247], a[180:183], v[208:211]\n"
"ds_read_b64_tr_b16 a[130:131], v39 offset:256\n"
"ds_read_b64_tr_b16 a[132:133], v39 offset:8192\n"
"v_mfma_f32_16x16x32_bf16 v[212:215], a[248:251], a[176:179], v[212:215]\n"
"ds_read_b64_tr_b16 a[134:135], v39 offset:8448\n"
"ds_read_b64_tr_b16 a[136:137], v39 offset:8\n"
"v_mfma_f32_16x16x32_bf16 v[212:215], a[252:255], a[180:183], v[212:215]\n"
"s_add_u32 s77, s71, 98304\n"
"ds_read_b64_tr_b16 a[138:139], v39 offset:264\n"
"v_mfma_f32_16x16x32_bf16 v[216:219], a[192:195], a[184:187], v[216:219]\n"
"s_add_u32 m0, s65, 143360\n"
"ds_read_b64_tr_b16 a[140:141], v39 offset:8200\n"
"ds_read_b64_tr_b16 a[142:143], v39 offset:8456\n"
"v_mfma_f32_16x16x32_bf16 v[216:219], a[196:199], a[188:191], v[216:219]\n"
"s_add_u32 s61, s75, 0\n"
"buffer_load_dwordx4 v44, s[20:23], s77 offen lds\n"
"ds_read_b64_tr_b16 a[144:145], v39 offset:512\n"
"v_mfma_f32_16x16x32_bf16 v[220:223], a[200:203], a[184:187], v[220:223]\n"
"s_and_b32 s61, s61, 15\n"
"ds_read_b64_tr_b16 a[146:147], v39 offset:768\n"
"ds_read_b64_tr_b16 a[148:149], v39 offset:8704\n"
"v_mfma_f32_16x16x32_bf16 v[220:223], a[204:207], a[188:191], v[220:223]\n"
"s_lshl_b32 s61, s61, 14\n"
"ds_read_b64_tr_b16 a[150:151], v39 offset:8960\n"
"v_mfma_f32_16x16x32_bf16 v[224:227], a[208:211], a[184:187], v[224:227]\n"
"s_add_u32 s71, s68, s61\n"
"s_add_u32 s76, s71, 0\n"
"ds_read_b64_tr_b16 a[152:153], v39 offset:520\n"
"ds_read_b64_tr_b16 a[154:155], v39 offset:776\n"
"v_mfma_f32_16x16x32_bf16 v[224:227], a[212:215], a[188:191], v[224:227]\n"
"s_add_u32 m0, s68, 65536\n"
"ds_read_b64_tr_b16 a[156:157], v39 offset:8712\n"
"ds_read_b64_tr_b16 a[158:159], v39 offset:8968\n"
"v_mfma_f32_16x16x32_bf16 v[228:231], a[216:219], a[184:187], v[228:231]\n"
"buffer_load_dwordx4 v10, s[88:91], s76 offen nt lds\n"
"v_mfma_f32_16x16x32_bf16 v[228:231], a[220:223], a[188:191], v[228:231]\n"
"s_waitcnt lgkmcnt(15)\n"
"v_mfma_f32_16x16x32_bf16 v[120:123], a[96:99], a[64:67], v[120:123]\n"
"s_add_u32 s77, s71, 4096\n"
"v_mfma_f32_16x16x32_bf16 v[120:123], a[100:103], a[68:71], v[120:123]\n"
"s_add_u32 m0, s68, 69632\n"
"v_mfma_f32_16x16x32_bf16 v[124:127], a[104:107], a[64:67], v[124:127]\n"
"buffer_load_dwordx4 v10, s[88:91], s77 offen nt lds\n"
"v_mfma_f32_16x16x32_bf16 v[124:127], a[108:111], a[68:71], v[124:127]\n"
"v_mfma_f32_16x16x32_bf16 v[128:131], a[112:115], a[64:67], v[128:131]\n"
"s_add_u32 s76, s71, 8192\n"
"v_mfma_f32_16x16x32_bf16 v[128:131], a[116:119], a[68:71], v[128:131]\n"
"s_add_u32 m0, s68, 73728\n"
"v_mfma_f32_16x16x32_bf16 v[132:135], a[120:123], a[64:67], v[132:135]\n"
"buffer_load_dwordx4 v10, s[88:91], s76 offen nt lds\n"
"v_mfma_f32_16x16x32_bf16 v[132:135], a[124:127], a[68:71], v[132:135]\n"
"s_waitcnt lgkmcnt(12)\n"
"v_mfma_f32_16x16x32_bf16 v[136:139], a[128:131], a[64:67], v[136:139]\n"
"s_add_u32 s77, s71, 12288\n"
"v_mfma_f32_16x16x32_bf16 v[136:139], a[132:135], a[68:71], v[136:139]\n"
"s_add_u32 m0, s68, 77824\n"
"s_waitcnt lgkmcnt(8)\n"
"v_mfma_f32_16x16x32_bf16 v[140:143], a[136:139], a[64:67], v[140:143]\n"
"s_add_u32 s61, s75, 1\n"
"buffer_load_dwordx4 v10, s[88:91], s77 offen nt lds\n"
"v_mfma_f32_16x16x32_bf16 v[140:143], a[140:143], a[68:71], v[140:143]\n"
"s_and_b32 s61, s61, 15\n"
"s_waitcnt lgkmcnt(4)\n"
"v_mfma_f32_16x16x32_bf16 v[144:147], a[144:147], a[64:67], v[144:147]\n"
"s_lshl_b32 s61, s61, 14\n"
"v_mfma_f32_16x16x32_bf16 v[144:147], a[148:151], a[68:71], v[144:147]\n"
"s_add_u32 s71, s68, s61\n"
"s_add_u32 s76, s71, 0\n"
"s_waitcnt lgkmcnt(0)\n"
"v_mfma_f32_16x16x32_bf16 v[148:151], a[152:155], a[64:67], v[148:151]\n"
"s_add_u32 m0, s68, 16384\n"
"v_mfma_f32_16x16x32_bf16 v[148:151], a[156:159], a[68:71], v[148:151]\n"
"buffer_load_dwordx4 v9, s[28:31], s76 offen nt lds\n"
"v_mfma_f32_16x16x32_bf16 v[152:155], a[96:99], a[72:75], v[152:155]\n"
"v_mfma_f32_16x16x32_bf16 v[152:155], a[100:103], a[76:79], v[152:155]\n"
"v_mfma_f32_16x16x32_bf16 v[156:159], a[104:107], a[72:75], v[156:159]\n"
"v_mfma_f32_16x16x32_bf16 v[156:159], a[108:111], a[76:79], v[156:159]\n"
"v_mfma_f32_16x16x32_bf16 v[160:163], a[112:115], a[72:75], v[160:163]\n"
"v_mfma_f32_16x16x32_bf16 v[160:163], a[116:119], a[76:79], v[160:163]\n"
"v_mfma_f32_16x16x32_bf16 v[164:167], a[120:123], a[72:75], v[164:167]\n"
"v_mfma_f32_16x16x32_bf16 v[164:167], a[124:127], a[76:79], v[164:167]\n"
"s_add_u32 s77, s71, 4096\n"
"v_mfma_f32_16x16x32_bf16 v[168:171], a[128:131], a[72:75], v[168:171]\n"
"s_add_u32 m0, s68, 20480\n"
"v_mfma_f32_16x16x32_bf16 v[168:171], a[132:135], a[76:79], v[168:171]\n"
"buffer_load_dwordx4 v9, s[28:31], s77 offen nt lds\n"
"v_mfma_f32_16x16x32_bf16 v[172:175], a[136:139], a[72:75], v[172:175]\n"
"v_mfma_f32_16x16x32_bf16 v[172:175], a[140:143], a[76:79], v[172:175]\n"
"s_waitcnt vmcnt(6)\n"
"s_barrier\n"
"v_mfma_f32_16x16x32_bf16 v[176:179], a[144:147], a[72:75], v[176:179]\n"
"ds_read_b128 a[160:163], v33 offset:0\n"
"ds_read_b128 a[164:167], v33 offset:512\n"
"v_mfma_f32_16x16x32_bf16 v[176:179], a[148:151], a[76:79], v[176:179]\n"
"ds_read_b64_tr_b16 a[192:193], v40 offset:0\n"
"ds_read_b64_tr_b16 a[194:195], v40 offset:256\n"
"v_mfma_f32_16x16x32_bf16 v[180:183], a[152:155], a[72:75], v[180:183]\n"
"ds_read_b64_tr_b16 a[196:197], v40 offset:8192\n"
"v_mfma_f32_16x16x32_bf16 v[180:183], a[156:159], a[76:79], v[180:183]\n"
"ds_read_b64_tr_b16 a[198:199], v40 offset:8448\n"
"ds_read_b128 a[168:171], v33 offset:2048\n"
"v_mfma_f32_16x16x32_bf16 v[184:187], a[96:99], a[80:83], v[184:187]\n"
"ds_read_b128 a[172:175], v33 offset:2560\n"
"v_mfma_f32_16x16x32_bf16 v[184:187], a[100:103], a[84:87], v[184:187]\n"
"s_add_u32 s76, s71, 8192\n"
"ds_read_b64_tr_b16 a[200:201], v40 offset:8\n"
"ds_read_b64_tr_b16 a[202:203], v40 offset:264\n"
"v_mfma_f32_16x16x32_bf16 v[188:191], a[104:107], a[80:83], v[188:191]\n"
"s_add_u32 m0, s68, 24576\n"
"ds_read_b64_tr_b16 a[204:205], v40 offset:8200\n"
"v_mfma_f32_16x16x32_bf16 v[188:191], a[108:111], a[84:87], v[188:191]\n"
"buffer_load_dwordx4 v9, s[28:31], s76 offen nt lds\n"
"ds_read_b64_tr_b16 a[206:207], v40 offset:8456\n"
"v_mfma_f32_16x16x32_bf16 v[192:195], a[112:115], a[80:83], v[192:195]\n"
"ds_read_b128 a[176:179], v33 offset:4096\n"
"ds_read_b128 a[180:183], v33 offset:4608\n"
"v_mfma_f32_16x16x32_bf16 v[192:195], a[116:119], a[84:87], v[192:195]\n"
"ds_read_b64_tr_b16 a[208:209], v40 offset:512\n"
"v_mfma_f32_16x16x32_bf16 v[196:199], a[120:123], a[80:83], v[196:199]\n"
"ds_read_b64_tr_b16 a[210:211], v40 offset:768\n"
"ds_read_b64_tr_b16 a[212:213], v40 offset:8704\n"
"v_mfma_f32_16x16x32_bf16 v[196:199], a[124:127], a[84:87], v[196:199]\n"
"ds_read_b64_tr_b16 a[214:215], v40 offset:8960\n"
"v_mfma_f32_16x16x32_bf16 v[200:203], a[128:131], a[80:83], v[200:203]\n"
"ds_read_b128 a[184:187], v36 offset:0\n"
"ds_read_b128 a[188:191], v36 offset:512\n"
"v_mfma_f32_16x16x32_bf16 v[200:203], a[132:135], a[84:87], v[200:203]\n"
"ds_read_b64_tr_b16 a[216:217], v40 offset:520\n"
"v_mfma_f32_16x16x32_bf16 v[204:207], a[136:139], a[80:83], v[204:207]\n"
"ds_read_b64_tr_b16 a[218:219], v40 offset:776\n"
"ds_read_b64_tr_b16 a[220:221], v40 offset:8712\n"
"v_mfma_f32_16x16x32_bf16 v[204:207], a[140:143], a[84:87], v[204:207]\n"
"s_add_u32 s77, s71, 12288\n"
"ds_read_b64_tr_b16 a[222:223], v40 offset:8968\n"
"v_mfma_f32_16x16x32_bf16 v[208:211], a[144:147], a[80:83], v[208:211]\n"
"s_add_u32 m0, s68, 28672\n"
"ds_read_b64_tr_b16 a[224:225], v41 offset:0\n"
"ds_read_b64_tr_b16 a[226:227], v41 offset:256\n"
"v_mfma_f32_16x16x32_bf16 v[208:211], a[148:151], a[84:87], v[208:211]\n"
"buffer_load_dwordx4 v9, s[28:31], s77 offen nt lds\n"
"ds_read_b64_tr_b16 a[228:229], v41 offset:8192\n"
"v_mfma_f32_16x16x32_bf16 v[212:215], a[152:155], a[80:83], v[212:215]\n"
"ds_read_b64_tr_b16 a[230:231], v41 offset:8448\n"
"v_mfma_f32_16x16x32_bf16 v[212:215], a[156:159], a[84:87], v[212:215]\n"
"ds_read_b64_tr_b16 a[232:233], v41 offset:8\n"
"ds_read_b64_tr_b16 a[234:235], v41 offset:264\n"
"v_mfma_f32_16x16x32_bf16 v[216:219], a[96:99], a[88:91], v[216:219]\n"
"ds_read_b64_tr_b16 a[236:237], v41 offset:8200\n"
"v_mfma_f32_16x16x32_bf16 v[216:219], a[100:103], a[92:95], v[216:219]\n"
"ds_read_b64_tr_b16 a[238:239], v41 offset:8456\n"
"ds_read_b64_tr_b16 a[240:241], v41 offset:512\n"
"v_mfma_f32_16x16x32_bf16 v[220:223], a[104:107], a[88:91], v[220:223]\n"
"ds_read_b64_tr_b16 a[242:243], v41 offset:768\n"
"v_mfma_f32_16x16x32_bf16 v[220:223], a[108:111], a[92:95], v[220:223]\n"
"ds_read_b64_tr_b16 a[244:245], v41 offset:8704\n"
"ds_read_b64_tr_b16 a[246:247], v41 offset:8960\n"
"v_mfma_f32_16x16x32_bf16 v[224:227], a[112:115], a[88:91], v[224:227]\n"
"ds_read_b64_tr_b16 a[248:249], v41 offset:520\n"
"v_mfma_f32_16x16x32_bf16 v[224:227], a[116:119], a[92:95], v[224:227]\n"
"s_add_u32 s76, s71, 0\n"
"ds_read_b64_tr_b16 a[250:251], v41 offset:776\n"
"ds_read_b64_tr_b16 a[252:253], v41 offset:8712\n"
"v_mfma_f32_16x16x32_bf16 v[228:231], a[120:123], a[88:91], v[228:231]\n"
"s_add_u32 m0, s68, 81920\n"
"ds_read_b64_tr_b16 a[254:255], v41 offset:8968\n"
"v_mfma_f32_16x16x32_bf16 v[228:231], a[124:127], a[92:95], v[228:231]\n"
"s_waitcnt lgkmcnt(15)\n"
"buffer_load_dwordx4 v10, s[88:91], s76 offen nt lds\n"
"v_mfma_f32_16x16x32_bf16 v[216:219], a[192:195], a[184:187], v[216:219]\n"
"v_mfma_f32_16x16x32_bf16 v[216:219], a[196:199], a[188:191], v[216:219]\n"
"v_mfma_f32_16x16x32_bf16 v[220:223], a[200:203], a[184:187], v[220:223]\n"
"v_mfma_f32_16x16x32_bf16 v[220:223], a[204:207], a[188:191], v[220:223]\n"
"v_mfma_f32_16x16x32_bf16 v[224:227], a[208:211], a[184:187], v[224:227]\n"
"v_mfma_f32_16x16x32_bf16 v[224:227], a[212:215], a[188:191], v[224:227]\n"
"v_mfma_f32_16x16x32_bf16 v[228:231], a[216:219], a[184:187], v[228:231]\n"
"v_mfma_f32_16x16x32_bf16 v[228:231], a[220:223], a[188:191], v[228:231]\n"
"s_add_u32 s77, s71, 4096\n"
"s_waitcnt lgkmcnt(0)\n"
"v_mul_f32 v216, s74, v216\n"
"v_mul_f32 v217, s74, v217\n"
"v_mfma_f32_16x16x32_bf16 v[120:123], a[192:195], a[160:163], v[120:123]\n"
"s_add_u32 m0, s68, 86016\n"
"v_mul_f32 v218, s74, v218\n"
"v_mul_f32 v219, s74, v219\n"
"v_mfma_f32_16x16x32_bf16 v[120:123], a[196:199], a[164:167], v[120:123]\n"
"buffer_load_dwordx4 v10, s[88:91], s77 offen nt lds\n"
"v_cvt_pk_bf16_f32 v104, v216, v217\n"
"v_mfma_f32_16x16x32_bf16 v[124:127], a[200:203], a[160:163], v[124:127]\n"
"v_cvt_pk_bf16_f32 v105, v218, v219\n"
"v_mul_f32 v220, s74, v220\n"
"v_mfma_f32_16x16x32_bf16 v[124:127], a[204:207], a[164:167], v[124:127]\n"
"v_mul_f32 v221, s74, v221\n"
"v_mul_f32 v222, s74, v222\n"
"v_mfma_f32_16x16x32_bf16 v[128:131], a[208:211], a[160:163], v[128:131]\n"
"v_mul_f32 v223, s74, v223\n"
"v_cvt_pk_bf16_f32 v106, v220, v221\n"
"v_mfma_f32_16x16x32_bf16 v[128:131], a[212:215], a[164:167], v[128:131]\n"
"v_cvt_pk_bf16_f32 v107, v222, v223\n"
"v_mfma_f32_16x16x32_bf16 v[132:135], a[216:219], a[160:163], v[132:135]\n"
"ds_write_b128 v46, v[104:107] offset:0\n"
"v_mul_f32 v224, s74, v224\n"
"v_mfma_f32_16x16x32_bf16 v[132:135], a[220:223], a[164:167], v[132:135]\n"
"v_mul_f32 v225, s74, v225\n"
"v_mul_f32 v226, s74, v226\n"
"v_mfma_f32_16x16x32_bf16 v[136:139], a[224:227], a[160:163], v[136:139]\n"
"v_mul_f32 v227, s74, v227\n"
"v_cvt_pk_bf16_f32 v108, v224, v225\n"
"v_mfma_f32_16x16x32_bf16 v[136:139], a[228:231], a[164:167], v[136:139]\n"
"s_add_u32 s76, s71, 8192\n"
"v_cvt_pk_bf16_f32 v109, v226, v227\n"
"v_mul_f32 v228, s74, v228\n"
"v_mfma_f32_16x16x32_bf16 v[140:143], a[232:235], a[160:163], v[140:143]\n"
"s_add_u32 m0, s68, 90112\n"
"v_mul_f32 v229, s74, v229\n"
"v_mul_f32 v230, s74, v230\n"
"v_mfma_f32_16x16x32_bf16 v[140:143], a[236:239], a[164:167], v[140:143]\n"
"buffer_load_dwordx4 v10, s[88:91], s76 offen nt lds\n"
"v_mul_f32 v231, s74, v231\n"
"v_mfma_f32_16x16x32_bf16 v[144:147], a[240:243], a[160:163], v[144:147]\n"
"v_cvt_pk_bf16_f32 v110, v228, v229\n"
"v_cvt_pk_bf16_f32 v111, v230, v231\n"
"v_mfma_f32_16x16x32_bf16 v[144:147], a[244:247], a[164:167], v[144:147]\n"
"ds_write_b128 v46, v[108:111] offset:1024\n"
"v_mul_f32 v120, s74, v120\n"
"v_mfma_f32_16x16x32_bf16 v[148:151], a[248:251], a[160:163], v[148:151]\n"
"v_mul_f32 v121, s74, v121\n"
"v_mul_f32 v122, s74, v122\n"
"v_mfma_f32_16x16x32_bf16 v[148:151], a[252:255], a[164:167], v[148:151]\n"
"v_mul_f32 v123, s74, v123\n"
"v_cvt_pk_bf16_f32 v56, v120, v121\n"
"v_mfma_f32_16x16x32_bf16 v[152:155], a[192:195], a[168:171], v[152:155]\n"
"v_cvt_pk_bf16_f32 v57, v122, v123\n"
"v_mul_f32 v124, s74, v124\n"
"v_mfma_f32_16x16x32_bf16 v[152:155], a[196:199], a[172:175], v[152:155]\n"
"v_mul_f32 v125, s74, v125\n"
"v_mul_f32 v126, s74, v126\n"
"v_mfma_f32_16x16x32_bf16 v[156:159], a[200:203], a[168:171], v[156:159]\n"
"v_mul_f32 v127, s74, v127\n"
"v_cvt_pk_bf16_f32 v58, v124, v125\n"
"v_mfma_f32_16x16x32_bf16 v[156:159], a[204:207], a[172:175], v[156:159]\n"
"s_add_u32 s77, s71, 12288\n"
"v_cvt_pk_bf16_f32 v59, v126, v127\n"
"v_mul_f32 v128, s74, v128\n"
"v_mfma_f32_16x16x32_bf16 v[160:163], a[208:211], a[168:171], v[160:163]\n"
"s_add_u32 m0, s68, 94208\n"
"v_mul_f32 v129, s74, v129\n"
"v_mul_f32 v130, s74, v130\n"
"v_mfma_f32_16x16x32_bf16 v[160:163], a[212:215], a[172:175], v[160:163]\n"
"buffer_load_dwordx4 v10, s[88:91], s77 offen nt lds\n"
"v_mul_f32 v131, s74, v131\n"
"v_mfma_f32_16x16x32_bf16 v[164:167], a[216:219], a[168:171], v[164:167]\n"
"v_cvt_pk_bf16_f32 v60, v128, v129\n"
"v_cvt_pk_bf16_f32 v61, v130, v131\n"
"v_mfma_f32_16x16x32_bf16 v[164:167], a[220:223], a[172:175], v[164:167]\n"
"v_mul_f32 v132, s74, v132\n"
"v_mul_f32 v133, s74, v133\n"
"v_mfma_f32_16x16x32_bf16 v[168:171], a[224:227], a[168:171], v[168:171]\n"
"v_mul_f32 v134, s74, v134\n"
"v_mul_f32 v135, s74, v135\n"
"v_mfma_f32_16x16x32_bf16 v[168:171], a[228:231], a[172:175], v[168:171]\n"
"s_add_u32 s61, s75, 2\n"
"v_cvt_pk_bf16_f32 v62, v132, v133\n"
"v_cvt_pk_bf16_f32 v63, v134, v135\n"
"v_mfma_f32_16x16x32_bf16 v[172:175], a[232:235], a[168:171], v[172:175]\n"
"s_and_b32 s61, s61, 15\n"
"v_mul_f32 v136, s74, v136\n"
"v_mul_f32 v137, s74, v137\n"
"v_mfma_f32_16x16x32_bf16 v[172:175], a[236:239], a[172:175], v[172:175]\n"
"s_lshl_b32 s61, s61, 14\n"
"v_mul_f32 v138, s74, v138\n"
"v_mul_f32 v139, s74, v139\n"
"v_mfma_f32_16x16x32_bf16 v[176:179], a[240:243], a[168:171], v[176:179]\n"
"s_add_u32 s71, s68, s61\n"
"v_cvt_pk_bf16_f32 v64, v136, v137\n"
"v_cvt_pk_bf16_f32 v65, v138, v139\n"
"v_mfma_f32_16x16x32_bf16 v[176:179], a[244:247], a[172:175], v[176:179]\n"
"s_add_u32 s76, s71, 0\n"
"v_mul_f32 v140, s74, v140\n"
"v_mul_f32 v141, s74, v141\n"
"v_mfma_f32_16x16x32_bf16 v[180:183], a[248:251], a[168:171], v[180:183]\n"
"s_add_u32 m0, s68, 32768\n"
"v_mul_f32 v142, s74, v142\n"
"v_mul_f32 v143, s74, v143\n"
"v_mfma_f32_16x16x32_bf16 v[180:183], a[252:255], a[172:175], v[180:183]\n"
"buffer_load_dwordx4 v9, s[28:31], s76 offen nt lds\n"
"v_cvt_pk_bf16_f32 v66, v140, v141\n"
"v_mfma_f32_16x16x32_bf16 v[184:187], a[192:195], a[176:179], v[184:187]\n"
"v_cvt_pk_bf16_f32 v67, v142, v143\n"
"v_mul_f32 v144, s74, v144\n"
"v_mfma_f32_16x16x32_bf16 v[184:187], a[196:199], a[180:183], v[184:187]\n"
"v_mul_f32 v145, s74, v145\n"
"v_mul_f32 v146, s74, v146\n"
"v_mfma_f32_16x16x32_bf16 v[188:191], a[200:203], a[176:179], v[188:191]\n"
"v_mul_f32 v147, s74, v147\n"
"v_cvt_pk_bf16_f32 v68, v144, v145\n"
"v_mfma_f32_16x16x32_bf16 v[188:191], a[204:207], a[180:183], v[188:191]\n"
"v_cvt_pk_bf16_f32 v69, v146, v147\n"
"v_mul_f32 v148, s74, v148\n"
"v_mfma_f32_16x16x32_bf16 v[192:195], a[208:211], a[176:179], v[192:195]\n"
"s_waitcnt vmcnt(9) lgkmcnt(0)\n"
"s_barrier\n"
"v_mul_f32 v149, s74, v149\n"
"v_mul_f32 v150, s74, v150\n"
"v_mfma_f32_16x16x32_bf16 v[192:195], a[212:215], a[180:183], v[192:195]\n"
"ds_read_b128 a[0:3], v1 offset:0\n"
"ds_read_b128 a[4:7], v1 offset:256\n"
"v_mfma_f32_16x16x32_bf16 v[196:199], a[216:219], a[176:179], v[196:199]\n"
"ds_read_b128 a[8:11], v2 offset:0\n"
"ds_read_b128 a[12:15], v2 offset:256\n"
"v_mfma_f32_16x16x32_bf16 v[196:199], a[220:223], a[180:183], v[196:199]\n"
"s_add_u32 s77, s71, 4096\n"
"ds_read_b128 a[16:19], v1 offset:4096\n"
"ds_read_b128 a[20:23], v1 offset:4352\n"
"v_mfma_f32_16x16x32_bf16 v[200:203], a[224:227], a[176:179], v[200:203]\n"
"s_add_u32 m0, s68, 36864\n"
"ds_read_b128 a[24:27], v2 offset:4096\n"
"ds_read_b128 a[28:31], v2 offset:4352\n"
"v_mfma_f32_16x16x32_bf16 v[200:203], a[228:231], a[180:183], v[200:203]\n"
"buffer_load_dwordx4 v9, s[28:31], s77 offen nt lds\n"
"ds_read_b128 v[112:115], v47\n"
"v_mfma_f32_16x16x32_bf16 v[204:207], a[232:235], a[176:179], v[204:207]\n"
"ds_read_b128 v[116:119], v47 offset:1024\n"
"v_mul_f32 v151, s74, v151\n"
"v_mfma_f32_16x16x32_bf16 v[204:207], a[236:239], a[180:183], v[204:207]\n"
"ds_read_b64_tr_b16 a[64:65], v5 offset:0\n"
"ds_read_b64_tr_b16 a[66:67], v5 offset:4096\n"
"v_mfma_f32_16x16x32_bf16 v[208:211], a[240:243], a[176:179], v[208:211]\n"
"ds_read_b64_tr_b16 a[68:69], v6 offset:0\n"
"ds_read_b64_tr_b16 a[70:71], v6 offset:4096\n"
"v_mfma_f32_16x16x32_bf16 v[208:211], a[244:247], a[180:183], v[208:211]\n"
"ds_read_b64_tr_b16 a[72:73], v5 offset:256\n"
"v_cvt_pk_bf16_f32 v70, v148, v149\n"
"v_mfma_f32_16x16x32_bf16 v[212:215], a[248:251], a[176:179], v[212:215]\n"
"ds_read_b64_tr_b16 a[74:75], v5 offset:4352\n"
"ds_read_b64_tr_b16 a[76:77], v6 offset:256\n"
"v_cvt_pk_bf16_f32 v71, v150, v151\n"
"v_mfma_f32_16x16x32_bf16 v[212:215], a[252:255], a[180:183], v[212:215]\n"
"s_waitcnt lgkmcnt(9)\n"
"ds_read_b64_tr_b16 a[78:79], v6 offset:4352\n"
"v_mul_f32 v152, s74, v152\n"
"v_mfma_f32_16x16x32_bf16 v[40:43], a[0:3], v[56:59], 0\n"
"ds_read_b64_tr_b16 a[80:81], v248 offset:0\n"
"ds_read_b64_tr_b16 a[82:83], v248 offset:4096\n"
"v_mfma_f32_16x16x32_bf16 v[40:43], a[4:7], v[60:63], v[40:43]\n"
"s_add_u32 s76, s71, 8192\n"
"ds_read_b64_tr_b16 a[84:85], v249 offset:0\n"
"v_mul_f32 v153, s74, v153\n"
"v_mfma_f32_16x16x32_bf16 v[40:43], a[8:11], v[64:67], v[40:43]\n"
"s_add_u32 m0, s68, 40960\n"
"ds_read_b64_tr_b16 a[86:87], v249 offset:4096\n"
"ds_read_b64_tr_b16 a[88:89], v248 offset:256\n"
"v_mfma_f32_16x16x32_bf16 v[40:43], a[12:15], v[68:71], v[40:43]\n"
"buffer_load_dwordx4 v9, s[28:31], s76 offen nt lds\n"
"v_mul_f32 v154, s74, v154\n"
"v_mfma_f32_16x16x32_bf16 v[44:47], a[16:19], v[56:59], 0\n"
"ds_read_b64_tr_b16 a[90:91], v248 offset:4352\n"
"ds_read_b64_tr_b16 a[92:93], v249 offset:256\n"
"v_mfma_f32_16x16x32_bf16 v[44:47], a[20:23], v[60:63], v[44:47]\n"
"ds_read_b64_tr_b16 a[94:95], v249 offset:4352\n"
"v_mul_f32 v155, s74, v155\n"
"v_mfma_f32_16x16x32_bf16 v[44:47], a[24:27], v[64:67], v[44:47]\n"
"v_cvt_pk_bf16_f32 v72, v152, v153\n"
"v_cvt_pk_bf16_f32 v73, v154, v155\n"
"v_mfma_f32_16x16x32_bf16 v[44:47], a[28:31], v[68:71], v[44:47]\n"
"v_mul_f32 v156, s74, v156\n"
"v_mul_f32 v157, s74, v157\n"
"v_mul_f32 v158, s74, v158\n"
"v_mul_f32 v159, s74, v159\n"
"v_cvt_pk_bf16_f32 v74, v156, v157\n"
"v_cvt_pk_bf16_f32 v75, v158, v159\n"
"v_mul_f32 v160, s74, v160\n"
"v_mul_f32 v161, s74, v161\n"
"v_mul_f32 v162, s74, v162\n"
"v_mul_f32 v163, s74, v163\n"
"v_cvt_pk_bf16_f32 v76, v160, v161\n"
"v_cvt_pk_bf16_f32 v77, v162, v163\n"
"v_mul_f32 v164, s74, v164\n"
"v_mul_f32 v165, s74, v165\n"
"v_mul_f32 v166, s74, v166\n"
"v_mul_f32 v167, s74, v167\n"
"v_cvt_pk_bf16_f32 v78, v164, v165\n"
"v_cvt_pk_bf16_f32 v79, v166, v167\n"
"v_mul_f32 v168, s74, v168\n"
"v_mul_f32 v169, s74, v169\n"
"v_mul_f32 v170, s74, v170\n"
"v_mul_f32 v171, s74, v171\n"
"v_cvt_pk_bf16_f32 v80, v168, v169\n"
"v_cvt_pk_bf16_f32 v81, v170, v171\n"
"v_mul_f32 v172, s74, v172\n"
"v_mul_f32 v173, s74, v173\n"
"v_mul_f32 v174, s74, v174\n"
"v_mul_f32 v175, s74, v175\n"
"v_cvt_pk_bf16_f32 v82, v172, v173\n"
"v_cvt_pk_bf16_f32 v83, v174, v175\n"
"v_mul_f32 v176, s74, v176\n"
"v_mul_f32 v177, s74, v177\n"
"v_mul_f32 v178, s74, v178\n"
"v_mul_f32 v179, s74, v179\n"
"v_cvt_pk_bf16_f32 v84, v176, v177\n"
"v_cvt_pk_bf16_f32 v85, v178, v179\n"
"v_mul_f32 v180, s74, v180\n"
"v_mul_f32 v181, s74, v181\n"
"v_mul_f32 v182, s74, v182\n"
"v_mul_f32 v183, s74, v183\n"
"v_cvt_pk_bf16_f32 v86, v180, v181\n"
"v_cvt_pk_bf16_f32 v87, v182, v183\n"
"v_mul_f32 v184, s74, v184\n"
"v_mul_f32 v185, s74, v185\n"
"v_mul_f32 v186, s74, v186\n"
"v_mul_f32 v187, s74, v187\n"
"v_cvt_pk_bf16_f32 v88, v184, v185\n"
"v_cvt_pk_bf16_f32 v89, v186, v187\n"
"v_mul_f32 v188, s74, v188\n"
"v_mul_f32 v189, s74, v189\n"
"v_mul_f32 v190, s74, v190\n"
"v_mul_f32 v191, s74, v191\n"
"v_cvt_pk_bf16_f32 v90, v188, v189\n"
"v_cvt_pk_bf16_f32 v91, v190, v191\n"
"v_mul_f32 v192, s74, v192\n"
"v_mul_f32 v193, s74, v193\n"
"v_mul_f32 v194, s74, v194\n"
"v_mul_f32 v195, s74, v195\n"
"v_cvt_pk_bf16_f32 v92, v192, v193\n"
"v_cvt_pk_bf16_f32 v93, v194, v195\n"
"v_mul_f32 v196, s74, v196\n"
"v_mul_f32 v197, s74, v197\n"
"v_mul_f32 v198, s74, v198\n"
"v_mul_f32 v199, s74, v199\n"
"v_cvt_pk_bf16_f32 v94, v196, v197\n"
"v_cvt_pk_bf16_f32 v95, v198, v199\n"
"v_mul_f32 v200, s74, v200\n"
"v_mul_f32 v201, s74, v201\n"
"v_mul_f32 v202, s74, v202\n"
"v_mul_f32 v203, s74, v203\n"
"v_cvt_pk_bf16_f32 v96, v200, v201\n"
"v_cvt_pk_bf16_f32 v97, v202, v203\n"
"v_mul_f32 v204, s74, v204\n"
"v_mul_f32 v205, s74, v205\n"
"v_mul_f32 v206, s74, v206\n"
"v_mul_f32 v207, s74, v207\n"
"v_cvt_pk_bf16_f32 v98, v204, v205\n"
"v_cvt_pk_bf16_f32 v99, v206, v207\n"
"v_mul_f32 v208, s74, v208\n"
"v_mul_f32 v209, s74, v209\n"
"v_mul_f32 v210, s74, v210\n"
"v_mul_f32 v211, s74, v211\n"
"v_cvt_pk_bf16_f32 v100, v208, v209\n"
"v_cvt_pk_bf16_f32 v101, v210, v211\n"
"v_mul_f32 v212, s74, v212\n"
"v_mul_f32 v213, s74, v213\n"
"v_mul_f32 v214, s74, v214\n"
"v_mul_f32 v215, s74, v215\n"
"v_cvt_pk_bf16_f32 v102, v212, v213\n"
"v_cvt_pk_bf16_f32 v103, v214, v215\n"
"v_exp_f32 v40, v40\n"
"v_exp_f32 v41, v41\n"
"v_exp_f32 v42, v42\n"
"v_exp_f32 v43, v43\n"
"v_exp_f32 v44, v44\n"
"v_exp_f32 v45, v45\n"
"v_cvt_pk_bf16_f32 v32, v40, v41\n"
"v_cvt_pk_bf16_f32 v33, v42, v43\n"
"v_mfma_f32_16x16x32_bf16 v[48:51], a[0:3], v[72:75], 0\n"
"ds_read_b128 a[32:35], v3 offset:0\n"
"v_exp_f32 v46, v46\n"
"v_mfma_f32_16x16x32_bf16 v[48:51], a[4:7], v[76:79], v[48:51]\n"
"v_exp_f32 v47, v47\n"
"v_mfma_f32_16x16x32_bf16 v[48:51], a[8:11], v[80:83], v[48:51]\n"
"ds_read_b128 a[36:39], v3 offset:256\n"
"v_cvt_pk_bf16_f32 v34, v44, v45\n"
"v_mfma_f32_16x16x32_bf16 v[48:51], a[12:15], v[84:87], v[48:51]\n"
"s_add_u32 s77, s71, 12288\n"
"ds_read_b128 a[40:43], v4 offset:0\n"
"v_cvt_pk_bf16_f32 v35, v46, v47\n"
"v_mfma_f32_16x16x32_bf16 v[52:55], a[16:19], v[72:75], 0\n"
"s_add_u32 m0, s68, 45056\n"
"ds_read_b128 a[44:47], v4 offset:256\n"
"ds_read_b128 a[48:51], v3 offset:4096\n"
"v_mfma_f32_16x16x32_bf16 v[52:55], a[20:23], v[76:79], v[52:55]\n"
"buffer_load_dwordx4 v9, s[28:31], s77 offen nt lds\n"
"ds_read_b128 a[52:55], v3 offset:4352\n"
"v_mfma_f32_16x16x32_bf16 v[52:55], a[24:27], v[80:83], v[52:55]\n"
"ds_read_b128 a[56:59], v4 offset:4096\n"
"ds_read_b128 a[60:63], v4 offset:4352\n"
"v_mfma_f32_16x16x32_bf16 v[52:55], a[28:31], v[84:87], v[52:55]\n"
"s_waitcnt lgkmcnt(8)\n"
"ds_read_b64_tr_b16 a[96:97], v7 offset:0\n"
"v_exp_f32 v48, v48\n"
"v_mfma_f32_16x16x32_bf16 v[120:123], a[64:67], v[32:35], 0\n"
"v_exp_f32 v49, v49\n"
"v_mfma_f32_16x16x32_bf16 v[124:127], a[68:71], v[32:35], 0\n"
"v_exp_f32 v50, v50\n"
"v_mfma_f32_16x16x32_bf16 v[128:131], a[72:75], v[32:35], 0\n"
"ds_read_b64_tr_b16 a[98:99], v7 offset:4096\n"
"v_exp_f32 v51, v51\n"
"v_mfma_f32_16x16x32_bf16 v[132:135], a[76:79], v[32:35], 0\n"
"v_cvt_pk_bf16_f32 v36, v48, v49\n"
"v_add_f32 v16, v16, v48\n"
"v_mfma_f32_16x16x32_bf16 v[136:139], a[80:83], v[32:35], 0\n"
"v_exp_f32 v52, v52\n"
"v_mfma_f32_16x16x32_bf16 v[140:143], a[84:87], v[32:35], 0\n"
"ds_read_b64_tr_b16 a[100:101], v8 offset:0\n"
"v_exp_f32 v53, v53\n"
"v_mfma_f32_16x16x32_bf16 v[144:147], a[88:91], v[32:35], 0\n"
"s_add_u32 s76, s71, 0\n"
"v_cvt_pk_bf16_f32 v37, v50, v51\n"
"v_add_f32 v16, v16, v49\n"
"v_mfma_f32_16x16x32_bf16 v[148:151], a[92:95], v[32:35], 0\n"
"s_add_u32 m0, s68, 98304\n"
"v_add_f32 v16, v16, v50\n"
"v_add_f32 v16, v16, v51\n"
"v_mfma_f32_16x16x32_bf16 v[24:27], v[20:23], v[32:35], 0\n"
"buffer_load_dwordx4 v10, s[88:91], s76 offen nt lds\n"
"ds_read_b64_tr_b16 a[102:103], v8 offset:4096\n"
"v_mfma_f32_16x16x32_bf16 v[40:43], a[0:3], v[88:91], 0\n"
"v_exp_f32 v54, v54\n"
"v_mfma_f32_16x16x32_bf16 v[40:43], a[4:7], v[92:95], v[40:43]\n"
"v_exp_f32 v55, v55\n"
"v_mfma_f32_16x16x32_bf16 v[40:43], a[8:11], v[96:99], v[40:43]\n"
"v_cvt_pk_bf16_f32 v38, v52, v53\n"
"v_add_f32 v16, v16, v52\n"
"v_mfma_f32_16x16x32_bf16 v[40:43], a[12:15], v[100:103], v[40:43]\n"
"v_cvt_pk_bf16_f32 v39, v54, v55\n"
"v_add_f32 v16, v16, v53\n"
"v_mfma_f32_16x16x32_bf16 v[44:47], a[16:19], v[88:91], 0\n"
"ds_read_b64_tr_b16 a[104:105], v7 offset:256\n"
"v_add_f32 v16, v16, v54\n"
"v_mfma_f32_16x16x32_bf16 v[44:47], a[20:23], v[92:95], v[44:47]\n"
"ds_read_b64_tr_b16 a[106:107], v7 offset:4352\n"
"v_add_f32 v16, v16, v55\n"
"v_mfma_f32_16x16x32_bf16 v[44:47], a[24:27], v[96:99], v[44:47]\n"
"s_add_u32 s77, s71, 4096\n"
"ds_read_b64_tr_b16 a[108:109], v8 offset:256\n"
"ds_read_b64_tr_b16 a[110:111], v8 offset:4352\n"
"v_mfma_f32_16x16x32_bf16 v[44:47], a[28:31], v[100:103], v[44:47]\n"
"s_add_u32 m0, s68, 102400\n"
"ds_read_b64_tr_b16 a[112:113], v250 offset:0\n"
"v_exp_f32 v40, v40\n"
"v_mfma_f32_16x16x32_bf16 v[152:155], a[64:67], v[36:39], 0\n"
"buffer_load_dwordx4 v10, s[88:91], s77 offen nt lds\n"
"v_exp_f32 v41, v41\n"
"v_mfma_f32_16x16x32_bf16 v[156:159], a[68:71], v[36:39], 0\n"
"v_exp_f32 v42, v42\n"
"v_mfma_f32_16x16x32_bf16 v[160:163], a[72:75], v[36:39], 0\n"
"v_exp_f32 v43, v43\n"
"v_mfma_f32_16x16x32_bf16 v[164:167], a[76:79], v[36:39], 0\n"
"v_cvt_pk_bf16_f32 v32, v40, v41\n"
"v_add_f32 v17, v17, v40\n"
"v_mfma_f32_16x16x32_bf16 v[168:171], a[80:83], v[36:39], 0\n"
"ds_read_b64_tr_b16 a[114:115], v250 offset:4096\n"
"v_exp_f32 v44, v44\n"
"v_mfma_f32_16x16x32_bf16 v[172:175], a[84:87], v[36:39], 0\n"
"v_exp_f32 v45, v45\n"
"v_mfma_f32_16x16x32_bf16 v[176:179], a[88:91], v[36:39], 0\n"
"v_cvt_pk_bf16_f32 v33, v42, v43\n"
"v_add_f32 v17, v17, v41\n"
"v_mfma_f32_16x16x32_bf16 v[180:183], a[92:95], v[36:39], 0\n"
"v_add_f32 v17, v17, v42\n"
"v_add_f32 v17, v17, v43\n"
"v_mfma_f32_16x16x32_bf16 v[48:51], a[0:3], v[104:107], 0\n"
"ds_read_b64_tr_b16 a[116:117], v251 offset:0\n"
"v_exp_f32 v46, v46\n"
"v_mfma_f32_16x16x32_bf16 v[48:51], a[4:7], v[108:111], v[48:51]\n"
"v_exp_f32 v47, v47\n"
"v_mfma_f32_16x16x32_bf16 v[48:51], a[8:11], v[112:115], v[48:51]\n"
"s_add_u32 s76, s71, 8192\n"
"v_cvt_pk_bf16_f32 v34, v44, v45\n"
"v_add_f32 v17, v17, v44\n"
"v_mfma_f32_16x16x32_bf16 v[48:51], a[12:15], v[116:119], v[48:51]\n"
"s_add_u32 m0, s68, 106496\n"
"v_cvt_pk_bf16_f32 v35, v46, v47\n"
"v_add_f32 v17, v17, v45\n"
"v_mfma_f32_16x16x32_bf16 v[52:55], a[16:19], v[104:107], 0\n"
"buffer_load_dwordx4 v10, s[88:91], s76 offen nt lds\n"
"v_add_f32 v17, v17, v46\n"
"v_mfma_f32_16x16x32_bf16 v[52:55], a[20:23], v[108:111], v[52:55]\n"
"ds_read_b64_tr_b16 a[118:119], v251 offset:4096\n"
"v_add_f32 v17, v17, v47\n"
"v_mfma_f32_16x16x32_bf16 v[52:55], a[24:27], v[112:115], v[52:55]\n"
"ds_read_b64_tr_b16 a[120:121], v250 offset:256\n"
"ds_read_b64_tr_b16 a[122:123], v250 offset:4352\n"
"v_mfma_f32_16x16x32_bf16 v[52:55], a[28:31], v[116:119], v[52:55]\n"
"v_exp_f32 v48, v48\n"
"v_mfma_f32_16x16x32_bf16 v[184:187], a[64:67], v[32:35], 0\n"
"ds_read_b64_tr_b16 a[124:125], v251 offset:256\n"
"v_exp_f32 v49, v49\n"
"v_mfma_f32_16x16x32_bf16 v[188:191], a[68:71], v[32:35], 0\n"
"v_exp_f32 v50, v50\n"
"v_mfma_f32_16x16x32_bf16 v[192:195], a[72:75], v[32:35], 0\n"
"v_exp_f32 v51, v51\n"
"v_mfma_f32_16x16x32_bf16 v[196:199], a[76:79], v[32:35], 0\n"
"ds_read_b64_tr_b16 a[126:127], v251 offset:4352\n"
"v_cvt_pk_bf16_f32 v36, v48, v49\n"
"v_mfma_f32_16x16x32_bf16 v[200:203], a[80:83], v[32:35], 0\n"
"s_add_u32 s77, s71, 12288\n"
"v_exp_f32 v52, v52\n"
"v_mfma_f32_16x16x32_bf16 v[204:207], a[84:87], v[32:35], 0\n"
"s_add_u32 m0, s68, 110592\n"
"v_exp_f32 v53, v53\n"
"v_mfma_f32_16x16x32_bf16 v[208:211], a[88:91], v[32:35], 0\n"
"s_add_u32 s61, s75, 3\n"
"buffer_load_dwordx4 v10, s[88:91], s77 offen nt lds\n"
"v_cvt_pk_bf16_f32 v37, v50, v51\n"
"v_mfma_f32_16x16x32_bf16 v[212:215], a[92:95], v[32:35], 0\n"
"s_waitcnt vmcnt(8) lgkmcnt(0)\n"
"s_barrier\n"
"v_mfma_f32_16x16x32_bf16 v[40:43], a[32:35], v[56:59], 0\n"
"s_lshr_b32 s60, s12, 7\n"
"v_exp_f32 v54, v54\n"
"v_mfma_f32_16x16x32_bf16 v[40:43], a[36:39], v[60:63], v[40:43]\n"
"s_sub_u32 s60, 3, s60\n"
"v_exp_f32 v55, v55\n"
"v_mfma_f32_16x16x32_bf16 v[40:43], a[40:43], v[64:67], v[40:43]\n"
"s_and_b32 s60, s60, 7\n"
"ds_read_b128 a[0:3], v1 offset:16384\n"
"v_cvt_pk_bf16_f32 v38, v52, v53\n"
"v_mfma_f32_16x16x32_bf16 v[40:43], a[44:47], v[68:71], v[40:43]\n"
"s_lshl_b32 s60, s60, 7\n"
"ds_read_b128 a[4:7], v1 offset:16640\n"
"v_cvt_pk_bf16_f32 v39, v54, v55\n"
"v_mfma_f32_16x16x32_bf16 v[44:47], a[48:51], v[56:59], 0\n"
"s_lshr_b32 s63, %[bid], 3\n"
"ds_read_b128 a[8:11], v2 offset:16384\n"
"ds_read_b128 a[12:15], v2 offset:16640\n"
"v_mfma_f32_16x16x32_bf16 v[44:47], a[52:55], v[60:63], v[44:47]\n"
"s_lshl2_add_u32 s63, s63, s52\n"
"ds_read_b128 a[16:19], v1 offset:20480\n"
"ds_read_b128 a[20:23], v1 offset:20736\n"
"v_mfma_f32_16x16x32_bf16 v[44:47], a[56:59], v[64:67], v[44:47]\n"
"s_lshl_b32 s63, s63, 12\n"
"ds_read_b128 a[24:27], v2 offset:20480\n"
"ds_read_b128 a[28:31], v2 offset:20736\n"
"v_mfma_f32_16x16x32_bf16 v[44:47], a[60:63], v[68:71], v[44:47]\n"
"s_add_u32 s60, s60, s63\n"
"s_and_b32 s61, s61, 15\n"
"s_lshl_b32 s61, s61, 14\n"
"s_add_u32 s71, s68, s61\n"
"s_add_u32 s76, s71, 0\n"
"v_exp_f32 v40, v40\n"
"v_mfma_f32_16x16x32_bf16 v[232:235], a[80:83], v[36:39], 0\n"
"s_add_u32 m0, s68, 49152\n"
"v_exp_f32 v41, v41\n"
"v_mfma_f32_16x16x32_bf16 v[236:239], a[84:87], v[36:39], 0\n"
"buffer_load_dwordx4 v9, s[28:31], s76 offen nt lds\n"
"v_exp_f32 v42, v42\n"
"v_mfma_f32_16x16x32_bf16 v[240:243], a[88:91], v[36:39], 0\n"
"v_exp_f32 v43, v43\n"
"v_mfma_f32_16x16x32_bf16 v[244:247], a[92:95], v[36:39], 0\n"
"v_cvt_pk_bf16_f32 v32, v40, v41\n"
"v_lshl_add_u32 v254, v0, 10, s60\n"
"v_mfma_f32_16x16x32_bf16 v[216:219], a[64:67], v[36:39], 0\n"
"v_exp_f32 v44, v44\n"
"v_mfma_f32_16x16x32_bf16 v[220:223], a[68:71], v[36:39], 0\n"
"v_exp_f32 v45, v45\n"
"v_mfma_f32_16x16x32_bf16 v[224:227], a[72:75], v[36:39], 0\n"
"s_add_u32 s77, s71, 4096\n"
"v_cvt_pk_bf16_f32 v33, v42, v43\n"
"v_cmp_gt_u32 vcc, 4, v0\n"
"v_mfma_f32_16x16x32_bf16 v[228:231], a[76:79], v[36:39], 0\n"
"s_add_u32 m0, s68, 53248\n"
"v_mov_b32 v255, 0x7ff00000\n"
"v_mfma_f32_16x16x32_bf16 v[28:31], v[20:23], v[36:39], 0\n"
"buffer_load_dwordx4 v9, s[28:31], s77 offen nt lds\n"
"ds_read_b64_tr_b16 a[64:65], v5 offset:16384\n"
"v_mfma_f32_16x16x32_bf16 v[48:51], a[32:35], v[72:75], 0\n"
"v_exp_f32 v46, v46\n"
"v_mfma_f32_16x16x32_bf16 v[48:51], a[36:39], v[76:79], v[48:51]\n"
"v_exp_f32 v47, v47\n"
"v_cndmask_b32 v254, v255, v254, vcc\n"
"v_mfma_f32_16x16x32_bf16 v[48:51], a[40:43], v[80:83], v[48:51]\n"
"ds_read_b64_tr_b16 a[66:67], v5 offset:20480\n"
"v_cvt_pk_bf16_f32 v34, v44, v45\n"
"v_mfma_f32_16x16x32_bf16 v[48:51], a[44:47], v[84:87], v[48:51]\n"
"ds_read_b64_tr_b16 a[68:69], v6 offset:16384\n"
"v_cvt_pk_bf16_f32 v35, v46, v47\n"
"v_mfma_f32_16x16x32_bf16 v[52:55], a[48:51], v[72:75], 0\n"
"ds_read_b64_tr_b16 a[70:71], v6 offset:20480\n"
"ds_read_b64_tr_b16 a[72:73], v5 offset:16640\n"
"v_mfma_f32_16x16x32_bf16 v[52:55], a[52:55], v[76:79], v[52:55]\n"
"ds_read_b64_tr_b16 a[74:75], v5 offset:20736\n"
"ds_read_b64_tr_b16 a[76:77], v6 offset:16640\n"
"v_mfma_f32_16x16x32_bf16 v[52:55], a[56:59], v[80:83], v[52:55]\n"
"ds_read_b64_tr_b16 a[78:79], v6 offset:20736\n"
"v_mfma_f32_16x16x32_bf16 v[52:55], a[60:63], v[84:87], v[52:55]\n"
"s_add_u32 s76, s71, 8192\n"
"v_exp_f32 v48, v48\n"
"v_mfma_f32_16x16x32_bf16 v[120:123], a[96:99], v[32:35], v[120:123]\n"
"s_add_u32 m0, s68, 57344\n"
"v_exp_f32 v49, v49\n"
"v_mfma_f32_16x16x32_bf16 v[124:127], a[100:103], v[32:35], v[124:127]\n"
"buffer_load_dwordx4 v9, s[28:31], s76 offen nt lds\n"
"v_exp_f32 v50, v50\n"
"v_mfma_f32_16x16x32_bf16 v[128:131], a[104:107], v[32:35], v[128:131]\n"
"v_exp_f32 v51, v51\n"
"v_mfma_f32_16x16x32_bf16 v[132:135], a[108:111], v[32:35], v[132:135]\n"
"v_cvt_pk_bf16_f32 v36, v48, v49\n"
"v_add_f32 v16, v16, v48\n"
"v_mfma_f32_16x16x32_bf16 v[136:139], a[112:115], v[32:35], v[136:139]\n"
"v_exp_f32 v52, v52\n"
"v_mfma_f32_16x16x32_bf16 v[140:143], a[116:119], v[32:35], v[140:143]\n"
"v_exp_f32 v53, v53\n"
"v_mfma_f32_16x16x32_bf16 v[144:147], a[120:123], v[32:35], v[144:147]\n"
"v_cvt_pk_bf16_f32 v37, v50, v51\n"
"v_add_f32 v16, v16, v49\n"
"v_mfma_f32_16x16x32_bf16 v[148:151], a[124:127], v[32:35], v[148:151]\n"
"v_add_f32 v16, v16, v50\n"
"v_add_f32 v16, v16, v51\n"
"v_mfma_f32_16x16x32_bf16 v[24:27], v[20:23], v[32:35], v[24:27]\n"
"ds_read_b64_tr_b16 a[80:81], v248 offset:16384\n"
"ds_read_b64_tr_b16 a[82:83], v248 offset:20480\n"
"v_mfma_f32_16x16x32_bf16 v[40:43], a[32:35], v[88:91], 0\n"
"v_exp_f32 v54, v54\n"
"v_mfma_f32_16x16x32_bf16 v[40:43], a[36:39], v[92:95], v[40:43]\n"
"v_exp_f32 v55, v55\n"
"v_mfma_f32_16x16x32_bf16 v[40:43], a[40:43], v[96:99], v[40:43]\n"
"s_add_u32 s77, s71, 12288\n"
"v_cvt_pk_bf16_f32 v38, v52, v53\n"
"v_add_f32 v16, v16, v52\n"
"v_mfma_f32_16x16x32_bf16 v[40:43], a[44:47], v[100:103], v[40:43]\n"
"s_add_u32 m0, s68, 61440\n"
"v_cvt_pk_bf16_f32 v39, v54, v55\n"
"v_add_f32 v16, v16, v53\n"
"v_mfma_f32_16x16x32_bf16 v[44:47], a[48:51], v[88:91], 0\n"
"buffer_load_dwordx4 v9, s[28:31], s77 offen nt lds\n"
"v_add_f32 v16, v16, v54\n"
"v_mfma_f32_16x16x32_bf16 v[44:47], a[52:55], v[92:95], v[44:47]\n"
"ds_read_b64_tr_b16 a[84:85], v249 offset:16384\n"
"v_add_f32 v16, v16, v55\n"
"v_mfma_f32_16x16x32_bf16 v[44:47], a[56:59], v[96:99], v[44:47]\n"
"ds_read_b64_tr_b16 a[86:87], v249 offset:20480\n"
"ds_read_b64_tr_b16 a[88:89], v248 offset:16640\n"
"v_mfma_f32_16x16x32_bf16 v[44:47], a[60:63], v[100:103], v[44:47]\n"
"v_exp_f32 v40, v40\n"
"v_mfma_f32_16x16x32_bf16 v[152:155], a[96:99], v[36:39], v[152:155]\n"
"v_exp_f32 v41, v41\n"
"v_mfma_f32_16x16x32_bf16 v[156:159], a[100:103], v[36:39], v[156:159]\n"
"v_exp_f32 v42, v42\n"
"v_mfma_f32_16x16x32_bf16 v[160:163], a[104:107], v[36:39], v[160:163]\n"
"s_add_u32 s76, s71, 0\n"
"v_exp_f32 v43, v43\n"
"v_mfma_f32_16x16x32_bf16 v[164:167], a[108:111], v[36:39], v[164:167]\n"
"s_add_u32 m0, s68, 114688\n"
"v_cvt_pk_bf16_f32 v32, v40, v41\n"
"v_add_f32 v17, v17, v40\n"
"v_mfma_f32_16x16x32_bf16 v[168:171], a[112:115], v[36:39], v[168:171]\n"
"buffer_load_dwordx4 v10, s[88:91], s76 offen nt lds\n"
"v_exp_f32 v44, v44\n"
"v_mfma_f32_16x16x32_bf16 v[172:175], a[116:119], v[36:39], v[172:175]\n"
"v_exp_f32 v45, v45\n"
"v_mfma_f32_16x16x32_bf16 v[176:179], a[120:123], v[36:39], v[176:179]\n"
"v_cvt_pk_bf16_f32 v33, v42, v43\n"
"v_add_f32 v17, v17, v41\n"
"v_mfma_f32_16x16x32_bf16 v[180:183], a[124:127], v[36:39], v[180:183]\n"
"s_waitcnt lgkmcnt(13)\n"
"v_add_f32 v17, v17, v42\n"
"v_add_f32 v17, v17, v43\n"
"v_mfma_f32_16x16x32_bf16 v[48:51], a[0:3], v[56:59], 0\n"
"v_exp_f32 v46, v46\n"
"v_mfma_f32_16x16x32_bf16 v[48:51], a[4:7], v[60:63], v[48:51]\n"
"v_exp_f32 v47, v47\n"
"v_mfma_f32_16x16x32_bf16 v[48:51], a[8:11], v[64:67], v[48:51]\n"
"v_cvt_pk_bf16_f32 v34, v44, v45\n"
"v_add_f32 v17, v17, v44\n"
"v_mfma_f32_16x16x32_bf16 v[48:51], a[12:15], v[68:71], v[48:51]\n"
"v_cvt_pk_bf16_f32 v35, v46, v47\n"
"v_add_f32 v17, v17, v45\n"
"v_mfma_f32_16x16x32_bf16 v[52:55], a[16:19], v[56:59], 0\n"
"s_add_u32 s77, s71, 4096\n"
"ds_read_b64_tr_b16 a[90:91], v248 offset:20736\n"
"v_add_f32 v17, v17, v46\n"
"v_mfma_f32_16x16x32_bf16 v[52:55], a[20:23], v[60:63], v[52:55]\n"
"s_add_u32 m0, s68, 118784\n"
"ds_read_b64_tr_b16 a[92:93], v249 offset:16640\n"
"v_add_f32 v17, v17, v47\n"
"v_mfma_f32_16x16x32_bf16 v[52:55], a[24:27], v[64:67], v[52:55]\n"
"buffer_load_dwordx4 v10, s[88:91], s77 offen nt lds\n"
"ds_read_b64_tr_b16 a[94:95], v249 offset:20736\n"
"v_mfma_f32_16x16x32_bf16 v[52:55], a[28:31], v[68:71], v[52:55]\n"
"ds_read_b128 a[32:35], v3 offset:16384\n"
"v_exp_f32 v48, v48\n"
"v_mfma_f32_16x16x32_bf16 v[184:187], a[96:99], v[32:35], v[184:187]\n"
"v_exp_f32 v49, v49\n"
"v_mfma_f32_16x16x32_bf16 v[188:191], a[100:103], v[32:35], v[188:191]\n"
"ds_read_b128 a[36:39], v3 offset:16640\n"
"v_exp_f32 v50, v50\n"
"v_mfma_f32_16x16x32_bf16 v[192:195], a[104:107], v[32:35], v[192:195]\n"
"v_exp_f32 v51, v51\n"
"v_mfma_f32_16x16x32_bf16 v[196:199], a[108:111], v[32:35], v[196:199]\n"
"ds_read_b128 a[40:43], v4 offset:16384\n"
"v_cvt_pk_bf16_f32 v36, v48, v49\n"
"v_mfma_f32_16x16x32_bf16 v[200:203], a[112:115], v[32:35], v[200:203]\n"
"ds_read_b128 a[44:47], v4 offset:16640\n"
"v_exp_f32 v52, v52\n"
"v_mfma_f32_16x16x32_bf16 v[204:207], a[116:119], v[32:35], v[204:207]\n"
"v_exp_f32 v53, v53\n"
"v_mfma_f32_16x16x32_bf16 v[208:211], a[120:123], v[32:35], v[208:211]\n"
"ds_read_b128 a[48:51], v3 offset:20480\n"
"v_cvt_pk_bf16_f32 v37, v50, v51\n"
"v_mfma_f32_16x16x32_bf16 v[212:215], a[124:127], v[32:35], v[212:215]\n"
"ds_read_b128 a[52:55], v3 offset:20736\n"
"ds_read_b128 a[56:59], v4 offset:20480\n"
"v_mfma_f32_16x16x32_bf16 v[40:43], a[0:3], v[72:75], 0\n"
"s_add_u32 s76, s71, 8192\n"
"ds_read_b128 a[60:63], v4 offset:20736\n"
"v_exp_f32 v54, v54\n"
"v_mfma_f32_16x16x32_bf16 v[40:43], a[4:7], v[76:79], v[40:43]\n"
"s_add_u32 m0, s68, 122880\n"
"ds_read_b64_tr_b16 a[96:97], v7 offset:16384\n"
"v_exp_f32 v55, v55\n"
"v_mfma_f32_16x16x32_bf16 v[40:43], a[8:11], v[80:83], v[40:43]\n"
"buffer_load_dwordx4 v10, s[88:91], s76 offen nt lds\n"
"v_cvt_pk_bf16_f32 v38, v52, v53\n"
"v_mfma_f32_16x16x32_bf16 v[40:43], a[12:15], v[84:87], v[40:43]\n"
"ds_read_b64_tr_b16 a[98:99], v7 offset:20480\n"
"v_cvt_pk_bf16_f32 v39, v54, v55\n"
"v_mfma_f32_16x16x32_bf16 v[44:47], a[16:19], v[72:75], 0\n"
"ds_read_b64_tr_b16 a[100:101], v8 offset:16384\n"
"ds_read_b64_tr_b16 a[102:103], v8 offset:20480\n"
"v_mfma_f32_16x16x32_bf16 v[44:47], a[20:23], v[76:79], v[44:47]\n"
"ds_read_b64_tr_b16 a[104:105], v7 offset:16640\n"
"ds_read_b64_tr_b16 a[106:107], v7 offset:20736\n"
"v_mfma_f32_16x16x32_bf16 v[44:47], a[24:27], v[80:83], v[44:47]\n"
"ds_read_b64_tr_b16 a[108:109], v8 offset:16640\n"
"ds_read_b64_tr_b16 a[110:111], v8 offset:20736\n"
"v_mfma_f32_16x16x32_bf16 v[44:47], a[28:31], v[84:87], v[44:47]\n"
"s_waitcnt lgkmcnt(15)\n"
"v_exp_f32 v40, v40\n"
"v_mfma_f32_16x16x32_bf16 v[120:123], a[64:67], v[36:39], v[120:123]\n"
"v_exp_f32 v41, v41\n"
"v_mfma_f32_16x16x32_bf16 v[124:127], a[68:71], v[36:39], v[124:127]\n"
"v_exp_f32 v42, v42\n"
"v_mfma_f32_16x16x32_bf16 v[128:131], a[72:75], v[36:39], v[128:131]\n"
"v_exp_f32 v43, v43\n"
"v_mfma_f32_16x16x32_bf16 v[132:135], a[76:79], v[36:39], v[132:135]\n"
"v_cvt_pk_bf16_f32 v32, v40, v41\n"
"v_add_f32 v16, v16, v40\n"
"v_mfma_f32_16x16x32_bf16 v[136:139], a[80:83], v[36:39], v[136:139]\n"
"v_exp_f32 v44, v44\n"
"v_mfma_f32_16x16x32_bf16 v[140:143], a[84:87], v[36:39], v[140:143]\n"
"v_exp_f32 v45, v45\n"
"v_mfma_f32_16x16x32_bf16 v[144:147], a[88:91], v[36:39], v[144:147]\n"
"s_add_u32 s77, s71, 12288\n"
"v_cvt_pk_bf16_f32 v33, v42, v43\n"
"v_add_f32 v16, v16, v41\n"
"v_mfma_f32_16x16x32_bf16 v[148:151], a[92:95], v[36:39], v[148:151]\n"
"s_add_u32 m0, s68, 126976\n"
"v_add_f32 v16, v16, v42\n"
"v_add_f32 v16, v16, v43\n"
"v_mfma_f32_16x16x32_bf16 v[24:27], v[20:23], v[36:39], v[24:27]\n"
"s_add_u32 s61, s75, 4\n"
"buffer_load_dwordx4 v10, s[88:91], s77 offen nt lds\n"
"ds_read_b64_tr_b16 a[112:113], v250 offset:16384\n"
"v_mfma_f32_16x16x32_bf16 v[48:51], a[0:3], v[88:91], 0\n"
"s_and_b32 s61, s61, 15\n"
"v_exp_f32 v46, v46\n"
"v_mfma_f32_16x16x32_bf16 v[48:51], a[4:7], v[92:95], v[48:51]\n"
"s_lshl_b32 s61, s61, 14\n"
"v_exp_f32 v47, v47\n"
"v_mfma_f32_16x16x32_bf16 v[48:51], a[8:11], v[96:99], v[48:51]\n"
"s_add_u32 s71, s68, s61\n"
"s_add_u32 s76, s71, 0\n"
"v_cvt_pk_bf16_f32 v34, v44, v45\n"
"v_add_f32 v16, v16, v44\n"
"v_mfma_f32_16x16x32_bf16 v[48:51], a[12:15], v[100:103], v[48:51]\n"
"s_add_u32 m0, s68, 0\n"
"v_cvt_pk_bf16_f32 v35, v46, v47\n"
"v_add_f32 v16, v16, v45\n"
"v_mfma_f32_16x16x32_bf16 v[52:55], a[16:19], v[88:91], 0\n"
"buffer_load_dwordx4 v9, s[28:31], s76 offen nt lds\n"
"v_add_f32 v16, v16, v46\n"
"v_mfma_f32_16x16x32_bf16 v[52:55], a[20:23], v[92:95], v[52:55]\n"
"ds_read_b64_tr_b16 a[114:115], v250 offset:20480\n"
"v_add_f32 v16, v16, v47\n"
"v_mfma_f32_16x16x32_bf16 v[52:55], a[24:27], v[96:99], v[52:55]\n"
"ds_read_b64_tr_b16 a[116:117], v251 offset:16384\n"
"ds_read_b64_tr_b16 a[118:119], v251 offset:20480\n"
"v_mfma_f32_16x16x32_bf16 v[52:55], a[28:31], v[100:103], v[52:55]\n"
"v_exp_f32 v48, v48\n"
"v_mfma_f32_16x16x32_bf16 v[152:155], a[64:67], v[32:35], v[152:155]\n"
"v_exp_f32 v49, v49\n"
"v_mfma_f32_16x16x32_bf16 v[156:159], a[68:71], v[32:35], v[156:159]\n"
"ds_read_b64_tr_b16 a[120:121], v250 offset:16640\n"
"v_exp_f32 v50, v50\n"
"v_mfma_f32_16x16x32_bf16 v[160:163], a[72:75], v[32:35], v[160:163]\n"
"v_exp_f32 v51, v51\n"
"v_mfma_f32_16x16x32_bf16 v[164:167], a[76:79], v[32:35], v[164:167]\n"
"v_cvt_pk_bf16_f32 v36, v48, v49\n"
"v_add_f32 v17, v17, v48\n"
"v_mfma_f32_16x16x32_bf16 v[168:171], a[80:83], v[32:35], v[168:171]\n"
"v_exp_f32 v52, v52\n"
"v_mfma_f32_16x16x32_bf16 v[172:175], a[84:87], v[32:35], v[172:175]\n"
"v_exp_f32 v53, v53\n"
"v_mfma_f32_16x16x32_bf16 v[176:179], a[88:91], v[32:35], v[176:179]\n"
"v_cvt_pk_bf16_f32 v37, v50, v51\n"
"v_add_f32 v17, v17, v49\n"
"v_mfma_f32_16x16x32_bf16 v[180:183], a[92:95], v[32:35], v[180:183]\n"
"v_add_f32 v17, v17, v50\n"
"v_add_f32 v17, v17, v51\n"
"v_mfma_f32_16x16x32_bf16 v[40:43], a[0:3], v[104:107], 0\n"
"v_exp_f32 v54, v54\n"
"v_mfma_f32_16x16x32_bf16 v[40:43], a[4:7], v[108:111], v[40:43]\n"
"v_exp_f32 v55, v55\n"
"v_mfma_f32_16x16x32_bf16 v[40:43], a[8:11], v[112:115], v[40:43]\n"
"s_add_u32 s77, s71, 4096\n"
"v_cvt_pk_bf16_f32 v38, v52, v53\n"
"v_add_f32 v17, v17, v52\n"
"v_mfma_f32_16x16x32_bf16 v[40:43], a[12:15], v[116:119], v[40:43]\n"
"s_add_u32 m0, s68, 4096\n"
"v_cvt_pk_bf16_f32 v39, v54, v55\n"
"v_add_f32 v17, v17, v53\n"
"v_mfma_f32_16x16x32_bf16 v[44:47], a[16:19], v[104:107], 0\n"
"buffer_load_dwordx4 v9, s[28:31], s77 offen nt lds\n"
"v_add_f32 v17, v17, v54\n"
"v_mfma_f32_16x16x32_bf16 v[44:47], a[20:23], v[108:111], v[44:47]\n"
"ds_read_b64_tr_b16 a[122:123], v250 offset:20736\n"
"v_add_f32 v17, v17, v55\n"
"v_mfma_f32_16x16x32_bf16 v[44:47], a[24:27], v[112:115], v[44:47]\n"
"ds_read_b64_tr_b16 a[124:125], v251 offset:16640\n"
"ds_read_b64_tr_b16 a[126:127], v251 offset:20736\n"
"v_mfma_f32_16x16x32_bf16 v[44:47], a[28:31], v[116:119], v[44:47]\n"
"v_exp_f32 v40, v40\n"
"v_mfma_f32_16x16x32_bf16 v[184:187], a[64:67], v[36:39], v[184:187]\n"
"v_exp_f32 v41, v41\n"
"v_mfma_f32_16x16x32_bf16 v[188:191], a[68:71], v[36:39], v[188:191]\n"
"s_add_u32 s76, s71, 8192\n"
"v_exp_f32 v42, v42\n"
"v_mfma_f32_16x16x32_bf16 v[192:195], a[72:75], v[36:39], v[192:195]\n"
"s_add_u32 m0, s68, 8192\n"
"v_exp_f32 v43, v43\n"
"v_mfma_f32_16x16x32_bf16 v[196:199], a[76:79], v[36:39], v[196:199]\n"
"buffer_load_dwordx4 v9, s[28:31], s76 offen nt lds\n"
"v_cvt_pk_bf16_f32 v32, v40, v41\n"
"v_mfma_f32_16x16x32_bf16 v[200:203], a[80:83], v[36:39], v[200:203]\n"
"v_exp_f32 v44, v44\n"
"v_mfma_f32_16x16x32_bf16 v[204:207], a[84:87], v[36:39], v[204:207]\n"
"v_exp_f32 v45, v45\n"
"v_mfma_f32_16x16x32_bf16 v[208:211], a[88:91], v[36:39], v[208:211]\n"
"v_cvt_pk_bf16_f32 v33, v42, v43\n"
"v_mfma_f32_16x16x32_bf16 v[212:215], a[92:95], v[36:39], v[212:215]\n"
"s_waitcnt vmcnt(11) lgkmcnt(0)\n"
"s_barrier\n"
"v_mfma_f32_16x16x32_bf16 v[48:51], a[32:35], v[56:59], 0\n"
"s_add_u32 s77, s71, 12288\n"
"v_exp_f32 v46, v46\n"
"v_mfma_f32_16x16x32_bf16 v[48:51], a[36:39], v[60:63], v[48:51]\n"
"s_add_u32 m0, s68, 12288\n"
"v_exp_f32 v47, v47\n"
"v_mfma_f32_16x16x32_bf16 v[48:51], a[40:43], v[64:67], v[48:51]\n"
"buffer_load_dwordx4 v9, s[28:31], s77 offen nt lds\n"
"v_cvt_pk_bf16_f32 v34, v44, v45\n"
"v_mfma_f32_16x16x32_bf16 v[48:51], a[44:47], v[68:71], v[48:51]\n"
"ds_read_b128 a[0:3], v1 offset:32768\n"
"v_cvt_pk_bf16_f32 v35, v46, v47\n"
"v_mfma_f32_16x16x32_bf16 v[52:55], a[48:51], v[56:59], 0\n"
"ds_read_b128 a[4:7], v1 offset:33024\n"
"ds_read_b128 a[8:11], v2 offset:32768\n"
"v_mfma_f32_16x16x32_bf16 v[52:55], a[52:55], v[60:63], v[52:55]\n"
"ds_read_b128 a[12:15], v2 offset:33024\n"
"v_mfma_f32_16x16x32_bf16 v[52:55], a[56:59], v[64:67], v[52:55]\n"
"ds_read_b128 a[16:19], v1 offset:36864\n"
"ds_read_b128 a[20:23], v1 offset:37120\n"
"v_mfma_f32_16x16x32_bf16 v[52:55], a[60:63], v[68:71], v[52:55]\n"
"v_exp_f32 v48, v48\n"
"v_mfma_f32_16x16x32_bf16 v[232:235], a[80:83], v[32:35], v[232:235]\n"
"v_exp_f32 v49, v49\n"
"v_mfma_f32_16x16x32_bf16 v[236:239], a[84:87], v[32:35], v[236:239]\n"
"v_exp_f32 v50, v50\n"
"v_mfma_f32_16x16x32_bf16 v[240:243], a[88:91], v[32:35], v[240:243]\n"
"v_exp_f32 v51, v51\n"
"v_mfma_f32_16x16x32_bf16 v[244:247], a[92:95], v[32:35], v[244:247]\n"
"ds_read_b128 a[24:27], v2 offset:36864\n"
"v_cvt_pk_bf16_f32 v36, v48, v49\n"
"v_mfma_f32_16x16x32_bf16 v[216:219], a[64:67], v[32:35], v[216:219]\n"
"s_add_u32 s76, s71, 0\n"
"v_exp_f32 v52, v52\n"
"v_mfma_f32_16x16x32_bf16 v[220:223], a[68:71], v[32:35], v[220:223]\n"
"s_add_u32 m0, s68, 65536\n"
"v_exp_f32 v53, v53\n"
"v_mfma_f32_16x16x32_bf16 v[224:227], a[72:75], v[32:35], v[224:227]\n"
"buffer_load_dwordx4 v10, s[88:91], s76 offen nt lds\n"
"v_cvt_pk_bf16_f32 v37, v50, v51\n"
"v_mfma_f32_16x16x32_bf16 v[228:231], a[76:79], v[32:35], v[228:231]\n"
"ds_read_b128 a[28:31], v2 offset:37120\n"
"v_mfma_f32_16x16x32_bf16 v[28:31], v[20:23], v[32:35], v[28:31]\n"
"ds_read_b64_tr_b16 a[64:65], v5 offset:32768\n"
"ds_read_b64_tr_b16 a[66:67], v5 offset:36864\n"
"v_mfma_f32_16x16x32_bf16 v[40:43], a[32:35], v[72:75], 0\n"
"v_exp_f32 v54, v54\n"
"v_mfma_f32_16x16x32_bf16 v[40:43], a[36:39], v[76:79], v[40:43]\n"
"v_exp_f32 v55, v55\n"
"v_mfma_f32_16x16x32_bf16 v[40:43], a[40:43], v[80:83], v[40:43]\n"
"ds_read_b64_tr_b16 a[68:69], v6 offset:32768\n"
"v_cvt_pk_bf16_f32 v38, v52, v53\n"
"v_mfma_f32_16x16x32_bf16 v[40:43], a[44:47], v[84:87], v[40:43]\n"
"s_add_u32 s77, s71, 4096\n"
"ds_read_b64_tr_b16 a[70:71], v6 offset:36864\n"
"v_cvt_pk_bf16_f32 v39, v54, v55\n"
"v_mfma_f32_16x16x32_bf16 v[44:47], a[48:51], v[72:75], 0\n"
"s_add_u32 m0, s68, 69632\n"
"ds_read_b64_tr_b16 a[72:73], v5 offset:33024\n"
"ds_read_b64_tr_b16 a[74:75], v5 offset:37120\n"
"v_mfma_f32_16x16x32_bf16 v[44:47], a[52:55], v[76:79], v[44:47]\n"
"buffer_load_dwordx4 v10, s[88:91], s77 offen nt lds\n"
"ds_read_b64_tr_b16 a[76:77], v6 offset:33024\n"
"v_mfma_f32_16x16x32_bf16 v[44:47], a[56:59], v[80:83], v[44:47]\n"
"ds_read_b64_tr_b16 a[78:79], v6 offset:37120\n"
"v_mfma_f32_16x16x32_bf16 v[44:47], a[60:63], v[84:87], v[44:47]\n"
"v_exp_f32 v40, v40\n"
"v_mfma_f32_16x16x32_bf16 v[120:123], a[96:99], v[36:39], v[120:123]\n"
"v_exp_f32 v41, v41\n"
"v_mfma_f32_16x16x32_bf16 v[124:127], a[100:103], v[36:39], v[124:127]\n"
"v_exp_f32 v42, v42\n"
"v_mfma_f32_16x16x32_bf16 v[128:131], a[104:107], v[36:39], v[128:131]\n"
"v_exp_f32 v43, v43\n"
"v_mfma_f32_16x16x32_bf16 v[132:135], a[108:111], v[36:39], v[132:135]\n"
"v_cvt_pk_bf16_f32 v32, v40, v41\n"
"v_add_f32 v16, v16, v40\n"
"v_mfma_f32_16x16x32_bf16 v[136:139], a[112:115], v[36:39], v[136:139]\n"
"v_exp_f32 v44, v44\n"
"v_mfma_f32_16x16x32_bf16 v[140:143], a[116:119], v[36:39], v[140:143]\n"
"v_exp_f32 v45, v45\n"
"v_mfma_f32_16x16x32_bf16 v[144:147], a[120:123], v[36:39], v[144:147]\n"
"s_add_u32 s76, s71, 8192\n"
"v_cvt_pk_bf16_f32 v33, v42, v43\n"
"v_add_f32 v16, v16, v41\n"
"v_mfma_f32_16x16x32_bf16 v[148:151], a[124:127], v[36:39], v[148:151]\n"
"s_add_u32 m0, s68, 73728\n"
"v_add_f32 v16, v16, v42\n"
"v_add_f32 v16, v16, v43\n"
"v_mfma_f32_16x16x32_bf16 v[24:27], v[20:23], v[36:39], v[24:27]\n"
"buffer_load_dwordx4 v10, s[88:91], s76 offen nt lds\n"
"ds_read_b64_tr_b16 a[80:81], v248 offset:32768\n"
"v_mfma_f32_16x16x32_bf16 v[48:51], a[32:35], v[88:91], 0\n"
"v_exp_f32 v46, v46\n"
"v_mfma_f32_16x16x32_bf16 v[48:51], a[36:39], v[92:95], v[48:51]\n"
"v_exp_f32 v47, v47\n"
"v_mfma_f32_16x16x32_bf16 v[48:51], a[40:43], v[96:99], v[48:51]\n"
"v_cvt_pk_bf16_f32 v34, v44, v45\n"
"v_add_f32 v16, v16, v44\n"
"v_mfma_f32_16x16x32_bf16 v[48:51], a[44:47], v[100:103], v[48:51]\n"
"v_cvt_pk_bf16_f32 v35, v46, v47\n"
"v_add_f32 v16, v16, v45\n"
"v_mfma_f32_16x16x32_bf16 v[52:55], a[48:51], v[88:91], 0\n"
"ds_read_b64_tr_b16 a[82:83], v248 offset:36864\n"
"v_add_f32 v16, v16, v46\n"
"v_mfma_f32_16x16x32_bf16 v[52:55], a[52:55], v[92:95], v[52:55]\n"
"ds_read_b64_tr_b16 a[84:85], v249 offset:32768\n"
"v_add_f32 v16, v16, v47\n"
"v_mfma_f32_16x16x32_bf16 v[52:55], a[56:59], v[96:99], v[52:55]\n"
"s_add_u32 s77, s71, 12288\n"
"ds_read_b64_tr_b16 a[86:87], v249 offset:36864\n"
"ds_read_b64_tr_b16 a[88:89], v248 offset:33024\n"
"v_mfma_f32_16x16x32_bf16 v[52:55], a[60:63], v[100:103], v[52:55]\n"
"s_add_u32 m0, s68, 77824\n"
"v_exp_f32 v48, v48\n"
"v_mfma_f32_16x16x32_bf16 v[152:155], a[96:99], v[32:35], v[152:155]\n"
"buffer_load_dwordx4 v10, s[88:91], s77 offen nt lds\n"
"v_exp_f32 v49, v49\n"
"v_mfma_f32_16x16x32_bf16 v[156:159], a[100:103], v[32:35], v[156:159]\n"
"v_exp_f32 v50, v50\n"
"v_mfma_f32_16x16x32_bf16 v[160:163], a[104:107], v[32:35], v[160:163]\n"
"v_exp_f32 v51, v51\n"
"v_mfma_f32_16x16x32_bf16 v[164:167], a[108:111], v[32:35], v[164:167]\n"
"v_cvt_pk_bf16_f32 v36, v48, v49\n"
"v_add_f32 v17, v17, v48\n"
"v_mfma_f32_16x16x32_bf16 v[168:171], a[112:115], v[32:35], v[168:171]\n"
"v_exp_f32 v52, v52\n"
"v_mfma_f32_16x16x32_bf16 v[172:175], a[116:119], v[32:35], v[172:175]\n"
"v_exp_f32 v53, v53\n"
"v_mfma_f32_16x16x32_bf16 v[176:179], a[120:123], v[32:35], v[176:179]\n"
"s_add_u32 s61, s75, 5\n"
"v_cvt_pk_bf16_f32 v37, v50, v51\n"
"v_add_f32 v17, v17, v49\n"
"v_mfma_f32_16x16x32_bf16 v[180:183], a[124:127], v[32:35], v[180:183]\n"
"s_and_b32 s61, s61, 15\n"
"s_waitcnt lgkmcnt(13)\n"
"v_add_f32 v17, v17, v50\n"
"v_add_f32 v17, v17, v51\n"
"v_mfma_f32_16x16x32_bf16 v[40:43], a[0:3], v[56:59], 0\n"
"s_lshl_b32 s61, s61, 14\n"
"v_exp_f32 v54, v54\n"
"v_mfma_f32_16x16x32_bf16 v[40:43], a[4:7], v[60:63], v[40:43]\n"
"s_add_u32 s71, s68, s61\n"
"v_exp_f32 v55, v55\n"
"v_mfma_f32_16x16x32_bf16 v[40:43], a[8:11], v[64:67], v[40:43]\n"
"s_add_u32 s76, s71, 0\n"
"v_cvt_pk_bf16_f32 v38, v52, v53\n"
"v_add_f32 v17, v17, v52\n"
"v_mfma_f32_16x16x32_bf16 v[40:43], a[12:15], v[68:71], v[40:43]\n"
"s_add_u32 m0, s68, 16384\n"
"v_cvt_pk_bf16_f32 v39, v54, v55\n"
"v_add_f32 v17, v17, v53\n"
"v_mfma_f32_16x16x32_bf16 v[44:47], a[16:19], v[56:59], 0\n"
"buffer_load_dwordx4 v9, s[28:31], s76 offen nt lds\n"
"v_add_f32 v17, v17, v54\n"
"v_mfma_f32_16x16x32_bf16 v[44:47], a[20:23], v[60:63], v[44:47]\n"
"ds_read_b64_tr_b16 a[90:91], v248 offset:37120\n"
"v_add_f32 v17, v17, v55\n"
"v_mfma_f32_16x16x32_bf16 v[44:47], a[24:27], v[64:67], v[44:47]\n"
"ds_read_b64_tr_b16 a[92:93], v249 offset:33024\n"
"ds_read_b64_tr_b16 a[94:95], v249 offset:37120\n"
"v_mfma_f32_16x16x32_bf16 v[44:47], a[28:31], v[68:71], v[44:47]\n"
"ds_read_b128 a[32:35], v3 offset:32768\n"
"v_exp_f32 v40, v40\n"
"v_mfma_f32_16x16x32_bf16 v[184:187], a[96:99], v[36:39], v[184:187]\n"
"v_exp_f32 v41, v41\n"
"v_mfma_f32_16x16x32_bf16 v[188:191], a[100:103], v[36:39], v[188:191]\n"
"ds_read_b128 a[36:39], v3 offset:33024\n"
"v_exp_f32 v42, v42\n"
"v_mfma_f32_16x16x32_bf16 v[192:195], a[104:107], v[36:39], v[192:195]\n"
"v_exp_f32 v43, v43\n"
"v_mfma_f32_16x16x32_bf16 v[196:199], a[108:111], v[36:39], v[196:199]\n"
"ds_read_b128 a[40:43], v4 offset:32768\n"
"v_cvt_pk_bf16_f32 v32, v40, v41\n"
"v_mfma_f32_16x16x32_bf16 v[200:203], a[112:115], v[36:39], v[200:203]\n"
"s_add_u32 s77, s71, 4096\n"
"ds_read_b128 a[44:47], v4 offset:33024\n"
"v_exp_f32 v44, v44\n"
"v_mfma_f32_16x16x32_bf16 v[204:207], a[116:119], v[36:39], v[204:207]\n"
"s_add_u32 m0, s68, 20480\n"
"v_exp_f32 v45, v45\n"
"v_mfma_f32_16x16x32_bf16 v[208:211], a[120:123], v[36:39], v[208:211]\n"
"buffer_load_dwordx4 v9, s[28:31], s77 offen nt lds\n"
"v_cvt_pk_bf16_f32 v33, v42, v43\n"
"v_mfma_f32_16x16x32_bf16 v[212:215], a[124:127], v[36:39], v[212:215]\n"
"ds_read_b128 a[48:51], v3 offset:36864\n"
"ds_read_b128 a[52:55], v3 offset:37120\n"
"v_mfma_f32_16x16x32_bf16 v[48:51], a[0:3], v[72:75], 0\n"
"ds_read_b128 a[56:59], v4 offset:36864\n"
"v_exp_f32 v46, v46\n"
"v_mfma_f32_16x16x32_bf16 v[48:51], a[4:7], v[76:79], v[48:51]\n"
"v_exp_f32 v47, v47\n"
"v_mfma_f32_16x16x32_bf16 v[48:51], a[8:11], v[80:83], v[48:51]\n"
"ds_read_b128 a[60:63], v4 offset:37120\n"
"v_cvt_pk_bf16_f32 v34, v44, v45\n"
"v_mfma_f32_16x16x32_bf16 v[48:51], a[12:15], v[84:87], v[48:51]\n"
"ds_read_b64_tr_b16 a[96:97], v7 offset:32768\n"
"v_cvt_pk_bf16_f32 v35, v46, v47\n"
"v_mfma_f32_16x16x32_bf16 v[52:55], a[16:19], v[72:75], 0\n"
"s_add_u32 s76, s71, 8192\n"
"ds_read_b64_tr_b16 a[98:99], v7 offset:36864\n"
"ds_read_b64_tr_b16 a[100:101], v8 offset:32768\n"
"v_mfma_f32_16x16x32_bf16 v[52:55], a[20:23], v[76:79], v[52:55]\n"
"s_add_u32 m0, s68, 24576\n"
"ds_read_b64_tr_b16 a[102:103], v8 offset:36864\n"
"ds_read_b64_tr_b16 a[104:105], v7 offset:33024\n"
"v_mfma_f32_16x16x32_bf16 v[52:55], a[24:27], v[80:83], v[52:55]\n"
"buffer_load_dwordx4 v9, s[28:31], s76 offen nt lds\n"
"ds_read_b64_tr_b16 a[106:107], v7 offset:37120\n"
"v_mfma_f32_16x16x32_bf16 v[52:55], a[28:31], v[84:87], v[52:55]\n"
"s_waitcnt lgkmcnt(14)\n"
"ds_read_b64_tr_b16 a[108:109], v8 offset:33024\n"
"v_exp_f32 v48, v48\n"
"v_mfma_f32_16x16x32_bf16 v[120:123], a[64:67], v[32:35], v[120:123]\n"
"v_exp_f32 v49, v49\n"
"v_mfma_f32_16x16x32_bf16 v[124:127], a[68:71], v[32:35], v[124:127]\n"
"v_exp_f32 v50, v50\n"
"v_mfma_f32_16x16x32_bf16 v[128:131], a[72:75], v[32:35], v[128:131]\n"
"v_exp_f32 v51, v51\n"
"v_mfma_f32_16x16x32_bf16 v[132:135], a[76:79], v[32:35], v[132:135]\n"
"v_cvt_pk_bf16_f32 v36, v48, v49\n"
"v_add_f32 v16, v16, v48\n"
"v_mfma_f32_16x16x32_bf16 v[136:139], a[80:83], v[32:35], v[136:139]\n"
"v_exp_f32 v52, v52\n"
"v_mfma_f32_16x16x32_bf16 v[140:143], a[84:87], v[32:35], v[140:143]\n"
"v_exp_f32 v53, v53\n"
"v_mfma_f32_16x16x32_bf16 v[144:147], a[88:91], v[32:35], v[144:147]\n"
"s_add_u32 s77, s71, 12288\n"
"v_cvt_pk_bf16_f32 v37, v50, v51\n"
"v_add_f32 v16, v16, v49\n"
"v_mfma_f32_16x16x32_bf16 v[148:151], a[92:95], v[32:35], v[148:151]\n"
"s_add_u32 m0, s68, 28672\n"
"v_add_f32 v16, v16, v50\n"
"v_add_f32 v16, v16, v51\n"
"v_mfma_f32_16x16x32_bf16 v[24:27], v[20:23], v[32:35], v[24:27]\n"
"buffer_load_dwordx4 v9, s[28:31], s77 offen nt lds\n"
"ds_read_b64_tr_b16 a[110:111], v8 offset:37120\n"
"v_mfma_f32_16x16x32_bf16 v[40:43], a[0:3], v[88:91], 0\n"
"v_exp_f32 v54, v54\n"
"v_mfma_f32_16x16x32_bf16 v[40:43], a[4:7], v[92:95], v[40:43]\n"
"v_exp_f32 v55, v55\n"
"v_mfma_f32_16x16x32_bf16 v[40:43], a[8:11], v[96:99], v[40:43]\n"
"v_cvt_pk_bf16_f32 v38, v52, v53\n"
"v_add_f32 v16, v16, v52\n"
"v_mfma_f32_16x16x32_bf16 v[40:43], a[12:15], v[100:103], v[40:43]\n"
"v_cvt_pk_bf16_f32 v39, v54, v55\n"
"v_add_f32 v16, v16, v53\n"
"v_mfma_f32_16x16x32_bf16 v[44:47], a[16:19], v[88:91], 0\n"
"ds_read_b64_tr_b16 a[112:113], v250 offset:32768\n"
"v_add_f32 v16, v16, v54\n"
"v_mfma_f32_16x16x32_bf16 v[44:47], a[20:23], v[92:95], v[44:47]\n"
"ds_read_b64_tr_b16 a[114:115], v250 offset:36864\n"
"v_add_f32 v16, v16, v55\n"
"v_mfma_f32_16x16x32_bf16 v[44:47], a[24:27], v[96:99], v[44:47]\n"
"ds_read_b64_tr_b16 a[116:117], v251 offset:32768\n"
"ds_read_b64_tr_b16 a[118:119], v251 offset:36864\n"
"v_mfma_f32_16x16x32_bf16 v[44:47], a[28:31], v[100:103], v[44:47]\n"
"s_add_u32 s76, s71, 0\n"
"v_exp_f32 v40, v40\n"
"v_mfma_f32_16x16x32_bf16 v[152:155], a[64:67], v[36:39], v[152:155]\n"
"s_add_u32 m0, s68, 81920\n"
"v_exp_f32 v41, v41\n"
"v_mfma_f32_16x16x32_bf16 v[156:159], a[68:71], v[36:39], v[156:159]\n"
"buffer_load_dwordx4 v10, s[88:91], s76 offen nt lds\n"
"v_exp_f32 v42, v42\n"
"v_mfma_f32_16x16x32_bf16 v[160:163], a[72:75], v[36:39], v[160:163]\n"
"v_exp_f32 v43, v43\n"
"v_mfma_f32_16x16x32_bf16 v[164:167], a[76:79], v[36:39], v[164:167]\n"
"v_cvt_pk_bf16_f32 v32, v40, v41\n"
"v_add_f32 v17, v17, v40\n"
"v_mfma_f32_16x16x32_bf16 v[168:171], a[80:83], v[36:39], v[168:171]\n"
"v_exp_f32 v44, v44\n"
"v_mfma_f32_16x16x32_bf16 v[172:175], a[84:87], v[36:39], v[172:175]\n"
"v_exp_f32 v45, v45\n"
"v_mfma_f32_16x16x32_bf16 v[176:179], a[88:91], v[36:39], v[176:179]\n"
"v_cvt_pk_bf16_f32 v33, v42, v43\n"
"v_add_f32 v17, v17, v41\n"
"v_mfma_f32_16x16x32_bf16 v[180:183], a[92:95], v[36:39], v[180:183]\n"
"v_add_f32 v17, v17, v42\n"
"v_add_f32 v17, v17, v43\n"
"v_mfma_f32_16x16x32_bf16 v[48:51], a[0:3], v[104:107], 0\n"
"v_exp_f32 v46, v46\n"
"v_mfma_f32_16x16x32_bf16 v[48:51], a[4:7], v[108:111], v[48:51]\n"
"v_exp_f32 v47, v47\n"
"v_mfma_f32_16x16x32_bf16 v[48:51], a[8:11], v[112:115], v[48:51]\n"
"s_add_u32 s77, s71, 4096\n"
"v_cvt_pk_bf16_f32 v34, v44, v45\n"
"v_add_f32 v17, v17, v44\n"
"v_mfma_f32_16x16x32_bf16 v[48:51], a[12:15], v[116:119], v[48:51]\n"
"s_add_u32 m0, s68, 86016\n"
"v_cvt_pk_bf16_f32 v35, v46, v47\n"
"v_add_f32 v17, v17, v45\n"
"v_mfma_f32_16x16x32_bf16 v[52:55], a[16:19], v[104:107], 0\n"
"buffer_load_dwordx4 v10, s[88:91], s77 offen nt lds\n"
"v_add_f32 v17, v17, v46\n"
"v_mfma_f32_16x16x32_bf16 v[52:55], a[20:23], v[108:111], v[52:55]\n"
"ds_read_b64_tr_b16 a[120:121], v250 offset:33024\n"
"v_add_f32 v17, v17, v47\n"
"v_mfma_f32_16x16x32_bf16 v[52:55], a[24:27], v[112:115], v[52:55]\n"
"ds_read_b64_tr_b16 a[122:123], v250 offset:37120\n"
"ds_read_b64_tr_b16 a[124:125], v251 offset:33024\n"
"v_mfma_f32_16x16x32_bf16 v[52:55], a[28:31], v[116:119], v[52:55]\n"
"v_exp_f32 v48, v48\n"
"v_mfma_f32_16x16x32_bf16 v[184:187], a[64:67], v[32:35], v[184:187]\n"
"v_exp_f32 v49, v49\n"
"v_mfma_f32_16x16x32_bf16 v[188:191], a[68:71], v[32:35], v[188:191]\n"
"v_exp_f32 v50, v50\n"
"v_mfma_f32_16x16x32_bf16 v[192:195], a[72:75], v[32:35], v[192:195]\n"
"v_exp_f32 v51, v51\n"
"v_mfma_f32_16x16x32_bf16 v[196:199], a[76:79], v[32:35], v[196:199]\n"
"ds_read_b64_tr_b16 a[126:127], v251 offset:37120\n"
"v_cvt_pk_bf16_f32 v36, v48, v49\n"
"v_mfma_f32_16x16x32_bf16 v[200:203], a[80:83], v[32:35], v[200:203]\n"
"s_add_u32 s76, s71, 8192\n"
"v_exp_f32 v52, v52\n"
"v_mfma_f32_16x16x32_bf16 v[204:207], a[84:87], v[32:35], v[204:207]\n"
"s_add_u32 m0, s68, 90112\n"
"v_exp_f32 v53, v53\n"
"v_mfma_f32_16x16x32_bf16 v[208:211], a[88:91], v[32:35], v[208:211]\n"
"buffer_load_dwordx4 v10, s[88:91], s76 offen nt lds\n"
"v_cvt_pk_bf16_f32 v37, v50, v51\n"
"v_mfma_f32_16x16x32_bf16 v[212:215], a[92:95], v[32:35], v[212:215]\n"
"s_waitcnt vmcnt(15) lgkmcnt(0)\n"
"s_barrier\n"
"v_mfma_f32_16x16x32_bf16 v[40:43], a[32:35], v[56:59], 0\n"
"v_exp_f32 v54, v54\n"
"v_mfma_f32_16x16x32_bf16 v[40:43], a[36:39], v[60:63], v[40:43]\n"
"v_exp_f32 v55, v55\n"
"v_mfma_f32_16x16x32_bf16 v[40:43], a[40:43], v[64:67], v[40:43]\n"
"ds_read_b128 a[0:3], v1 offset:49152\n"
"v_cvt_pk_bf16_f32 v38, v52, v53\n"
"v_mfma_f32_16x16x32_bf16 v[40:43], a[44:47], v[68:71], v[40:43]\n"
"ds_read_b128 a[4:7], v1 offset:49408\n"
"v_cvt_pk_bf16_f32 v39, v54, v55\n"
"v_mfma_f32_16x16x32_bf16 v[44:47], a[48:51], v[56:59], 0\n"
"ds_read_b128 a[8:11], v2 offset:49152\n"
"v_mfma_f32_16x16x32_bf16 v[44:47], a[52:55], v[60:63], v[44:47]\n"
"ds_read_b128 a[12:15], v2 offset:49408\n"
"ds_read_b128 a[16:19], v1 offset:53248\n"
"v_mfma_f32_16x16x32_bf16 v[44:47], a[56:59], v[64:67], v[44:47]\n"
"ds_read_b128 a[20:23], v1 offset:53504\n"
"v_mfma_f32_16x16x32_bf16 v[44:47], a[60:63], v[68:71], v[44:47]\n"
"v_exp_f32 v40, v40\n"
"v_mfma_f32_16x16x32_bf16 v[232:235], a[80:83], v[36:39], v[232:235]\n"
"v_exp_f32 v41, v41\n"
"v_mfma_f32_16x16x32_bf16 v[236:239], a[84:87], v[36:39], v[236:239]\n"
"s_add_u32 s77, s71, 12288\n"
"v_exp_f32 v42, v42\n"
"v_mfma_f32_16x16x32_bf16 v[240:243], a[88:91], v[36:39], v[240:243]\n"
"s_add_u32 m0, s68, 94208\n"
"v_exp_f32 v43, v43\n"
"v_mfma_f32_16x16x32_bf16 v[244:247], a[92:95], v[36:39], v[244:247]\n"
"buffer_load_dwordx4 v10, s[88:91], s77 offen nt lds\n"
"v_cvt_pk_bf16_f32 v32, v40, v41\n"
"v_mfma_f32_16x16x32_bf16 v[216:219], a[64:67], v[36:39], v[216:219]\n"
"v_exp_f32 v44, v44\n"
"v_mfma_f32_16x16x32_bf16 v[220:223], a[68:71], v[36:39], v[220:223]\n"
"s_add_u32 s61, s75, 6\n"
"v_exp_f32 v45, v45\n"
"v_mfma_f32_16x16x32_bf16 v[224:227], a[72:75], v[36:39], v[224:227]\n"
"s_and_b32 s61, s61, 15\n"
"ds_read_b128 a[24:27], v2 offset:53248\n"
"v_cvt_pk_bf16_f32 v33, v42, v43\n"
"v_mfma_f32_16x16x32_bf16 v[228:231], a[76:79], v[36:39], v[228:231]\n"
"s_lshl_b32 s61, s61, 14\n"
"ds_read_b128 a[28:31], v2 offset:53504\n"
"v_mfma_f32_16x16x32_bf16 v[28:31], v[20:23], v[36:39], v[28:31]\n"
"s_add_u32 s71, s68, s61\n"
"ds_read_b64_tr_b16 a[64:65], v5 offset:49152\n"
"ds_read_b64_tr_b16 a[66:67], v5 offset:53248\n"
"v_mfma_f32_16x16x32_bf16 v[48:51], a[32:35], v[72:75], 0\n"
"s_add_u32 s76, s71, 0\n"
"v_exp_f32 v46, v46\n"
"v_mfma_f32_16x16x32_bf16 v[48:51], a[36:39], v[76:79], v[48:51]\n"
"s_add_u32 m0, s68, 32768\n"
"v_exp_f32 v47, v47\n"
"v_mfma_f32_16x16x32_bf16 v[48:51], a[40:43], v[80:83], v[48:51]\n"
"buffer_load_dwordx4 v9, s[28:31], s76 offen nt lds\n"
"v_cvt_pk_bf16_f32 v34, v44, v45\n"
"v_mfma_f32_16x16x32_bf16 v[48:51], a[44:47], v[84:87], v[48:51]\n"
"ds_read_b64_tr_b16 a[68:69], v6 offset:49152\n"
"v_cvt_pk_bf16_f32 v35, v46, v47\n"
"v_mfma_f32_16x16x32_bf16 v[52:55], a[48:51], v[72:75], 0\n"
"ds_read_b64_tr_b16 a[70:71], v6 offset:53248\n"
"ds_read_b64_tr_b16 a[72:73], v5 offset:49408\n"
"v_mfma_f32_16x16x32_bf16 v[52:55], a[52:55], v[76:79], v[52:55]\n"
"ds_read_b64_tr_b16 a[74:75], v5 offset:53504\n"
"ds_read_b64_tr_b16 a[76:77], v6 offset:49408\n"
"v_mfma_f32_16x16x32_bf16 v[52:55], a[56:59], v[80:83], v[52:55]\n"
"ds_read_b64_tr_b16 a[78:79], v6 offset:53504\n"
"ds_read_b64_tr_b16 a[80:81], v248 offset:49152\n"
"v_mfma_f32_16x16x32_bf16 v[52:55], a[60:63], v[84:87], v[52:55]\n"
"v_exp_f32 v48, v48\n"
"v_mfma_f32_16x16x32_bf16 v[120:123], a[96:99], v[32:35], v[120:123]\n"
"v_exp_f32 v49, v49\n"
"v_mfma_f32_16x16x32_bf16 v[124:127], a[100:103], v[32:35], v[124:127]\n"
"v_exp_f32 v50, v50\n"
"v_mfma_f32_16x16x32_bf16 v[128:131], a[104:107], v[32:35], v[128:131]\n"
"v_exp_f32 v51, v51\n"
"v_mfma_f32_16x16x32_bf16 v[132:135], a[108:111], v[32:35], v[132:135]\n"
"v_cvt_pk_bf16_f32 v36, v48, v49\n"
"v_add_f32 v16, v16, v48\n"
"v_mfma_f32_16x16x32_bf16 v[136:139], a[112:115], v[32:35], v[136:139]\n"
"v_exp_f32 v52, v52\n"
"v_mfma_f32_16x16x32_bf16 v[140:143], a[116:119], v[32:35], v[140:143]\n"
"v_exp_f32 v53, v53\n"
"v_mfma_f32_16x16x32_bf16 v[144:147], a[120:123], v[32:35], v[144:147]\n"
"s_add_u32 s77, s71, 4096\n"
"v_cvt_pk_bf16_f32 v37, v50, v51\n"
"v_add_f32 v16, v16, v49\n"
"v_mfma_f32_16x16x32_bf16 v[148:151], a[124:127], v[32:35], v[148:151]\n"
"s_add_u32 m0, s68, 36864\n"
"v_add_f32 v16, v16, v50\n"
"v_add_f32 v16, v16, v51\n"
"v_mfma_f32_16x16x32_bf16 v[24:27], v[20:23], v[32:35], v[24:27]\n"
"buffer_load_dwordx4 v9, s[28:31], s77 offen nt lds\n"
"ds_read_b64_tr_b16 a[82:83], v248 offset:53248\n"
"v_mfma_f32_16x16x32_bf16 v[40:43], a[32:35], v[88:91], 0\n"
"v_exp_f32 v54, v54\n"
"v_mfma_f32_16x16x32_bf16 v[40:43], a[36:39], v[92:95], v[40:43]\n"
"v_exp_f32 v55, v55\n"
"v_mfma_f32_16x16x32_bf16 v[40:43], a[40:43], v[96:99], v[40:43]\n"
"s_add_u32 s76, s71, 8192\n"
"v_cvt_pk_bf16_f32 v38, v52, v53\n"
"v_add_f32 v16, v16, v52\n"
"v_mfma_f32_16x16x32_bf16 v[40:43], a[44:47], v[100:103], v[40:43]\n"
"s_add_u32 m0, s68, 40960\n"
"v_cvt_pk_bf16_f32 v39, v54, v55\n"
"v_add_f32 v16, v16, v53\n"
"v_mfma_f32_16x16x32_bf16 v[44:47], a[48:51], v[88:91], 0\n"
"buffer_load_dwordx4 v9, s[28:31], s76 offen nt lds\n"
"v_add_f32 v16, v16, v54\n"
"v_mfma_f32_16x16x32_bf16 v[44:47], a[52:55], v[92:95], v[44:47]\n"
"ds_read_b64_tr_b16 a[84:85], v249 offset:49152\n"
"v_add_f32 v16, v16, v55\n"
"v_mfma_f32_16x16x32_bf16 v[44:47], a[56:59], v[96:99], v[44:47]\n"
"ds_read_b64_tr_b16 a[86:87], v249 offset:53248\n"
"ds_read_b64_tr_b16 a[88:89], v248 offset:49408\n"
"v_mfma_f32_16x16x32_bf16 v[44:47], a[60:63], v[100:103], v[44:47]\n"
"v_exp_f32 v40, v40\n"
"v_mfma_f32_16x16x32_bf16 v[152:155], a[96:99], v[36:39], v[152:155]\n"
"v_exp_f32 v41, v41\n"
"v_mfma_f32_16x16x32_bf16 v[156:159], a[100:103], v[36:39], v[156:159]\n"
"v_exp_f32 v42, v42\n"
"v_mfma_f32_16x16x32_bf16 v[160:163], a[104:107], v[36:39], v[160:163]\n"
"v_exp_f32 v43, v43\n"
"v_mfma_f32_16x16x32_bf16 v[164:167], a[108:111], v[36:39], v[164:167]\n"
"s_add_u32 s77, s71, 12288\n"
"v_cvt_pk_bf16_f32 v32, v40, v41\n"
"v_add_f32 v17, v17, v40\n"
"v_mfma_f32_16x16x32_bf16 v[168:171], a[112:115], v[36:39], v[168:171]\n"
"s_add_u32 m0, s68, 45056\n"
"v_exp_f32 v44, v44\n"
"v_mfma_f32_16x16x32_bf16 v[172:175], a[116:119], v[36:39], v[172:175]\n"
"buffer_load_dwordx4 v9, s[28:31], s77 offen nt lds\n"
"v_exp_f32 v45, v45\n"
"v_mfma_f32_16x16x32_bf16 v[176:179], a[120:123], v[36:39], v[176:179]\n"
"v_cvt_pk_bf16_f32 v33, v42, v43\n"
"v_add_f32 v17, v17, v41\n"
"v_mfma_f32_16x16x32_bf16 v[180:183], a[124:127], v[36:39], v[180:183]\n"
"s_waitcnt lgkmcnt(13)\n"
"v_add_f32 v17, v17, v42\n"
"v_add_f32 v17, v17, v43\n"
"v_mfma_f32_16x16x32_bf16 v[48:51], a[0:3], v[56:59], 0\n"
"v_exp_f32 v46, v46\n"
"v_mfma_f32_16x16x32_bf16 v[48:51], a[4:7], v[60:63], v[48:51]\n"
"v_exp_f32 v47, v47\n"
"v_mfma_f32_16x16x32_bf16 v[48:51], a[8:11], v[64:67], v[48:51]\n"
"v_cvt_pk_bf16_f32 v34, v44, v45\n"
"v_add_f32 v17, v17, v44\n"
"v_mfma_f32_16x16x32_bf16 v[48:51], a[12:15], v[68:71], v[48:51]\n"
"v_cvt_pk_bf16_f32 v35, v46, v47\n"
"v_add_f32 v17, v17, v45\n"
"v_mfma_f32_16x16x32_bf16 v[52:55], a[16:19], v[56:59], 0\n"
"ds_read_b64_tr_b16 a[90:91], v248 offset:53504\n"
"v_add_f32 v17, v17, v46\n"
"v_mfma_f32_16x16x32_bf16 v[52:55], a[20:23], v[60:63], v[52:55]\n"
"ds_read_b64_tr_b16 a[92:93], v249 offset:49408\n"
"v_add_f32 v17, v17, v47\n"
"v_mfma_f32_16x16x32_bf16 v[52:55], a[24:27], v[64:67], v[52:55]\n"
"ds_read_b64_tr_b16 a[94:95], v249 offset:53504\n"
"ds_read_b128 a[32:35], v3 offset:49152\n"
"v_mfma_f32_16x16x32_bf16 v[52:55], a[28:31], v[68:71], v[52:55]\n"
"ds_read_b128 a[36:39], v3 offset:49408\n"
"v_exp_f32 v48, v48\n"
"v_mfma_f32_16x16x32_bf16 v[184:187], a[96:99], v[32:35], v[184:187]\n"
"v_exp_f32 v49, v49\n"
"v_mfma_f32_16x16x32_bf16 v[188:191], a[100:103], v[32:35], v[188:191]\n"
"s_add_u32 s76, s71, 0\n"
"ds_read_b128 a[40:43], v4 offset:49152\n"
"v_exp_f32 v50, v50\n"
"v_mfma_f32_16x16x32_bf16 v[192:195], a[104:107], v[32:35], v[192:195]\n"
"s_add_u32 m0, s68, 98304\n"
"v_exp_f32 v51, v51\n"
"v_mfma_f32_16x16x32_bf16 v[196:199], a[108:111], v[32:35], v[196:199]\n"
"buffer_load_dwordx4 v10, s[88:91], s76 offen nt lds\n"
"v_cvt_pk_bf16_f32 v36, v48, v49\n"
"v_mfma_f32_16x16x32_bf16 v[200:203], a[112:115], v[32:35], v[200:203]\n"
"ds_read_b128 a[44:47], v4 offset:49408\n"
"v_exp_f32 v52, v52\n"
"v_mfma_f32_16x16x32_bf16 v[204:207], a[116:119], v[32:35], v[204:207]\n"
"v_exp_f32 v53, v53\n"
"v_mfma_f32_16x16x32_bf16 v[208:211], a[120:123], v[32:35], v[208:211]\n"
"ds_read_b128 a[48:51], v3 offset:53248\n"
"v_cvt_pk_bf16_f32 v37, v50, v51\n"
"v_mfma_f32_16x16x32_bf16 v[212:215], a[124:127], v[32:35], v[212:215]\n"
"ds_read_b128 a[52:55], v3 offset:53504\n"
"ds_read_b128 a[56:59], v4 offset:53248\n"
"v_mfma_f32_16x16x32_bf16 v[40:43], a[0:3], v[72:75], 0\n"
"s_add_u32 s77, s71, 4096\n"
"ds_read_b128 a[60:63], v4 offset:53504\n"
"v_exp_f32 v54, v54\n"
"v_mfma_f32_16x16x32_bf16 v[40:43], a[4:7], v[76:79], v[40:43]\n"
"s_add_u32 m0, s68, 102400\n"
"v_exp_f32 v55, v55\n"
"v_mfma_f32_16x16x32_bf16 v[40:43], a[8:11], v[80:83], v[40:43]\n"
"buffer_load_dwordx4 v10, s[88:91], s77 offen nt lds\n"
"v_cvt_pk_bf16_f32 v38, v52, v53\n"
"v_mfma_f32_16x16x32_bf16 v[40:43], a[12:15], v[84:87], v[40:43]\n"
"ds_read_b64_tr_b16 a[96:97], v7 offset:49152\n"
"v_cvt_pk_bf16_f32 v39, v54, v55\n"
"v_mfma_f32_16x16x32_bf16 v[44:47], a[16:19], v[72:75], 0\n"
"ds_read_b64_tr_b16 a[98:99], v7 offset:53248\n"
"ds_read_b64_tr_b16 a[100:101], v8 offset:49152\n"
"v_mfma_f32_16x16x32_bf16 v[44:47], a[20:23], v[76:79], v[44:47]\n"
"ds_read_b64_tr_b16 a[102:103], v8 offset:53248\n"
"ds_read_b64_tr_b16 a[104:105], v7 offset:49408\n"
"v_mfma_f32_16x16x32_bf16 v[44:47], a[24:27], v[80:83], v[44:47]\n"
"ds_read_b64_tr_b16 a[106:107], v7 offset:53504\n"
"ds_read_b64_tr_b16 a[108:109], v8 offset:49408\n"
"v_mfma_f32_16x16x32_bf16 v[44:47], a[28:31], v[84:87], v[44:47]\n"
"s_waitcnt lgkmcnt(15)\n"
"v_exp_f32 v40, v40\n"
"v_mfma_f32_16x16x32_bf16 v[120:123], a[64:67], v[36:39], v[120:123]\n"
"v_exp_f32 v41, v41\n"
"v_mfma_f32_16x16x32_bf16 v[124:127], a[68:71], v[36:39], v[124:127]\n"
"v_exp_f32 v42, v42\n"
"v_mfma_f32_16x16x32_bf16 v[128:131], a[72:75], v[36:39], v[128:131]\n"
"v_exp_f32 v43, v43\n"
"v_mfma_f32_16x16x32_bf16 v[132:135], a[76:79], v[36:39], v[132:135]\n"
"v_cvt_pk_bf16_f32 v32, v40, v41\n"
"v_add_f32 v16, v16, v40\n"
"v_mfma_f32_16x16x32_bf16 v[136:139], a[80:83], v[36:39], v[136:139]\n"
"v_exp_f32 v44, v44\n"
"v_mfma_f32_16x16x32_bf16 v[140:143], a[84:87], v[36:39], v[140:143]\n"
"v_exp_f32 v45, v45\n"
"v_mfma_f32_16x16x32_bf16 v[144:147], a[88:91], v[36:39], v[144:147]\n"
"s_add_u32 s76, s71, 8192\n"
"v_cvt_pk_bf16_f32 v33, v42, v43\n"
"v_add_f32 v16, v16, v41\n"
"v_mfma_f32_16x16x32_bf16 v[148:151], a[92:95], v[36:39], v[148:151]\n"
"s_add_u32 m0, s68, 106496\n"
"v_add_f32 v16, v16, v42\n"
"v_add_f32 v16, v16, v43\n"
"v_mfma_f32_16x16x32_bf16 v[24:27], v[20:23], v[36:39], v[24:27]\n"
"buffer_load_dwordx4 v10, s[88:91], s76 offen nt lds\n"
"ds_read_b64_tr_b16 a[110:111], v8 offset:53504\n"
"v_mfma_f32_16x16x32_bf16 v[48:51], a[0:3], v[88:91], 0\n"
"v_exp_f32 v46, v46\n"
"v_mfma_f32_16x16x32_bf16 v[48:51], a[4:7], v[92:95], v[48:51]\n"
"v_exp_f32 v47, v47\n"
"v_mfma_f32_16x16x32_bf16 v[48:51], a[8:11], v[96:99], v[48:51]\n"
"s_add_u32 s77, s71, 12288\n"
"v_cvt_pk_bf16_f32 v34, v44, v45\n"
"v_add_f32 v16, v16, v44\n"
"v_mfma_f32_16x16x32_bf16 v[48:51], a[12:15], v[100:103], v[48:51]\n"
"s_add_u32 m0, s68, 110592\n"
"v_cvt_pk_bf16_f32 v35, v46, v47\n"
"v_add_f32 v16, v16, v45\n"
"v_mfma_f32_16x16x32_bf16 v[52:55], a[16:19], v[88:91], 0\n"
"buffer_load_dwordx4 v10, s[88:91], s77 offen nt lds\n"
"v_add_f32 v16, v16, v46\n"
"v_mfma_f32_16x16x32_bf16 v[52:55], a[20:23], v[92:95], v[52:55]\n"
"ds_read_b64_tr_b16 a[112:113], v250 offset:49152\n"
"v_add_f32 v16, v16, v47\n"
"v_mfma_f32_16x16x32_bf16 v[52:55], a[24:27], v[96:99], v[52:55]\n"
"ds_read_b64_tr_b16 a[114:115], v250 offset:53248\n"
"ds_read_b64_tr_b16 a[116:117], v251 offset:49152\n"
"v_mfma_f32_16x16x32_bf16 v[52:55], a[28:31], v[100:103], v[52:55]\n"
"v_exp_f32 v48, v48\n"
"v_mfma_f32_16x16x32_bf16 v[152:155], a[64:67], v[32:35], v[152:155]\n"
"v_exp_f32 v49, v49\n"
"v_mfma_f32_16x16x32_bf16 v[156:159], a[68:71], v[32:35], v[156:159]\n"
"v_exp_f32 v50, v50\n"
"v_mfma_f32_16x16x32_bf16 v[160:163], a[72:75], v[32:35], v[160:163]\n"
"v_exp_f32 v51, v51\n"
"v_mfma_f32_16x16x32_bf16 v[164:167], a[76:79], v[32:35], v[164:167]\n"
"v_cvt_pk_bf16_f32 v36, v48, v49\n"
"v_add_f32 v17, v17, v48\n"
"v_mfma_f32_16x16x32_bf16 v[168:171], a[80:83], v[32:35], v[168:171]\n"
"v_exp_f32 v52, v52\n"
"v_mfma_f32_16x16x32_bf16 v[172:175], a[84:87], v[32:35], v[172:175]\n"
"v_exp_f32 v53, v53\n"
"v_mfma_f32_16x16x32_bf16 v[176:179], a[88:91], v[32:35], v[176:179]\n"
"v_cvt_pk_bf16_f32 v37, v50, v51\n"
"v_add_f32 v17, v17, v49\n"
"v_mfma_f32_16x16x32_bf16 v[180:183], a[92:95], v[32:35], v[180:183]\n"
"v_add_f32 v17, v17, v50\n"
"v_add_f32 v17, v17, v51\n"
"v_mfma_f32_16x16x32_bf16 v[40:43], a[0:3], v[104:107], 0\n"
"v_exp_f32 v54, v54\n"
"v_mfma_f32_16x16x32_bf16 v[40:43], a[4:7], v[108:111], v[40:43]\n"
"v_exp_f32 v55, v55\n"
"v_mfma_f32_16x16x32_bf16 v[40:43], a[8:11], v[112:115], v[40:43]\n"
"v_cvt_pk_bf16_f32 v38, v52, v53\n"
"v_add_f32 v17, v17, v52\n"
"v_mfma_f32_16x16x32_bf16 v[40:43], a[12:15], v[116:119], v[40:43]\n"
"v_cvt_pk_bf16_f32 v39, v54, v55\n"
"v_add_f32 v17, v17, v53\n"
"v_mfma_f32_16x16x32_bf16 v[44:47], a[16:19], v[104:107], 0\n"
"ds_read_b64_tr_b16 a[118:119], v251 offset:53248\n"
"v_add_f32 v17, v17, v54\n"
"v_mfma_f32_16x16x32_bf16 v[44:47], a[20:23], v[108:111], v[44:47]\n"
"ds_read_b64_tr_b16 a[120:121], v250 offset:49408\n"
"v_add_f32 v17, v17, v55\n"
"v_mfma_f32_16x16x32_bf16 v[44:47], a[24:27], v[112:115], v[44:47]\n"
"ds_read_b64_tr_b16 a[122:123], v250 offset:53504\n"
"ds_read_b64_tr_b16 a[124:125], v251 offset:49408\n"
"v_mfma_f32_16x16x32_bf16 v[44:47], a[28:31], v[116:119], v[44:47]\n"
"v_exp_f32 v40, v40\n"
"v_mfma_f32_16x16x32_bf16 v[184:187], a[64:67], v[36:39], v[184:187]\n"
"v_exp_f32 v41, v41\n"
"v_mfma_f32_16x16x32_bf16 v[188:191], a[68:71], v[36:39], v[188:191]\n"
"v_exp_f32 v42, v42\n"
"v_mfma_f32_16x16x32_bf16 v[192:195], a[72:75], v[36:39], v[192:195]\n"
"v_exp_f32 v43, v43\n"
"v_mfma_f32_16x16x32_bf16 v[196:199], a[76:79], v[36:39], v[196:199]\n"
"ds_read_b64_tr_b16 a[126:127], v251 offset:53504\n"
"v_cvt_pk_bf16_f32 v32, v40, v41\n"
"v_mfma_f32_16x16x32_bf16 v[200:203], a[80:83], v[36:39], v[200:203]\n"
"v_exp_f32 v44, v44\n"
"v_mfma_f32_16x16x32_bf16 v[204:207], a[84:87], v[36:39], v[204:207]\n"
"v_exp_f32 v45, v45\n"
"v_mfma_f32_16x16x32_bf16 v[208:211], a[88:91], v[36:39], v[208:211]\n"
"v_cvt_pk_bf16_f32 v33, v42, v43\n"
"v_mfma_f32_16x16x32_bf16 v[212:215], a[92:95], v[36:39], v[212:215]\n"
"s_waitcnt vmcnt(16) lgkmcnt(0)\n"
"s_barrier\n"
"v_mfma_f32_16x16x32_bf16 v[48:51], a[32:35], v[56:59], 0\n"
"s_add_u32 s61, s75, 7\n"
"v_exp_f32 v46, v46\n"
"v_mfma_f32_16x16x32_bf16 v[48:51], a[36:39], v[60:63], v[48:51]\n"
"s_and_b32 s61, s61, 15\n"
"v_exp_f32 v47, v47\n"
"v_mfma_f32_16x16x32_bf16 v[48:51], a[40:43], v[64:67], v[48:51]\n"
"s_lshl_b32 s61, s61, 14\n"
"ds_read_b128 a[0:3], v1 offset:0\n"
"v_cvt_pk_bf16_f32 v34, v44, v45\n"
"v_mfma_f32_16x16x32_bf16 v[48:51], a[44:47], v[68:71], v[48:51]\n"
"s_add_u32 s71, s68, s61\n"
"ds_read_b128 a[4:7], v1 offset:256\n"
"v_cvt_pk_bf16_f32 v35, v46, v47\n"
"v_mfma_f32_16x16x32_bf16 v[52:55], a[48:51], v[56:59], 0\n"
"s_add_u32 s76, s71, 0\n"
"ds_read_b128 a[8:11], v2 offset:0\n"
"v_mfma_f32_16x16x32_bf16 v[52:55], a[52:55], v[60:63], v[52:55]\n"
"s_add_u32 m0, s68, 49152\n"
"ds_read_b128 a[12:15], v2 offset:256\n"
"ds_read_b128 a[16:19], v1 offset:4096\n"
"v_mfma_f32_16x16x32_bf16 v[52:55], a[56:59], v[64:67], v[52:55]\n"
"buffer_load_dwordx4 v9, s[28:31], s76 offen nt lds\n"
"ds_read_b128 a[20:23], v1 offset:4352\n"
"v_mfma_f32_16x16x32_bf16 v[52:55], a[60:63], v[68:71], v[52:55]\n"
"v_exp_f32 v48, v48\n"
"v_mfma_f32_16x16x32_bf16 v[232:235], a[80:83], v[32:35], v[232:235]\n"
"v_exp_f32 v49, v49\n"
"v_mfma_f32_16x16x32_bf16 v[236:239], a[84:87], v[32:35], v[236:239]\n"
"v_exp_f32 v50, v50\n"
"v_mfma_f32_16x16x32_bf16 v[240:243], a[88:91], v[32:35], v[240:243]\n"
"v_exp_f32 v51, v51\n"
"v_mfma_f32_16x16x32_bf16 v[244:247], a[92:95], v[32:35], v[244:247]\n"
"v_cvt_pk_bf16_f32 v36, v48, v49\n"
"v_mfma_f32_16x16x32_bf16 v[216:219], a[64:67], v[32:35], v[216:219]\n"
"v_exp_f32 v52, v52\n"
"v_mfma_f32_16x16x32_bf16 v[220:223], a[68:71], v[32:35], v[220:223]\n"
"v_exp_f32 v53, v53\n"
"v_mfma_f32_16x16x32_bf16 v[224:227], a[72:75], v[32:35], v[224:227]\n"
"s_add_u32 s77, s71, 4096\n"
"ds_read_b128 a[24:27], v2 offset:4096\n"
"v_cvt_pk_bf16_f32 v37, v50, v51\n"
"v_mfma_f32_16x16x32_bf16 v[228:231], a[76:79], v[32:35], v[228:231]\n"
"s_add_u32 m0, s68, 53248\n"
"ds_read_b128 a[28:31], v2 offset:4352\n"
"v_mfma_f32_16x16x32_bf16 v[28:31], v[20:23], v[32:35], v[28:31]\n"
"buffer_load_dwordx4 v9, s[28:31], s77 offen nt lds\n"
"ds_read_b64_tr_b16 a[64:65], v5 offset:0\n"
"v_mfma_f32_16x16x32_bf16 v[40:43], a[32:35], v[72:75], 0\n"
"v_exp_f32 v54, v54\n"
"v_mfma_f32_16x16x32_bf16 v[40:43], a[36:39], v[76:79], v[40:43]\n"
"v_exp_f32 v55, v55\n"
"v_mfma_f32_16x16x32_bf16 v[40:43], a[40:43], v[80:83], v[40:43]\n"
"ds_read_b64_tr_b16 a[66:67], v5 offset:4096\n"
"v_cvt_pk_bf16_f32 v38, v52, v53\n"
"v_mfma_f32_16x16x32_bf16 v[40:43], a[44:47], v[84:87], v[40:43]\n"
"ds_read_b64_tr_b16 a[68:69], v6 offset:0\n"
"v_cvt_pk_bf16_f32 v39, v54, v55\n"
"v_mfma_f32_16x16x32_bf16 v[44:47], a[48:51], v[72:75], 0\n"
"ds_read_b64_tr_b16 a[70:71], v6 offset:4096\n"
"ds_read_b64_tr_b16 a[72:73], v5 offset:256\n"
"v_mfma_f32_16x16x32_bf16 v[44:47], a[52:55], v[76:79], v[44:47]\n"
"ds_read_b64_tr_b16 a[74:75], v5 offset:4352\n"
"ds_read_b64_tr_b16 a[76:77], v6 offset:256\n"
"v_mfma_f32_16x16x32_bf16 v[44:47], a[56:59], v[80:83], v[44:47]\n"
"ds_read_b64_tr_b16 a[78:79], v6 offset:4352\n"
"v_mfma_f32_16x16x32_bf16 v[44:47], a[60:63], v[84:87], v[44:47]\n"
"s_add_u32 s76, s71, 8192\n"
"v_exp_f32 v40, v40\n"
"v_mfma_f32_16x16x32_bf16 v[120:123], a[96:99], v[36:39], v[120:123]\n"
"s_add_u32 m0, s68, 57344\n"
"v_exp_f32 v41, v41\n"
"v_mfma_f32_16x16x32_bf16 v[124:127], a[100:103], v[36:39], v[124:127]\n"
"buffer_load_dwordx4 v9, s[28:31], s76 offen nt lds\n"
"v_exp_f32 v42, v42\n"
"v_mfma_f32_16x16x32_bf16 v[128:131], a[104:107], v[36:39], v[128:131]\n"
"v_exp_f32 v43, v43\n"
"v_mfma_f32_16x16x32_bf16 v[132:135], a[108:111], v[36:39], v[132:135]\n"
"v_cvt_pk_bf16_f32 v32, v40, v41\n"
"v_add_f32 v16, v16, v40\n"
"v_mfma_f32_16x16x32_bf16 v[136:139], a[112:115], v[36:39], v[136:139]\n"
"v_exp_f32 v44, v44\n"
"v_mfma_f32_16x16x32_bf16 v[140:143], a[116:119], v[36:39], v[140:143]\n"
"v_exp_f32 v45, v45\n"
"v_mfma_f32_16x16x32_bf16 v[144:147], a[120:123], v[36:39], v[144:147]\n"
"v_cvt_pk_bf16_f32 v33, v42, v43\n"
"v_add_f32 v16, v16, v41\n"
"v_mfma_f32_16x16x32_bf16 v[148:151], a[124:127], v[36:39], v[148:151]\n"
"v_add_f32 v16, v16, v42\n"
"v_add_f32 v16, v16, v43\n"
"v_mfma_f32_16x16x32_bf16 v[24:27], v[20:23], v[36:39], v[24:27]\n"
"ds_read_b64_tr_b16 a[80:81], v248 offset:0\n"
"ds_read_b64_tr_b16 a[82:83], v248 offset:4096\n"
"v_mfma_f32_16x16x32_bf16 v[48:51], a[32:35], v[88:91], 0\n"
"v_exp_f32 v46, v46\n"
"v_mfma_f32_16x16x32_bf16 v[48:51], a[36:39], v[92:95], v[48:51]\n"
"v_exp_f32 v47, v47\n"
"v_mfma_f32_16x16x32_bf16 v[48:51], a[40:43], v[96:99], v[48:51]\n"
"s_add_u32 s77, s71, 12288\n"
"v_cvt_pk_bf16_f32 v34, v44, v45\n"
"v_add_f32 v16, v16, v44\n"
"v_mfma_f32_16x16x32_bf16 v[48:51], a[44:47], v[100:103], v[48:51]\n"
"s_add_u32 m0, s68, 61440\n"
"v_cvt_pk_bf16_f32 v35, v46, v47\n"
"v_add_f32 v16, v16, v45\n"
"v_mfma_f32_16x16x32_bf16 v[52:55], a[48:51], v[88:91], 0\n"
"buffer_load_dwordx4 v9, s[28:31], s77 offen nt lds\n"
"v_add_f32 v16, v16, v46\n"
"v_mfma_f32_16x16x32_bf16 v[52:55], a[52:55], v[92:95], v[52:55]\n"
"ds_read_b64_tr_b16 a[84:85], v249 offset:0\n"
"v_add_f32 v16, v16, v47\n"
"v_mfma_f32_16x16x32_bf16 v[52:55], a[56:59], v[96:99], v[52:55]\n"
"ds_read_b64_tr_b16 a[86:87], v249 offset:4096\n"
"ds_read_b64_tr_b16 a[88:89], v248 offset:256\n"
"v_mfma_f32_16x16x32_bf16 v[52:55], a[60:63], v[100:103], v[52:55]\n"
"v_exp_f32 v48, v48\n"
"v_mfma_f32_16x16x32_bf16 v[152:155], a[96:99], v[32:35], v[152:155]\n"
"v_exp_f32 v49, v49\n"
"v_mfma_f32_16x16x32_bf16 v[156:159], a[100:103], v[32:35], v[156:159]\n"
"v_exp_f32 v50, v50\n"
"v_mfma_f32_16x16x32_bf16 v[160:163], a[104:107], v[32:35], v[160:163]\n"
"s_add_u32 s76, s71, 0\n"
"v_exp_f32 v51, v51\n"
"v_mfma_f32_16x16x32_bf16 v[164:167], a[108:111], v[32:35], v[164:167]\n"
"s_add_u32 m0, s68, 114688\n"
"v_cvt_pk_bf16_f32 v36, v48, v49\n"
"v_add_f32 v17, v17, v48\n"
"v_mfma_f32_16x16x32_bf16 v[168:171], a[112:115], v[32:35], v[168:171]\n"
"buffer_load_dwordx4 v10, s[88:91], s76 offen nt lds\n"
"v_exp_f32 v52, v52\n"
"v_mfma_f32_16x16x32_bf16 v[172:175], a[116:119], v[32:35], v[172:175]\n"
"v_exp_f32 v53, v53\n"
"v_mfma_f32_16x16x32_bf16 v[176:179], a[120:123], v[32:35], v[176:179]\n"
"v_cvt_pk_bf16_f32 v37, v50, v51\n"
"v_add_f32 v17, v17, v49\n"
"v_mfma_f32_16x16x32_bf16 v[180:183], a[124:127], v[32:35], v[180:183]\n"
"s_waitcnt lgkmcnt(13)\n"
"v_add_f32 v17, v17, v50\n"
"v_add_f32 v17, v17, v51\n"
"v_mfma_f32_16x16x32_bf16 v[40:43], a[0:3], v[56:59], 0\n"
"v_exp_f32 v54, v54\n"
"v_mfma_f32_16x16x32_bf16 v[40:43], a[4:7], v[60:63], v[40:43]\n"
"v_exp_f32 v55, v55\n"
"v_mfma_f32_16x16x32_bf16 v[40:43], a[8:11], v[64:67], v[40:43]\n"
"v_cvt_pk_bf16_f32 v38, v52, v53\n"
"v_add_f32 v17, v17, v52\n"
"v_mfma_f32_16x16x32_bf16 v[40:43], a[12:15], v[68:71], v[40:43]\n"
"v_cvt_pk_bf16_f32 v39, v54, v55\n"
"v_add_f32 v17, v17, v53\n"
"v_mfma_f32_16x16x32_bf16 v[44:47], a[16:19], v[56:59], 0\n"
"s_add_u32 s77, s71, 4096\n"
"ds_read_b64_tr_b16 a[90:91], v248 offset:4352\n"
"v_add_f32 v17, v17, v54\n"
"v_mfma_f32_16x16x32_bf16 v[44:47], a[20:23], v[60:63], v[44:47]\n"
"s_add_u32 m0, s68, 118784\n"
"ds_read_b64_tr_b16 a[92:93], v249 offset:256\n"
"v_add_f32 v17, v17, v55\n"
"v_mfma_f32_16x16x32_bf16 v[44:47], a[24:27], v[64:67], v[44:47]\n"
"buffer_load_dwordx4 v10, s[88:91], s77 offen nt lds\n"
"ds_read_b64_tr_b16 a[94:95], v249 offset:4352\n"
"v_mfma_f32_16x16x32_bf16 v[44:47], a[28:31], v[68:71], v[44:47]\n"
"ds_read_b128 a[32:35], v3 offset:0\n"
"v_exp_f32 v40, v40\n"
"v_mfma_f32_16x16x32_bf16 v[184:187], a[96:99], v[36:39], v[184:187]\n"
"v_exp_f32 v41, v41\n"
"v_mfma_f32_16x16x32_bf16 v[188:191], a[100:103], v[36:39], v[188:191]\n"
"ds_read_b128 a[36:39], v3 offset:256\n"
"v_exp_f32 v42, v42\n"
"v_mfma_f32_16x16x32_bf16 v[192:195], a[104:107], v[36:39], v[192:195]\n"
"v_exp_f32 v43, v43\n"
"v_mfma_f32_16x16x32_bf16 v[196:199], a[108:111], v[36:39], v[196:199]\n"
"ds_read_b128 a[40:43], v4 offset:0\n"
"v_cvt_pk_bf16_f32 v32, v40, v41\n"
"v_mfma_f32_16x16x32_bf16 v[200:203], a[112:115], v[36:39], v[200:203]\n"
"ds_read_b128 a[44:47], v4 offset:256\n"
"v_exp_f32 v44, v44\n"
"v_mfma_f32_16x16x32_bf16 v[204:207], a[116:119], v[36:39], v[204:207]\n"
"v_exp_f32 v45, v45\n"
"v_mfma_f32_16x16x32_bf16 v[208:211], a[120:123], v[36:39], v[208:211]\n"
"ds_read_b128 a[48:51], v3 offset:4096\n"
"v_cvt_pk_bf16_f32 v33, v42, v43\n"
"v_mfma_f32_16x16x32_bf16 v[212:215], a[124:127], v[36:39], v[212:215]\n"
"ds_read_b128 a[52:55], v3 offset:4352\n"
"ds_read_b128 a[56:59], v4 offset:4096\n"
"v_mfma_f32_16x16x32_bf16 v[48:51], a[0:3], v[72:75], 0\n"
"s_add_u32 s76, s71, 8192\n"
"ds_read_b128 a[60:63], v4 offset:4352\n"
"v_exp_f32 v46, v46\n"
"v_mfma_f32_16x16x32_bf16 v[48:51], a[4:7], v[76:79], v[48:51]\n"
"s_add_u32 m0, s68, 122880\n"
"v_exp_f32 v47, v47\n"
"v_mfma_f32_16x16x32_bf16 v[48:51], a[8:11], v[80:83], v[48:51]\n"
"buffer_load_dwordx4 v10, s[88:91], s76 offen nt lds\n"
"v_cvt_pk_bf16_f32 v34, v44, v45\n"
"v_mfma_f32_16x16x32_bf16 v[48:51], a[12:15], v[84:87], v[48:51]\n"
"ds_read_b64_tr_b16 a[96:97], v7 offset:0\n"
"v_cvt_pk_bf16_f32 v35, v46, v47\n"
"v_mfma_f32_16x16x32_bf16 v[52:55], a[16:19], v[72:75], 0\n"
"ds_read_b64_tr_b16 a[98:99], v7 offset:4096\n"
"ds_read_b64_tr_b16 a[100:101], v8 offset:0\n"
"v_mfma_f32_16x16x32_bf16 v[52:55], a[20:23], v[76:79], v[52:55]\n"
"ds_read_b64_tr_b16 a[102:103], v8 offset:4096\n"
"ds_read_b64_tr_b16 a[104:105], v7 offset:256\n"
"v_mfma_f32_16x16x32_bf16 v[52:55], a[24:27], v[80:83], v[52:55]\n"
"ds_read_b64_tr_b16 a[106:107], v7 offset:4352\n"
"ds_read_b64_tr_b16 a[108:109], v8 offset:256\n"
"v_mfma_f32_16x16x32_bf16 v[52:55], a[28:31], v[84:87], v[52:55]\n"
"s_waitcnt lgkmcnt(15)\n"
"v_exp_f32 v48, v48\n"
"v_mfma_f32_16x16x32_bf16 v[120:123], a[64:67], v[32:35], v[120:123]\n"
"v_exp_f32 v49, v49\n"
"v_mfma_f32_16x16x32_bf16 v[124:127], a[68:71], v[32:35], v[124:127]\n"
"v_exp_f32 v50, v50\n"
"v_mfma_f32_16x16x32_bf16 v[128:131], a[72:75], v[32:35], v[128:131]\n"
"v_exp_f32 v51, v51\n"
"v_mfma_f32_16x16x32_bf16 v[132:135], a[76:79], v[32:35], v[132:135]\n"
"v_cvt_pk_bf16_f32 v36, v48, v49\n"
"v_add_f32 v16, v16, v48\n"
"v_mfma_f32_16x16x32_bf16 v[136:139], a[80:83], v[32:35], v[136:139]\n"
"v_exp_f32 v52, v52\n"
"v_mfma_f32_16x16x32_bf16 v[140:143], a[84:87], v[32:35], v[140:143]\n"
"v_exp_f32 v53, v53\n"
"v_mfma_f32_16x16x32_bf16 v[144:147], a[88:91], v[32:35], v[144:147]\n"
"s_add_u32 s77, s71, 12288\n"
"v_cvt_pk_bf16_f32 v37, v50, v51\n"
"v_add_f32 v16, v16, v49\n"
"v_mfma_f32_16x16x32_bf16 v[148:151], a[92:95], v[32:35], v[148:151]\n"
"s_add_u32 m0, s68, 126976\n"
"v_add_f32 v16, v16, v50\n"
"v_add_f32 v16, v16, v51\n"
"v_mfma_f32_16x16x32_bf16 v[24:27], v[20:23], v[32:35], v[24:27]\n"
"buffer_load_dwordx4 v10, s[88:91], s77 offen nt lds\n"
"ds_read_b64_tr_b16 a[110:111], v8 offset:4352\n"
"v_mfma_f32_16x16x32_bf16 v[40:43], a[0:3], v[88:91], 0\n"
"v_exp_f32 v54, v54\n"
"v_mfma_f32_16x16x32_bf16 v[40:43], a[4:7], v[92:95], v[40:43]\n"
"v_exp_f32 v55, v55\n"
"v_mfma_f32_16x16x32_bf16 v[40:43], a[8:11], v[96:99], v[40:43]\n"
"s_mov_b32 s70, 0\n"
"v_cvt_pk_bf16_f32 v38, v52, v53\n"
"v_add_f32 v16, v16, v52\n"
"v_mfma_f32_16x16x32_bf16 v[40:43], a[12:15], v[100:103], v[40:43]\n"
"v_cvt_pk_bf16_f32 v39, v54, v55\n"
"v_add_f32 v16, v16, v53\n"
"v_mfma_f32_16x16x32_bf16 v[44:47], a[16:19], v[88:91], 0\n"
"buffer_load_dword v0, v254, s[40:43], s70 offen\n"
"v_add_f32 v16, v16, v54\n"
"v_mfma_f32_16x16x32_bf16 v[44:47], a[20:23], v[92:95], v[44:47]\n"
"ds_read_b64_tr_b16 a[112:113], v250 offset:0\n"
"v_add_f32 v16, v16, v55\n"
"v_mfma_f32_16x16x32_bf16 v[44:47], a[24:27], v[96:99], v[44:47]\n"
"ds_read_b64_tr_b16 a[114:115], v250 offset:4096\n"
"ds_read_b64_tr_b16 a[116:117], v251 offset:0\n"
"v_mfma_f32_16x16x32_bf16 v[44:47], a[28:31], v[100:103], v[44:47]\n"
"v_exp_f32 v40, v40\n"
"v_mfma_f32_16x16x32_bf16 v[152:155], a[64:67], v[36:39], v[152:155]\n"
"v_exp_f32 v41, v41\n"
"v_mfma_f32_16x16x32_bf16 v[156:159], a[68:71], v[36:39], v[156:159]\n"
"v_exp_f32 v42, v42\n"
"v_mfma_f32_16x16x32_bf16 v[160:163], a[72:75], v[36:39], v[160:163]\n"
"v_exp_f32 v43, v43\n"
"v_mfma_f32_16x16x32_bf16 v[164:167], a[76:79], v[36:39], v[164:167]\n"
"v_cvt_pk_bf16_f32 v32, v40, v41\n"
"v_add_f32 v17, v17, v40\n"
"v_mfma_f32_16x16x32_bf16 v[168:171], a[80:83], v[36:39], v[168:171]\n"
"v_exp_f32 v44, v44\n"
"v_mfma_f32_16x16x32_bf16 v[172:175], a[84:87], v[36:39], v[172:175]\n"
"v_exp_f32 v45, v45\n"
"v_mfma_f32_16x16x32_bf16 v[176:179], a[88:91], v[36:39], v[176:179]\n"
"v_cvt_pk_bf16_f32 v33, v42, v43\n"
"v_add_f32 v17, v17, v41\n"
"v_mfma_f32_16x16x32_bf16 v[180:183], a[92:95], v[36:39], v[180:183]\n"
"v_add_f32 v17, v17, v42\n"
"v_add_f32 v17, v17, v43\n"
"v_mfma_f32_16x16x32_bf16 v[48:51], a[0:3], v[104:107], 0\n"
"v_exp_f32 v46, v46\n"
"v_mfma_f32_16x16x32_bf16 v[48:51], a[4:7], v[108:111], v[48:51]\n"
"v_exp_f32 v47, v47\n"
"v_mfma_f32_16x16x32_bf16 v[48:51], a[8:11], v[112:115], v[48:51]\n"
"v_cvt_pk_bf16_f32 v34, v44, v45\n"
"v_add_f32 v17, v17, v44\n"
"v_mfma_f32_16x16x32_bf16 v[48:51], a[12:15], v[116:119], v[48:51]\n"
"v_cvt_pk_bf16_f32 v35, v46, v47\n"
"v_add_f32 v17, v17, v45\n"
"v_mfma_f32_16x16x32_bf16 v[52:55], a[16:19], v[104:107], 0\n"
"ds_read_b64_tr_b16 a[118:119], v251 offset:4096\n"
"v_add_f32 v17, v17, v46\n"
"v_mfma_f32_16x16x32_bf16 v[52:55], a[20:23], v[108:111], v[52:55]\n"
"ds_read_b64_tr_b16 a[120:121], v250 offset:256\n"
"v_add_f32 v17, v17, v47\n"
"v_mfma_f32_16x16x32_bf16 v[52:55], a[24:27], v[112:115], v[52:55]\n"
"ds_read_b64_tr_b16 a[122:123], v250 offset:4352\n"
"ds_read_b64_tr_b16 a[124:125], v251 offset:256\n"
"v_mfma_f32_16x16x32_bf16 v[52:55], a[28:31], v[116:119], v[52:55]\n"
"v_exp_f32 v48, v48\n"
"v_mfma_f32_16x16x32_bf16 v[184:187], a[64:67], v[32:35], v[184:187]\n"
"v_exp_f32 v49, v49\n"
"v_mfma_f32_16x16x32_bf16 v[188:191], a[68:71], v[32:35], v[188:191]\n"
"v_exp_f32 v50, v50\n"
"v_mfma_f32_16x16x32_bf16 v[192:195], a[72:75], v[32:35], v[192:195]\n"
"v_exp_f32 v51, v51\n"
"v_mfma_f32_16x16x32_bf16 v[196:199], a[76:79], v[32:35], v[196:199]\n"
"ds_read_b64_tr_b16 a[126:127], v251 offset:4352\n"
"v_cvt_pk_bf16_f32 v36, v48, v49\n"
"v_mfma_f32_16x16x32_bf16 v[200:203], a[80:83], v[32:35], v[200:203]\n"
"v_exp_f32 v52, v52\n"
"v_mfma_f32_16x16x32_bf16 v[204:207], a[84:87], v[32:35], v[204:207]\n"
"v_exp_f32 v53, v53\n"
"v_mfma_f32_16x16x32_bf16 v[208:211], a[88:91], v[32:35], v[208:211]\n"
"v_cvt_pk_bf16_f32 v37, v50, v51\n"
"v_mfma_f32_16x16x32_bf16 v[212:215], a[92:95], v[32:35], v[212:215]\n"
"s_waitcnt vmcnt(17) lgkmcnt(0)\n"
"s_barrier\n"
"v_mfma_f32_16x16x32_bf16 v[40:43], a[32:35], v[56:59], 0\n"
"s_add_u32 s61, s75, 8\n"
"v_exp_f32 v54, v54\n"
"v_mfma_f32_16x16x32_bf16 v[40:43], a[36:39], v[60:63], v[40:43]\n"
"s_and_b32 s61, s61, 15\n"
"v_exp_f32 v55, v55\n"
"v_mfma_f32_16x16x32_bf16 v[40:43], a[40:43], v[64:67], v[40:43]\n"
"s_lshl_b32 s61, s61, 14\n"
"ds_read_b128 a[0:3], v1 offset:16384\n"
"v_cvt_pk_bf16_f32 v38, v52, v53\n"
"v_mfma_f32_16x16x32_bf16 v[40:43], a[44:47], v[68:71], v[40:43]\n"
"s_add_u32 s71, s68, s61\n"
"ds_read_b128 a[4:7], v1 offset:16640\n"
"v_cvt_pk_bf16_f32 v39, v54, v55\n"
"v_mfma_f32_16x16x32_bf16 v[44:47], a[48:51], v[56:59], 0\n"
"s_add_u32 s76, s71, 0\n"
"ds_read_b128 a[8:11], v2 offset:16384\n"
"v_mfma_f32_16x16x32_bf16 v[44:47], a[52:55], v[60:63], v[44:47]\n"
"s_add_u32 m0, s68, 0\n"
"ds_read_b128 a[12:15], v2 offset:16640\n"
"ds_read_b128 a[16:19], v1 offset:20480\n"
"v_mfma_f32_16x16x32_bf16 v[44:47], a[56:59], v[64:67], v[44:47]\n"
"buffer_load_dwordx4 v9, s[28:31], s76 offen nt lds\n"
"ds_read_b128 a[20:23], v1 offset:20736\n"
"v_mfma_f32_16x16x32_bf16 v[44:47], a[60:63], v[68:71], v[44:47]\n"
"v_exp_f32 v40, v40\n"
"v_mfma_f32_16x16x32_bf16 v[232:235], a[80:83], v[36:39], v[232:235]\n"
"v_exp_f32 v41, v41\n"
"v_mfma_f32_16x16x32_bf16 v[236:239], a[84:87], v[36:39], v[236:239]\n"
"v_exp_f32 v42, v42\n"
"v_mfma_f32_16x16x32_bf16 v[240:243], a[88:91], v[36:39], v[240:243]\n"
"v_exp_f32 v43, v43\n"
"v_mfma_f32_16x16x32_bf16 v[244:247], a[92:95], v[36:39], v[244:247]\n"
"v_cvt_pk_bf16_f32 v32, v40, v41\n"
"v_mfma_f32_16x16x32_bf16 v[216:219], a[64:67], v[36:39], v[216:219]\n"
"v_exp_f32 v44, v44\n"
"v_mfma_f32_16x16x32_bf16 v[220:223], a[68:71], v[36:39], v[220:223]\n"
"v_exp_f32 v45, v45\n"
"v_mfma_f32_16x16x32_bf16 v[224:227], a[72:75], v[36:39], v[224:227]\n"
"s_add_u32 s77, s71, 4096\n"
"ds_read_b128 a[24:27], v2 offset:20480\n"
"v_cvt_pk_bf16_f32 v33, v42, v43\n"
"v_mfma_f32_16x16x32_bf16 v[228:231], a[76:79], v[36:39], v[228:231]\n"
"s_add_u32 m0, s68, 4096\n"
"ds_read_b128 a[28:31], v2 offset:20736\n"
"v_mfma_f32_16x16x32_bf16 v[28:31], v[20:23], v[36:39], v[28:31]\n"
"buffer_load_dwordx4 v9, s[28:31], s77 offen nt lds\n"
"ds_read_b64_tr_b16 a[64:65], v5 offset:16384\n"
"v_mfma_f32_16x16x32_bf16 v[48:51], a[32:35], v[72:75], 0\n"
"v_exp_f32 v46, v46\n"
"v_mfma_f32_16x16x32_bf16 v[48:51], a[36:39], v[76:79], v[48:51]\n"
"v_exp_f32 v47, v47\n"
"v_mfma_f32_16x16x32_bf16 v[48:51], a[40:43], v[80:83], v[48:51]\n"
"ds_read_b64_tr_b16 a[66:67], v5 offset:20480\n"
"v_cvt_pk_bf16_f32 v34, v44, v45\n"
"v_mfma_f32_16x16x32_bf16 v[48:51], a[44:47], v[84:87], v[48:51]\n"
"ds_read_b64_tr_b16 a[68:69], v6 offset:16384\n"
"v_cvt_pk_bf16_f32 v35, v46, v47\n"
"v_mfma_f32_16x16x32_bf16 v[52:55], a[48:51], v[72:75], 0\n"
"ds_read_b64_tr_b16 a[70:71], v6 offset:20480\n"
"ds_read_b64_tr_b16 a[72:73], v5 offset:16640\n"
"v_mfma_f32_16x16x32_bf16 v[52:55], a[52:55], v[76:79], v[52:55]\n"
"ds_read_b64_tr_b16 a[74:75], v5 offset:20736\n"
"ds_read_b64_tr_b16 a[76:77], v6 offset:16640\n"
"v_mfma_f32_16x16x32_bf16 v[52:55], a[56:59], v[80:83], v[52:55]\n"
"ds_read_b64_tr_b16 a[78:79], v6 offset:20736\n"
"v_mfma_f32_16x16x32_bf16 v[52:55], a[60:63], v[84:87], v[52:55]\n"
"s_add_u32 s76, s71, 8192\n"
"v_exp_f32 v48, v48\n"
"v_mfma_f32_16x16x32_bf16 v[120:123], a[96:99], v[32:35], v[120:123]\n"
"s_add_u32 m0, s68, 8192\n"
"v_exp_f32 v49, v49\n"
"v_mfma_f32_16x16x32_bf16 v[124:127], a[100:103], v[32:35], v[124:127]\n"
"buffer_load_dwordx4 v9, s[28:31], s76 offen nt lds\n"
"v_exp_f32 v50, v50\n"
"v_mfma_f32_16x16x32_bf16 v[128:131], a[104:107], v[32:35], v[128:131]\n"
"v_exp_f32 v51, v51\n"
"v_mfma_f32_16x16x32_bf16 v[132:135], a[108:111], v[32:35], v[132:135]\n"
"v_cvt_pk_bf16_f32 v36, v48, v49\n"
"v_add_f32 v16, v16, v48\n"
"v_mfma_f32_16x16x32_bf16 v[136:139], a[112:115], v[32:35], v[136:139]\n"
"v_exp_f32 v52, v52\n"
"v_mfma_f32_16x16x32_bf16 v[140:143], a[116:119], v[32:35], v[140:143]\n"
"v_exp_f32 v53, v53\n"
"v_mfma_f32_16x16x32_bf16 v[144:147], a[120:123], v[32:35], v[144:147]\n"
"v_cvt_pk_bf16_f32 v37, v50, v51\n"
"v_add_f32 v16, v16, v49\n"
"v_mfma_f32_16x16x32_bf16 v[148:151], a[124:127], v[32:35], v[148:151]\n"
"v_add_f32 v16, v16, v50\n"
"v_add_f32 v16, v16, v51\n"
"v_mfma_f32_16x16x32_bf16 v[24:27], v[20:23], v[32:35], v[24:27]\n"
"ds_read_b64_tr_b16 a[80:81], v248 offset:16384\n"
"ds_read_b64_tr_b16 a[82:83], v248 offset:20480\n"
"v_mfma_f32_16x16x32_bf16 v[40:43], a[32:35], v[88:91], 0\n"
"v_exp_f32 v54, v54\n"
"v_mfma_f32_16x16x32_bf16 v[40:43], a[36:39], v[92:95], v[40:43]\n"
"v_exp_f32 v55, v55\n"
"v_mfma_f32_16x16x32_bf16 v[40:43], a[40:43], v[96:99], v[40:43]\n"
"s_add_u32 s77, s71, 12288\n"
"v_cvt_pk_bf16_f32 v38, v52, v53\n"
"v_add_f32 v16, v16, v52\n"
"v_mfma_f32_16x16x32_bf16 v[40:43], a[44:47], v[100:103], v[40:43]\n"
"s_add_u32 m0, s68, 12288\n"
"v_cvt_pk_bf16_f32 v39, v54, v55\n"
"v_add_f32 v16, v16, v53\n"
"v_mfma_f32_16x16x32_bf16 v[44:47], a[48:51], v[88:91], 0\n"
"buffer_load_dwordx4 v9, s[28:31], s77 offen nt lds\n"
"v_add_f32 v16, v16, v54\n"
"v_mfma_f32_16x16x32_bf16 v[44:47], a[52:55], v[92:95], v[44:47]\n"
"ds_read_b64_tr_b16 a[84:85], v249 offset:16384\n"
"v_add_f32 v16, v16, v55\n"
"v_mfma_f32_16x16x32_bf16 v[44:47], a[56:59], v[96:99], v[44:47]\n"
"ds_read_b64_tr_b16 a[86:87], v249 offset:20480\n"
"ds_read_b64_tr_b16 a[88:89], v248 offset:16640\n"
"v_mfma_f32_16x16x32_bf16 v[44:47], a[60:63], v[100:103], v[44:47]\n"
"v_exp_f32 v40, v40\n"
"v_mfma_f32_16x16x32_bf16 v[152:155], a[96:99], v[36:39], v[152:155]\n"
"v_exp_f32 v41, v41\n"
"v_mfma_f32_16x16x32_bf16 v[156:159], a[100:103], v[36:39], v[156:159]\n"
"v_exp_f32 v42, v42\n"
"v_mfma_f32_16x16x32_bf16 v[160:163], a[104:107], v[36:39], v[160:163]\n"
"s_add_u32 s76, s71, 0\n"
"v_exp_f32 v43, v43\n"
"v_mfma_f32_16x16x32_bf16 v[164:167], a[108:111], v[36:39], v[164:167]\n"
"s_add_u32 m0, s68, 65536\n"
"v_cvt_pk_bf16_f32 v32, v40, v41\n"
"v_add_f32 v17, v17, v40\n"
"v_mfma_f32_16x16x32_bf16 v[168:171], a[112:115], v[36:39], v[168:171]\n"
"buffer_load_dwordx4 v10, s[88:91], s76 offen nt lds\n"
"v_exp_f32 v44, v44\n"
"v_mfma_f32_16x16x32_bf16 v[172:175], a[116:119], v[36:39], v[172:175]\n"
"v_exp_f32 v45, v45\n"
"v_mfma_f32_16x16x32_bf16 v[176:179], a[120:123], v[36:39], v[176:179]\n"
"v_cvt_pk_bf16_f32 v33, v42, v43\n"
"v_add_f32 v17, v17, v41\n"
"v_mfma_f32_16x16x32_bf16 v[180:183], a[124:127], v[36:39], v[180:183]\n"
"s_waitcnt lgkmcnt(13)\n"
"v_add_f32 v17, v17, v42\n"
"v_add_f32 v17, v17, v43\n"
"v_mfma_f32_16x16x32_bf16 v[48:51], a[0:3], v[56:59], 0\n"
"v_exp_f32 v46, v46\n"
"v_mfma_f32_16x16x32_bf16 v[48:51], a[4:7], v[60:63], v[48:51]\n"
"v_exp_f32 v47, v47\n"
"v_mfma_f32_16x16x32_bf16 v[48:51], a[8:11], v[64:67], v[48:51]\n"
"v_cvt_pk_bf16_f32 v34, v44, v45\n"
"v_add_f32 v17, v17, v44\n"
"v_mfma_f32_16x16x32_bf16 v[48:51], a[12:15], v[68:71], v[48:51]\n"
"v_cvt_pk_bf16_f32 v35, v46, v47\n"
"v_add_f32 v17, v17, v45\n"
"v_mfma_f32_16x16x32_bf16 v[52:55], a[16:19], v[56:59], 0\n"
"s_add_u32 s77, s71, 4096\n"
"ds_read_b64_tr_b16 a[90:91], v248 offset:20736\n"
"v_add_f32 v17, v17, v46\n"
"v_mfma_f32_16x16x32_bf16 v[52:55], a[20:23], v[60:63], v[52:55]\n"
"s_add_u32 m0, s68, 69632\n"
"ds_read_b64_tr_b16 a[92:93], v249 offset:16640\n"
"v_add_f32 v17, v17, v47\n"
"v_mfma_f32_16x16x32_bf16 v[52:55], a[24:27], v[64:67], v[52:55]\n"
"buffer_load_dwordx4 v10, s[88:91], s77 offen nt lds\n"
"ds_read_b64_tr_b16 a[94:95], v249 offset:20736\n"
"v_mfma_f32_16x16x32_bf16 v[52:55], a[28:31], v[68:71], v[52:55]\n"
"ds_read_b128 a[32:35], v3 offset:16384\n"
"v_exp_f32 v48, v48\n"
"v_mfma_f32_16x16x32_bf16 v[184:187], a[96:99], v[32:35], v[184:187]\n"
"v_exp_f32 v49, v49\n"
"v_mfma_f32_16x16x32_bf16 v[188:191], a[100:103], v[32:35], v[188:191]\n"
"ds_read_b128 a[36:39], v3 offset:16640\n"
"v_exp_f32 v50, v50\n"
"v_mfma_f32_16x16x32_bf16 v[192:195], a[104:107], v[32:35], v[192:195]\n"
"v_exp_f32 v51, v51\n"
"v_mfma_f32_16x16x32_bf16 v[196:199], a[108:111], v[32:35], v[196:199]\n"
"ds_read_b128 a[40:43], v4 offset:16384\n"
"v_cvt_pk_bf16_f32 v36, v48, v49\n"
"v_mfma_f32_16x16x32_bf16 v[200:203], a[112:115], v[32:35], v[200:203]\n"
"ds_read_b128 a[44:47], v4 offset:16640\n"
"v_exp_f32 v52, v52\n"
"v_mfma_f32_16x16x32_bf16 v[204:207], a[116:119], v[32:35], v[204:207]\n"
"v_exp_f32 v53, v53\n"
"v_mfma_f32_16x16x32_bf16 v[208:211], a[120:123], v[32:35], v[208:211]\n"
"ds_read_b128 a[48:51], v3 offset:20480\n"
"v_cvt_pk_bf16_f32 v37, v50, v51\n"
"v_mfma_f32_16x16x32_bf16 v[212:215], a[124:127], v[32:35], v[212:215]\n"
"ds_read_b128 a[52:55], v3 offset:20736\n"
"ds_read_b128 a[56:59], v4 offset:20480\n"
"v_mfma_f32_16x16x32_bf16 v[40:43], a[0:3], v[72:75], 0\n"
"s_add_u32 s76, s71, 8192\n"
"ds_read_b128 a[60:63], v4 offset:20736\n"
"v_exp_f32 v54, v54\n"
"v_mfma_f32_16x16x32_bf16 v[40:43], a[4:7], v[76:79], v[40:43]\n"
"s_add_u32 m0, s68, 73728\n"
"v_exp_f32 v55, v55\n"
"v_mfma_f32_16x16x32_bf16 v[40:43], a[8:11], v[80:83], v[40:43]\n"
"buffer_load_dwordx4 v10, s[88:91], s76 offen nt lds\n"
"v_cvt_pk_bf16_f32 v38, v52, v53\n"
"v_mfma_f32_16x16x32_bf16 v[40:43], a[12:15], v[84:87], v[40:43]\n"
"ds_read_b64_tr_b16 a[96:97], v7 offset:16384\n"
"v_cvt_pk_bf16_f32 v39, v54, v55\n"
"v_mfma_f32_16x16x32_bf16 v[44:47], a[16:19], v[72:75], 0\n"
"ds_read_b64_tr_b16 a[98:99], v7 offset:20480\n"
"ds_read_b64_tr_b16 a[100:101], v8 offset:16384\n"
"v_mfma_f32_16x16x32_bf16 v[44:47], a[20:23], v[76:79], v[44:47]\n"
"ds_read_b64_tr_b16 a[102:103], v8 offset:20480\n"
"ds_read_b64_tr_b16 a[104:105], v7 offset:16640\n"
"v_mfma_f32_16x16x32_bf16 v[44:47], a[24:27], v[80:83], v[44:47]\n"
"ds_read_b64_tr_b16 a[106:107], v7 offset:20736\n"
"ds_read_b64_tr_b16 a[108:109], v8 offset:16640\n"
"v_mfma_f32_16x16x32_bf16 v[44:47], a[28:31], v[84:87], v[44:47]\n"
"s_waitcnt lgkmcnt(15)\n"
"v_exp_f32 v40, v40\n"
"v_mfma_f32_16x16x32_bf16 v[120:123], a[64:67], v[36:39], v[120:123]\n"
"v_exp_f32 v41, v41\n"
"v_mfma_f32_16x16x32_bf16 v[124:127], a[68:71], v[36:39], v[124:127]\n"
"v_exp_f32 v42, v42\n"
"v_mfma_f32_16x16x32_bf16 v[128:131], a[72:75], v[36:39], v[128:131]\n"
"v_exp_f32 v43, v43\n"
"v_mfma_f32_16x16x32_bf16 v[132:135], a[76:79], v[36:39], v[132:135]\n"
"v_cvt_pk_bf16_f32 v32, v40, v41\n"
"v_add_f32 v16, v16, v40\n"
"v_mfma_f32_16x16x32_bf16 v[136:139], a[80:83], v[36:39], v[136:139]\n"
"v_exp_f32 v44, v44\n"
"v_mfma_f32_16x16x32_bf16 v[140:143], a[84:87], v[36:39], v[140:143]\n"
"v_exp_f32 v45, v45\n"
"v_mfma_f32_16x16x32_bf16 v[144:147], a[88:91], v[36:39], v[144:147]\n"
"s_add_u32 s77, s71, 12288\n"
"v_cvt_pk_bf16_f32 v33, v42, v43\n"
"v_add_f32 v16, v16, v41\n"
"v_mfma_f32_16x16x32_bf16 v[148:151], a[92:95], v[36:39], v[148:151]\n"
"s_add_u32 m0, s68, 77824\n"
"v_add_f32 v16, v16, v42\n"
"v_add_f32 v16, v16, v43\n"
"v_mfma_f32_16x16x32_bf16 v[24:27], v[20:23], v[36:39], v[24:27]\n"
"buffer_load_dwordx4 v10, s[88:91], s77 offen nt lds\n"
"ds_read_b64_tr_b16 a[110:111], v8 offset:20736\n"
"v_mfma_f32_16x16x32_bf16 v[48:51], a[0:3], v[88:91], 0\n"
"v_exp_f32 v46, v46\n"
"v_mfma_f32_16x16x32_bf16 v[48:51], a[4:7], v[92:95], v[48:51]\n"
"v_exp_f32 v47, v47\n"
"v_mfma_f32_16x16x32_bf16 v[48:51], a[8:11], v[96:99], v[48:51]\n"
"s_mov_b32 s70, 524288\n"
"v_cvt_pk_bf16_f32 v34, v44, v45\n"
"v_add_f32 v16, v16, v44\n"
"v_mfma_f32_16x16x32_bf16 v[48:51], a[12:15], v[100:103], v[48:51]\n"
"v_cvt_pk_bf16_f32 v35, v46, v47\n"
"v_add_f32 v16, v16, v45\n"
"v_mfma_f32_16x16x32_bf16 v[52:55], a[16:19], v[88:91], 0\n"
"buffer_load_dword v0, v254, s[40:43], s70 offen\n"
"v_add_f32 v16, v16, v46\n"
"v_mfma_f32_16x16x32_bf16 v[52:55], a[20:23], v[92:95], v[52:55]\n"
"ds_read_b64_tr_b16 a[112:113], v250 offset:16384\n"
"v_add_f32 v16, v16, v47\n"
"v_mfma_f32_16x16x32_bf16 v[52:55], a[24:27], v[96:99], v[52:55]\n"
"ds_read_b64_tr_b16 a[114:115], v250 offset:20480\n"
"ds_read_b64_tr_b16 a[116:117], v251 offset:16384\n"
"v_mfma_f32_16x16x32_bf16 v[52:55], a[28:31], v[100:103], v[52:55]\n"
"v_exp_f32 v48, v48\n"
"v_mfma_f32_16x16x32_bf16 v[152:155], a[64:67], v[32:35], v[152:155]\n"
"v_exp_f32 v49, v49\n"
"v_mfma_f32_16x16x32_bf16 v[156:159], a[68:71], v[32:35], v[156:159]\n"
"v_exp_f32 v50, v50\n"
"v_mfma_f32_16x16x32_bf16 v[160:163], a[72:75], v[32:35], v[160:163]\n"
"v_exp_f32 v51, v51\n"
"v_mfma_f32_16x16x32_bf16 v[164:167], a[76:79], v[32:35], v[164:167]\n"
"v_cvt_pk_bf16_f32 v36, v48, v49\n"
"v_add_f32 v17, v17, v48\n"
"v_mfma_f32_16x16x32_bf16 v[168:171], a[80:83], v[32:35], v[168:171]\n"
"v_exp_f32 v52, v52\n"
"v_mfma_f32_16x16x32_bf16 v[172:175], a[84:87], v[32:35], v[172:175]\n"
"v_exp_f32 v53, v53\n"
"v_mfma_f32_16x16x32_bf16 v[176:179], a[88:91], v[32:35], v[176:179]\n"
"v_cvt_pk_bf16_f32 v37, v50, v51\n"
"v_add_f32 v17, v17, v49\n"
"v_mfma_f32_16x16x32_bf16 v[180:183], a[92:95], v[32:35], v[180:183]\n"
"v_add_f32 v17, v17, v50\n"
"v_add_f32 v17, v17, v51\n"
"v_mfma_f32_16x16x32_bf16 v[40:43], a[0:3], v[104:107], 0\n"
"v_exp_f32 v54, v54\n"
"v_mfma_f32_16x16x32_bf16 v[40:43], a[4:7], v[108:111], v[40:43]\n"
"v_exp_f32 v55, v55\n"
"v_mfma_f32_16x16x32_bf16 v[40:43], a[8:11], v[112:115], v[40:43]\n"
"v_cvt_pk_bf16_f32 v38, v52, v53\n"
"v_add_f32 v17, v17, v52\n"
"v_mfma_f32_16x16x32_bf16 v[40:43], a[12:15], v[116:119], v[40:43]\n"
"v_cvt_pk_bf16_f32 v39, v54, v55\n"
"v_add_f32 v17, v17, v53\n"
"v_mfma_f32_16x16x32_bf16 v[44:47], a[16:19], v[104:107], 0\n"
"ds_read_b64_tr_b16 a[118:119], v251 offset:20480\n"
"v_add_f32 v17, v17, v54\n"
"v_mfma_f32_16x16x32_bf16 v[44:47], a[20:23], v[108:111], v[44:47]\n"
"ds_read_b64_tr_b16 a[120:121], v250 offset:16640\n"
"v_add_f32 v17, v17, v55\n"
"v_mfma_f32_16x16x32_bf16 v[44:47], a[24:27], v[112:115], v[44:47]\n"
"ds_read_b64_tr_b16 a[122:123], v250 offset:20736\n"
"ds_read_b64_tr_b16 a[124:125], v251 offset:16640\n"
"v_mfma_f32_16x16x32_bf16 v[44:47], a[28:31], v[116:119], v[44:47]\n"
"v_exp_f32 v40, v40\n"
"v_mfma_f32_16x16x32_bf16 v[184:187], a[64:67], v[36:39], v[184:187]\n"
"v_exp_f32 v41, v41\n"
"v_mfma_f32_16x16x32_bf16 v[188:191], a[68:71], v[36:39], v[188:191]\n"
"v_exp_f32 v42, v42\n"
"v_mfma_f32_16x16x32_bf16 v[192:195], a[72:75], v[36:39], v[192:195]\n"
"v_exp_f32 v43, v43\n"
"v_mfma_f32_16x16x32_bf16 v[196:199], a[76:79], v[36:39], v[196:199]\n"
"ds_read_b64_tr_b16 a[126:127], v251 offset:20736\n"
"v_cvt_pk_bf16_f32 v32, v40, v41\n"
"v_mfma_f32_16x16x32_bf16 v[200:203], a[80:83], v[36:39], v[200:203]\n"
"v_exp_f32 v44, v44\n"
"v_mfma_f32_16x16x32_bf16 v[204:207], a[84:87], v[36:39], v[204:207]\n"
"v_exp_f32 v45, v45\n"
"v_mfma_f32_16x16x32_bf16 v[208:211], a[88:91], v[36:39], v[208:211]\n"
"v_cvt_pk_bf16_f32 v33, v42, v43\n"
"v_mfma_f32_16x16x32_bf16 v[212:215], a[92:95], v[36:39], v[212:215]\n"
"s_waitcnt vmcnt(18) lgkmcnt(0)\n"
"s_barrier\n"
"v_mfma_f32_16x16x32_bf16 v[48:51], a[32:35], v[56:59], 0\n"
"s_add_u32 s61, s75, 9\n"
"v_exp_f32 v46, v46\n"
"v_mfma_f32_16x16x32_bf16 v[48:51], a[36:39], v[60:63], v[48:51]\n"
"s_and_b32 s61, s61, 15\n"
"v_exp_f32 v47, v47\n"
"v_mfma_f32_16x16x32_bf16 v[48:51], a[40:43], v[64:67], v[48:51]\n"
"s_lshl_b32 s61, s61, 14\n"
"ds_read_b128 a[0:3], v1 offset:32768\n"
"v_cvt_pk_bf16_f32 v34, v44, v45\n"
"v_mfma_f32_16x16x32_bf16 v[48:51], a[44:47], v[68:71], v[48:51]\n"
"s_add_u32 s71, s68, s61\n"
"ds_read_b128 a[4:7], v1 offset:33024\n"
"v_cvt_pk_bf16_f32 v35, v46, v47\n"
"v_mfma_f32_16x16x32_bf16 v[52:55], a[48:51], v[56:59], 0\n"
"s_add_u32 s76, s71, 0\n"
"ds_read_b128 a[8:11], v2 offset:32768\n"
"v_mfma_f32_16x16x32_bf16 v[52:55], a[52:55], v[60:63], v[52:55]\n"
"s_add_u32 m0, s68, 16384\n"
"ds_read_b128 a[12:15], v2 offset:33024\n"
"ds_read_b128 a[16:19], v1 offset:36864\n"
"v_mfma_f32_16x16x32_bf16 v[52:55], a[56:59], v[64:67], v[52:55]\n"
"buffer_load_dwordx4 v9, s[28:31], s76 offen nt lds\n"
"ds_read_b128 a[20:23], v1 offset:37120\n"
"v_mfma_f32_16x16x32_bf16 v[52:55], a[60:63], v[68:71], v[52:55]\n"
"v_exp_f32 v48, v48\n"
"v_mfma_f32_16x16x32_bf16 v[232:235], a[80:83], v[32:35], v[232:235]\n"
"v_exp_f32 v49, v49\n"
"v_mfma_f32_16x16x32_bf16 v[236:239], a[84:87], v[32:35], v[236:239]\n"
"v_exp_f32 v50, v50\n"
"v_mfma_f32_16x16x32_bf16 v[240:243], a[88:91], v[32:35], v[240:243]\n"
"v_exp_f32 v51, v51\n"
"v_mfma_f32_16x16x32_bf16 v[244:247], a[92:95], v[32:35], v[244:247]\n"
"v_cvt_pk_bf16_f32 v36, v48, v49\n"
"v_mfma_f32_16x16x32_bf16 v[216:219], a[64:67], v[32:35], v[216:219]\n"
"v_exp_f32 v52, v52\n"
"v_mfma_f32_16x16x32_bf16 v[220:223], a[68:71], v[32:35], v[220:223]\n"
"v_exp_f32 v53, v53\n"
"v_mfma_f32_16x16x32_bf16 v[224:227], a[72:75], v[32:35], v[224:227]\n"
"s_add_u32 s77, s71, 4096\n"
"ds_read_b128 a[24:27], v2 offset:36864\n"
"v_cvt_pk_bf16_f32 v37, v50, v51\n"
"v_mfma_f32_16x16x32_bf16 v[228:231], a[76:79], v[32:35], v[228:231]\n"
"s_add_u32 m0, s68, 20480\n"
"ds_read_b128 a[28:31], v2 offset:37120\n"
"v_mfma_f32_16x16x32_bf16 v[28:31], v[20:23], v[32:35], v[28:31]\n"
"buffer_load_dwordx4 v9, s[28:31], s77 offen nt lds\n"
"ds_read_b64_tr_b16 a[64:65], v5 offset:32768\n"
"v_mfma_f32_16x16x32_bf16 v[40:43], a[32:35], v[72:75], 0\n"
"v_exp_f32 v54, v54\n"
"v_mfma_f32_16x16x32_bf16 v[40:43], a[36:39], v[76:79], v[40:43]\n"
"v_exp_f32 v55, v55\n"
"v_mfma_f32_16x16x32_bf16 v[40:43], a[40:43], v[80:83], v[40:43]\n"
"ds_read_b64_tr_b16 a[66:67], v5 offset:36864\n"
"v_cvt_pk_bf16_f32 v38, v52, v53\n"
"v_mfma_f32_16x16x32_bf16 v[40:43], a[44:47], v[84:87], v[40:43]\n"
"ds_read_b64_tr_b16 a[68:69], v6 offset:32768\n"
"v_cvt_pk_bf16_f32 v39, v54, v55\n"
"v_mfma_f32_16x16x32_bf16 v[44:47], a[48:51], v[72:75], 0\n"
"ds_read_b64_tr_b16 a[70:71], v6 offset:36864\n"
"ds_read_b64_tr_b16 a[72:73], v5 offset:33024\n"
"v_mfma_f32_16x16x32_bf16 v[44:47], a[52:55], v[76:79], v[44:47]\n"
"ds_read_b64_tr_b16 a[74:75], v5 offset:37120\n"
"ds_read_b64_tr_b16 a[76:77], v6 offset:33024\n"
"v_mfma_f32_16x16x32_bf16 v[44:47], a[56:59], v[80:83], v[44:47]\n"
"ds_read_b64_tr_b16 a[78:79], v6 offset:37120\n"
"v_mfma_f32_16x16x32_bf16 v[44:47], a[60:63], v[84:87], v[44:47]\n"
"s_add_u32 s76, s71, 8192\n"
"v_exp_f32 v40, v40\n"
"v_mfma_f32_16x16x32_bf16 v[120:123], a[96:99], v[36:39], v[120:123]\n"
"s_add_u32 m0, s68, 24576\n"
"v_exp_f32 v41, v41\n"
"v_mfma_f32_16x16x32_bf16 v[124:127], a[100:103], v[36:39], v[124:127]\n"
"buffer_load_dwordx4 v9, s[28:31], s76 offen nt lds\n"
"v_exp_f32 v42, v42\n"
"v_mfma_f32_16x16x32_bf16 v[128:131], a[104:107], v[36:39], v[128:131]\n"
"v_exp_f32 v43, v43\n"
"v_mfma_f32_16x16x32_bf16 v[132:135], a[108:111], v[36:39], v[132:135]\n"
"v_cvt_pk_bf16_f32 v32, v40, v41\n"
"v_add_f32 v16, v16, v40\n"
"v_mfma_f32_16x16x32_bf16 v[136:139], a[112:115], v[36:39], v[136:139]\n"
"v_exp_f32 v44, v44\n"
"v_mfma_f32_16x16x32_bf16 v[140:143], a[116:119], v[36:39], v[140:143]\n"
"v_exp_f32 v45, v45\n"
"v_mfma_f32_16x16x32_bf16 v[144:147], a[120:123], v[36:39], v[144:147]\n"
"v_cvt_pk_bf16_f32 v33, v42, v43\n"
"v_add_f32 v16, v16, v41\n"
"v_mfma_f32_16x16x32_bf16 v[148:151], a[124:127], v[36:39], v[148:151]\n"
"v_add_f32 v16, v16, v42\n"
"v_add_f32 v16, v16, v43\n"
"v_mfma_f32_16x16x32_bf16 v[24:27], v[20:23], v[36:39], v[24:27]\n"
"ds_read_b64_tr_b16 a[80:81], v248 offset:32768\n"
"ds_read_b64_tr_b16 a[82:83], v248 offset:36864\n"
"v_mfma_f32_16x16x32_bf16 v[48:51], a[32:35], v[88:91], 0\n"
"v_exp_f32 v46, v46\n"
"v_mfma_f32_16x16x32_bf16 v[48:51], a[36:39], v[92:95], v[48:51]\n"
"v_exp_f32 v47, v47\n"
"v_mfma_f32_16x16x32_bf16 v[48:51], a[40:43], v[96:99], v[48:51]\n"
"s_add_u32 s77, s71, 12288\n"
"v_cvt_pk_bf16_f32 v34, v44, v45\n"
"v_add_f32 v16, v16, v44\n"
"v_mfma_f32_16x16x32_bf16 v[48:51], a[44:47], v[100:103], v[48:51]\n"
"s_add_u32 m0, s68, 28672\n"
"v_cvt_pk_bf16_f32 v35, v46, v47\n"
"v_add_f32 v16, v16, v45\n"
"v_mfma_f32_16x16x32_bf16 v[52:55], a[48:51], v[88:91], 0\n"
"buffer_load_dwordx4 v9, s[28:31], s77 offen nt lds\n"
"v_add_f32 v16, v16, v46\n"
"v_mfma_f32_16x16x32_bf16 v[52:55], a[52:55], v[92:95], v[52:55]\n"
"ds_read_b64_tr_b16 a[84:85], v249 offset:32768\n"
"v_add_f32 v16, v16, v47\n"
"v_mfma_f32_16x16x32_bf16 v[52:55], a[56:59], v[96:99], v[52:55]\n"
"ds_read_b64_tr_b16 a[86:87], v249 offset:36864\n"
"ds_read_b64_tr_b16 a[88:89], v248 offset:33024\n"
"v_mfma_f32_16x16x32_bf16 v[52:55], a[60:63], v[100:103], v[52:55]\n"
"v_exp_f32 v48, v48\n"
"v_mfma_f32_16x16x32_bf16 v[152:155], a[96:99], v[32:35], v[152:155]\n"
"v_exp_f32 v49, v49\n"
"v_mfma_f32_16x16x32_bf16 v[156:159], a[100:103], v[32:35], v[156:159]\n"
"v_exp_f32 v50, v50\n"
"v_mfma_f32_16x16x32_bf16 v[160:163], a[104:107], v[32:35], v[160:163]\n"
"s_add_u32 s76, s71, 0\n"
"v_exp_f32 v51, v51\n"
"v_mfma_f32_16x16x32_bf16 v[164:167], a[108:111], v[32:35], v[164:167]\n"
"s_add_u32 m0, s68, 81920\n"
"v_cvt_pk_bf16_f32 v36, v48, v49\n"
"v_add_f32 v17, v17, v48\n"
"v_mfma_f32_16x16x32_bf16 v[168:171], a[112:115], v[32:35], v[168:171]\n"
"buffer_load_dwordx4 v10, s[88:91], s76 offen nt lds\n"
"v_exp_f32 v52, v52\n"
"v_mfma_f32_16x16x32_bf16 v[172:175], a[116:119], v[32:35], v[172:175]\n"
"v_exp_f32 v53, v53\n"
"v_mfma_f32_16x16x32_bf16 v[176:179], a[120:123], v[32:35], v[176:179]\n"
"v_cvt_pk_bf16_f32 v37, v50, v51\n"
"v_add_f32 v17, v17, v49\n"
"v_mfma_f32_16x16x32_bf16 v[180:183], a[124:127], v[32:35], v[180:183]\n"
"s_waitcnt lgkmcnt(13)\n"
"v_add_f32 v17, v17, v50\n"
"v_add_f32 v17, v17, v51\n"
"v_mfma_f32_16x16x32_bf16 v[40:43], a[0:3], v[56:59], 0\n"
"v_exp_f32 v54, v54\n"
"v_mfma_f32_16x16x32_bf16 v[40:43], a[4:7], v[60:63], v[40:43]\n"
"v_exp_f32 v55, v55\n"
"v_mfma_f32_16x16x32_bf16 v[40:43], a[8:11], v[64:67], v[40:43]\n"
"v_cvt_pk_bf16_f32 v38, v52, v53\n"
"v_add_f32 v17, v17, v52\n"
"v_mfma_f32_16x16x32_bf16 v[40:43], a[12:15], v[68:71], v[40:43]\n"
"v_cvt_pk_bf16_f32 v39, v54, v55\n"
"v_add_f32 v17, v17, v53\n"
"v_mfma_f32_16x16x32_bf16 v[44:47], a[16:19], v[56:59], 0\n"
"s_add_u32 s77, s71, 4096\n"
"ds_read_b64_tr_b16 a[90:91], v248 offset:37120\n"
"v_add_f32 v17, v17, v54\n"
"v_mfma_f32_16x16x32_bf16 v[44:47], a[20:23], v[60:63], v[44:47]\n"
"s_add_u32 m0, s68, 86016\n"
"ds_read_b64_tr_b16 a[92:93], v249 offset:33024\n"
"v_add_f32 v17, v17, v55\n"
"v_mfma_f32_16x16x32_bf16 v[44:47], a[24:27], v[64:67], v[44:47]\n"
"buffer_load_dwordx4 v10, s[88:91], s77 offen nt lds\n"
"ds_read_b64_tr_b16 a[94:95], v249 offset:37120\n"
"v_mfma_f32_16x16x32_bf16 v[44:47], a[28:31], v[68:71], v[44:47]\n"
"ds_read_b128 a[32:35], v3 offset:32768\n"
"v_exp_f32 v40, v40\n"
"v_mfma_f32_16x16x32_bf16 v[184:187], a[96:99], v[36:39], v[184:187]\n"
"v_exp_f32 v41, v41\n"
"v_mfma_f32_16x16x32_bf16 v[188:191], a[100:103], v[36:39], v[188:191]\n"
"ds_read_b128 a[36:39], v3 offset:33024\n"
"v_exp_f32 v42, v42\n"
"v_mfma_f32_16x16x32_bf16 v[192:195], a[104:107], v[36:39], v[192:195]\n"
"v_exp_f32 v43, v43\n"
"v_mfma_f32_16x16x32_bf16 v[196:199], a[108:111], v[36:39], v[196:199]\n"
"ds_read_b128 a[40:43], v4 offset:32768\n"
"v_cvt_pk_bf16_f32 v32, v40, v41\n"
"v_mfma_f32_16x16x32_bf16 v[200:203], a[112:115], v[36:39], v[200:203]\n"
"ds_read_b128 a[44:47], v4 offset:33024\n"
"v_exp_f32 v44, v44\n"
"v_mfma_f32_16x16x32_bf16 v[204:207], a[116:119], v[36:39], v[204:207]\n"
"v_exp_f32 v45, v45\n"
"v_mfma_f32_16x16x32_bf16 v[208:211], a[120:123], v[36:39], v[208:211]\n"
"ds_read_b128 a[48:51], v3 offset:36864\n"
"v_cvt_pk_bf16_f32 v33, v42, v43\n"
"v_mfma_f32_16x16x32_bf16 v[212:215], a[124:127], v[36:39], v[212:215]\n"
"ds_read_b128 a[52:55], v3 offset:37120\n"
"ds_read_b128 a[56:59], v4 offset:36864\n"
"v_mfma_f32_16x16x32_bf16 v[48:51], a[0:3], v[72:75], 0\n"
"s_add_u32 s76, s71, 8192\n"
"ds_read_b128 a[60:63], v4 offset:37120\n"
"v_exp_f32 v46, v46\n"
"v_mfma_f32_16x16x32_bf16 v[48:51], a[4:7], v[76:79], v[48:51]\n"
"s_add_u32 m0, s68, 90112\n"
"v_exp_f32 v47, v47\n"
"v_mfma_f32_16x16x32_bf16 v[48:51], a[8:11], v[80:83], v[48:51]\n"
"buffer_load_dwordx4 v10, s[88:91], s76 offen nt lds\n"
"v_cvt_pk_bf16_f32 v34, v44, v45\n"
"v_mfma_f32_16x16x32_bf16 v[48:51], a[12:15], v[84:87], v[48:51]\n"
"ds_read_b64_tr_b16 a[96:97], v7 offset:32768\n"
"v_cvt_pk_bf16_f32 v35, v46, v47\n"
"v_mfma_f32_16x16x32_bf16 v[52:55], a[16:19], v[72:75], 0\n"
"ds_read_b64_tr_b16 a[98:99], v7 offset:36864\n"
"ds_read_b64_tr_b16 a[100:101], v8 offset:32768\n"
"v_mfma_f32_16x16x32_bf16 v[52:55], a[20:23], v[76:79], v[52:55]\n"
"ds_read_b64_tr_b16 a[102:103], v8 offset:36864\n"
"ds_read_b64_tr_b16 a[104:105], v7 offset:33024\n"
"v_mfma_f32_16x16x32_bf16 v[52:55], a[24:27], v[80:83], v[52:55]\n"
"ds_read_b64_tr_b16 a[106:107], v7 offset:37120\n"
"ds_read_b64_tr_b16 a[108:109], v8 offset:33024\n"
"v_mfma_f32_16x16x32_bf16 v[52:55], a[28:31], v[84:87], v[52:55]\n"
"s_waitcnt lgkmcnt(15)\n"
"v_exp_f32 v48, v48\n"
"v_mfma_f32_16x16x32_bf16 v[120:123], a[64:67], v[32:35], v[120:123]\n"
"v_exp_f32 v49, v49\n"
"v_mfma_f32_16x16x32_bf16 v[124:127], a[68:71], v[32:35], v[124:127]\n"
"v_exp_f32 v50, v50\n"
"v_mfma_f32_16x16x32_bf16 v[128:131], a[72:75], v[32:35], v[128:131]\n"
"v_exp_f32 v51, v51\n"
"v_mfma_f32_16x16x32_bf16 v[132:135], a[76:79], v[32:35], v[132:135]\n"
"v_cvt_pk_bf16_f32 v36, v48, v49\n"
"v_add_f32 v16, v16, v48\n"
"v_mfma_f32_16x16x32_bf16 v[136:139], a[80:83], v[32:35], v[136:139]\n"
"v_exp_f32 v52, v52\n"
"v_mfma_f32_16x16x32_bf16 v[140:143], a[84:87], v[32:35], v[140:143]\n"
"v_exp_f32 v53, v53\n"
"v_mfma_f32_16x16x32_bf16 v[144:147], a[88:91], v[32:35], v[144:147]\n"
"s_add_u32 s77, s71, 12288\n"
"v_cvt_pk_bf16_f32 v37, v50, v51\n"
"v_add_f32 v16, v16, v49\n"
"v_mfma_f32_16x16x32_bf16 v[148:151], a[92:95], v[32:35], v[148:151]\n"
"s_add_u32 m0, s68, 94208\n"
"v_add_f32 v16, v16, v50\n"
"v_add_f32 v16, v16, v51\n"
"v_mfma_f32_16x16x32_bf16 v[24:27], v[20:23], v[32:35], v[24:27]\n"
"buffer_load_dwordx4 v10, s[88:91], s77 offen nt lds\n"
"ds_read_b64_tr_b16 a[110:111], v8 offset:37120\n"
"v_mfma_f32_16x16x32_bf16 v[40:43], a[0:3], v[88:91], 0\n"
"v_exp_f32 v54, v54\n"
"v_mfma_f32_16x16x32_bf16 v[40:43], a[4:7], v[92:95], v[40:43]\n"
"v_exp_f32 v55, v55\n"
"v_mfma_f32_16x16x32_bf16 v[40:43], a[8:11], v[96:99], v[40:43]\n"
"s_mov_b32 s70, 1048576\n"
"v_cvt_pk_bf16_f32 v38, v52, v53\n"
"v_add_f32 v16, v16, v52\n"
"v_mfma_f32_16x16x32_bf16 v[40:43], a[12:15], v[100:103], v[40:43]\n"
"v_cvt_pk_bf16_f32 v39, v54, v55\n"
"v_add_f32 v16, v16, v53\n"
"v_mfma_f32_16x16x32_bf16 v[44:47], a[16:19], v[88:91], 0\n"
"buffer_load_dword v0, v254, s[40:43], s70 offen\n"
"v_add_f32 v16, v16, v54\n"
"v_mfma_f32_16x16x32_bf16 v[44:47], a[20:23], v[92:95], v[44:47]\n"
"ds_read_b64_tr_b16 a[112:113], v250 offset:32768\n"
"v_add_f32 v16, v16, v55\n"
"v_mfma_f32_16x16x32_bf16 v[44:47], a[24:27], v[96:99], v[44:47]\n"
"ds_read_b64_tr_b16 a[114:115], v250 offset:36864\n"
"ds_read_b64_tr_b16 a[116:117], v251 offset:32768\n"
"v_mfma_f32_16x16x32_bf16 v[44:47], a[28:31], v[100:103], v[44:47]\n"
"v_exp_f32 v40, v40\n"
"v_mfma_f32_16x16x32_bf16 v[152:155], a[64:67], v[36:39], v[152:155]\n"
"v_exp_f32 v41, v41\n"
"v_mfma_f32_16x16x32_bf16 v[156:159], a[68:71], v[36:39], v[156:159]\n"
"v_exp_f32 v42, v42\n"
"v_mfma_f32_16x16x32_bf16 v[160:163], a[72:75], v[36:39], v[160:163]\n"
"v_exp_f32 v43, v43\n"
"v_mfma_f32_16x16x32_bf16 v[164:167], a[76:79], v[36:39], v[164:167]\n"
"v_cvt_pk_bf16_f32 v32, v40, v41\n"
"v_add_f32 v17, v17, v40\n"
"v_mfma_f32_16x16x32_bf16 v[168:171], a[80:83], v[36:39], v[168:171]\n"
"v_exp_f32 v44, v44\n"
"v_mfma_f32_16x16x32_bf16 v[172:175], a[84:87], v[36:39], v[172:175]\n"
"v_exp_f32 v45, v45\n"
"v_mfma_f32_16x16x32_bf16 v[176:179], a[88:91], v[36:39], v[176:179]\n"
"v_cvt_pk_bf16_f32 v33, v42, v43\n"
"v_add_f32 v17, v17, v41\n"
"v_mfma_f32_16x16x32_bf16 v[180:183], a[92:95], v[36:39], v[180:183]\n"
"v_add_f32 v17, v17, v42\n"
"v_add_f32 v17, v17, v43\n"
"v_mfma_f32_16x16x32_bf16 v[48:51], a[0:3], v[104:107], 0\n"
"v_exp_f32 v46, v46\n"
"v_mfma_f32_16x16x32_bf16 v[48:51], a[4:7], v[108:111], v[48:51]\n"
"v_exp_f32 v47, v47\n"
"v_mfma_f32_16x16x32_bf16 v[48:51], a[8:11], v[112:115], v[48:51]\n"
"v_cvt_pk_bf16_f32 v34, v44, v45\n"
"v_add_f32 v17, v17, v44\n"
"v_mfma_f32_16x16x32_bf16 v[48:51], a[12:15], v[116:119], v[48:51]\n"
"v_cvt_pk_bf16_f32 v35, v46, v47\n"
"v_add_f32 v17, v17, v45\n"
"v_mfma_f32_16x16x32_bf16 v[52:55], a[16:19], v[104:107], 0\n"
"ds_read_b64_tr_b16 a[118:119], v251 offset:36864\n"
"v_add_f32 v17, v17, v46\n"
"v_mfma_f32_16x16x32_bf16 v[52:55], a[20:23], v[108:111], v[52:55]\n"
"ds_read_b64_tr_b16 a[120:121], v250 offset:33024\n"
"v_add_f32 v17, v17, v47\n"
"v_mfma_f32_16x16x32_bf16 v[52:55], a[24:27], v[112:115], v[52:55]\n"
"ds_read_b64_tr_b16 a[122:123], v250 offset:37120\n"
"ds_read_b64_tr_b16 a[124:125], v251 offset:33024\n"
"v_mfma_f32_16x16x32_bf16 v[52:55], a[28:31], v[116:119], v[52:55]\n"
"v_exp_f32 v48, v48\n"
"v_mfma_f32_16x16x32_bf16 v[184:187], a[64:67], v[32:35], v[184:187]\n"
"v_exp_f32 v49, v49\n"
"v_mfma_f32_16x16x32_bf16 v[188:191], a[68:71], v[32:35], v[188:191]\n"
"v_exp_f32 v50, v50\n"
"v_mfma_f32_16x16x32_bf16 v[192:195], a[72:75], v[32:35], v[192:195]\n"
"v_exp_f32 v51, v51\n"
"v_mfma_f32_16x16x32_bf16 v[196:199], a[76:79], v[32:35], v[196:199]\n"
"ds_read_b64_tr_b16 a[126:127], v251 offset:37120\n"
"v_cvt_pk_bf16_f32 v36, v48, v49\n"
"v_mfma_f32_16x16x32_bf16 v[200:203], a[80:83], v[32:35], v[200:203]\n"
"v_exp_f32 v52, v52\n"
"v_mfma_f32_16x16x32_bf16 v[204:207], a[84:87], v[32:35], v[204:207]\n"
"v_exp_f32 v53, v53\n"
"v_mfma_f32_16x16x32_bf16 v[208:211], a[88:91], v[32:35], v[208:211]\n"
"v_cvt_pk_bf16_f32 v37, v50, v51\n"
"v_mfma_f32_16x16x32_bf16 v[212:215], a[92:95], v[32:35], v[212:215]\n"
"s_waitcnt vmcnt(19) lgkmcnt(0)\n"
"s_barrier\n"
"v_mfma_f32_16x16x32_bf16 v[40:43], a[32:35], v[56:59], 0\n"
"s_add_u32 s61, s75, 10\n"
"v_exp_f32 v54, v54\n"
"v_mfma_f32_16x16x32_bf16 v[40:43], a[36:39], v[60:63], v[40:43]\n"
"s_and_b32 s61, s61, 15\n"
"v_exp_f32 v55, v55\n"
"v_mfma_f32_16x16x32_bf16 v[40:43], a[40:43], v[64:67], v[40:43]\n"
"s_lshl_b32 s61, s61, 14\n"
"v_cvt_pk_bf16_f32 v38, v52, v53\n"
"v_lshrrev_b32 v253, 2, v11\n"
"v_mfma_f32_16x16x32_bf16 v[40:43], a[44:47], v[68:71], v[40:43]\n"
"s_add_u32 s71, s68, s61\n"
"ds_read_b128 a[0:3], v1 offset:49152\n"
"v_cvt_pk_bf16_f32 v39, v54, v55\n"
"v_mfma_f32_16x16x32_bf16 v[44:47], a[48:51], v[56:59], 0\n"
"s_add_u32 s76, s71, 0\n"
"ds_read_b128 a[4:7], v1 offset:49408\n"
"ds_read_b128 a[8:11], v2 offset:49152\n"
"v_mfma_f32_16x16x32_bf16 v[44:47], a[52:55], v[60:63], v[44:47]\n"
"s_add_u32 m0, s68, 32768\n"
"ds_read_b128 a[12:15], v2 offset:49408\n"
"v_mfma_f32_16x16x32_bf16 v[44:47], a[56:59], v[64:67], v[44:47]\n"
"buffer_load_dwordx4 v9, s[28:31], s76 offen nt lds\n"
"ds_read_b128 a[16:19], v1 offset:53248\n"
"v_mfma_f32_16x16x32_bf16 v[44:47], a[60:63], v[68:71], v[44:47]\n"
"v_exp_f32 v40, v40\n"
"v_mfma_f32_16x16x32_bf16 v[232:235], a[80:83], v[36:39], v[232:235]\n"
"s_lshl_b32 s60, s55, 7\n"
"v_exp_f32 v41, v41\n"
"v_mfma_f32_16x16x32_bf16 v[236:239], a[84:87], v[36:39], v[236:239]\n"
"v_exp_f32 v42, v42\n"
"v_mfma_f32_16x16x32_bf16 v[240:243], a[88:91], v[36:39], v[240:243]\n"
"s_add_u32 s60, s60, 32768\n"
"v_exp_f32 v43, v43\n"
"v_mfma_f32_16x16x32_bf16 v[244:247], a[92:95], v[36:39], v[244:247]\n"
"ds_read_b128 a[20:23], v1 offset:53504\n"
"v_cvt_pk_bf16_f32 v32, v40, v41\n"
"v_mfma_f32_16x16x32_bf16 v[216:219], a[64:67], v[36:39], v[216:219]\n"
"v_exp_f32 v44, v44\n"
"v_mfma_f32_16x16x32_bf16 v[220:223], a[68:71], v[36:39], v[220:223]\n"
"v_exp_f32 v45, v45\n"
"v_mfma_f32_16x16x32_bf16 v[224:227], a[72:75], v[36:39], v[224:227]\n"
"s_add_u32 s77, s71, 4096\n"
"ds_read_b128 a[24:27], v2 offset:53248\n"
"v_cvt_pk_bf16_f32 v33, v42, v43\n"
"v_mfma_f32_16x16x32_bf16 v[228:231], a[76:79], v[36:39], v[228:231]\n"
"s_add_u32 m0, s68, 36864\n"
"ds_read_b128 a[28:31], v2 offset:53504\n"
"v_mfma_f32_16x16x32_bf16 v[28:31], v[20:23], v[36:39], v[28:31]\n"
"buffer_load_dwordx4 v9, s[28:31], s77 offen nt lds\n"
"buffer_load_dword v15, v253, s[48:51], s60 offen sc1\n"
"v_mfma_f32_16x16x32_bf16 v[48:51], a[32:35], v[72:75], 0\n"
"v_exp_f32 v46, v46\n"
"v_mfma_f32_16x16x32_bf16 v[48:51], a[36:39], v[76:79], v[48:51]\n"
"v_exp_f32 v47, v47\n"
"v_mfma_f32_16x16x32_bf16 v[48:51], a[40:43], v[80:83], v[48:51]\n"
"ds_read_b64_tr_b16 a[64:65], v5 offset:49152\n"
"v_cvt_pk_bf16_f32 v34, v44, v45\n"
"v_mfma_f32_16x16x32_bf16 v[48:51], a[44:47], v[84:87], v[48:51]\n"
"ds_read_b64_tr_b16 a[66:67], v5 offset:53248\n"
"v_cvt_pk_bf16_f32 v35, v46, v47\n"
"v_mfma_f32_16x16x32_bf16 v[52:55], a[48:51], v[72:75], 0\n"
"ds_read_b64_tr_b16 a[68:69], v6 offset:49152\n"
"ds_read_b64_tr_b16 a[70:71], v6 offset:53248\n"
"v_mfma_f32_16x16x32_bf16 v[52:55], a[52:55], v[76:79], v[52:55]\n"
"ds_read_b64_tr_b16 a[72:73], v5 offset:49408\n"
"ds_read_b64_tr_b16 a[74:75], v5 offset:53504\n"
"v_mfma_f32_16x16x32_bf16 v[52:55], a[56:59], v[80:83], v[52:55]\n"
"ds_read_b64_tr_b16 a[76:77], v6 offset:49408\n"
"ds_read_b64_tr_b16 a[78:79], v6 offset:53504\n"
"v_mfma_f32_16x16x32_bf16 v[52:55], a[60:63], v[84:87], v[52:55]\n"
"s_add_u32 s76, s71, 8192\n"
"v_exp_f32 v48, v48\n"
"v_mfma_f32_16x16x32_bf16 v[120:123], a[96:99], v[32:35], v[120:123]\n"
"s_add_u32 m0, s68, 40960\n"
"v_exp_f32 v49, v49\n"
"v_mfma_f32_16x16x32_bf16 v[124:127], a[100:103], v[32:35], v[124:127]\n"
"buffer_load_dwordx4 v9, s[28:31], s76 offen nt lds\n"
"v_exp_f32 v50, v50\n"
"v_mfma_f32_16x16x32_bf16 v[128:131], a[104:107], v[32:35], v[128:131]\n"
"v_exp_f32 v51, v51\n"
"v_mfma_f32_16x16x32_bf16 v[132:135], a[108:111], v[32:35], v[132:135]\n"
"v_cvt_pk_bf16_f32 v36, v48, v49\n"
"v_add_f32 v16, v16, v48\n"
"v_mfma_f32_16x16x32_bf16 v[136:139], a[112:115], v[32:35], v[136:139]\n"
"v_exp_f32 v52, v52\n"
"v_mfma_f32_16x16x32_bf16 v[140:143], a[116:119], v[32:35], v[140:143]\n"
"v_exp_f32 v53, v53\n"
"v_mfma_f32_16x16x32_bf16 v[144:147], a[120:123], v[32:35], v[144:147]\n"
"v_cvt_pk_bf16_f32 v37, v50, v51\n"
"v_add_f32 v16, v16, v49\n"
"v_mfma_f32_16x16x32_bf16 v[148:151], a[124:127], v[32:35], v[148:151]\n"
"v_add_f32 v16, v16, v50\n"
"v_add_f32 v16, v16, v51\n"
"v_mfma_f32_16x16x32_bf16 v[24:27], v[20:23], v[32:35], v[24:27]\n"
"ds_read_b64_tr_b16 a[80:81], v248 offset:49152\n"
"ds_read_b64_tr_b16 a[82:83], v248 offset:53248\n"
"v_mfma_f32_16x16x32_bf16 v[40:43], a[32:35], v[88:91], 0\n"
"v_exp_f32 v54, v54\n"
"v_mfma_f32_16x16x32_bf16 v[40:43], a[36:39], v[92:95], v[40:43]\n"
"v_exp_f32 v55, v55\n"
"v_mfma_f32_16x16x32_bf16 v[40:43], a[40:43], v[96:99], v[40:43]\n"
"s_add_u32 s77, s71, 12288\n"
"v_cvt_pk_bf16_f32 v38, v52, v53\n"
"v_add_f32 v16, v16, v52\n"
"v_mfma_f32_16x16x32_bf16 v[40:43], a[44:47], v[100:103], v[40:43]\n"
"s_add_u32 m0, s68, 45056\n"
"v_cvt_pk_bf16_f32 v39, v54, v55\n"
"v_add_f32 v16, v16, v53\n"
"v_mfma_f32_16x16x32_bf16 v[44:47], a[48:51], v[88:91], 0\n"
"buffer_load_dwordx4 v9, s[28:31], s77 offen nt lds\n"
"v_add_f32 v16, v16, v54\n"
"v_mfma_f32_16x16x32_bf16 v[44:47], a[52:55], v[92:95], v[44:47]\n"
"ds_read_b64_tr_b16 a[84:85], v249 offset:49152\n"
"v_add_f32 v16, v16, v55\n"
"v_mfma_f32_16x16x32_bf16 v[44:47], a[56:59], v[96:99], v[44:47]\n"
"ds_read_b64_tr_b16 a[86:87], v249 offset:53248\n"
"ds_read_b64_tr_b16 a[88:89], v248 offset:49408\n"
"v_mfma_f32_16x16x32_bf16 v[44:47], a[60:63], v[100:103], v[44:47]\n"
"v_exp_f32 v40, v40\n"
"v_mfma_f32_16x16x32_bf16 v[152:155], a[96:99], v[36:39], v[152:155]\n"
"v_exp_f32 v41, v41\n"
"v_mfma_f32_16x16x32_bf16 v[156:159], a[100:103], v[36:39], v[156:159]\n"
"v_exp_f32 v42, v42\n"
"v_mfma_f32_16x16x32_bf16 v[160:163], a[104:107], v[36:39], v[160:163]\n"
"s_add_u32 s76, s71, 0\n"
"v_exp_f32 v43, v43\n"
"v_mfma_f32_16x16x32_bf16 v[164:167], a[108:111], v[36:39], v[164:167]\n"
"s_add_u32 m0, s68, 98304\n"
"v_cvt_pk_bf16_f32 v32, v40, v41\n"
"v_add_f32 v17, v17, v40\n"
"v_mfma_f32_16x16x32_bf16 v[168:171], a[112:115], v[36:39], v[168:171]\n"
"buffer_load_dwordx4 v10, s[88:91], s76 offen nt lds\n"
"v_exp_f32 v44, v44\n"
"v_mfma_f32_16x16x32_bf16 v[172:175], a[116:119], v[36:39], v[172:175]\n"
"v_exp_f32 v45, v45\n"
"v_mfma_f32_16x16x32_bf16 v[176:179], a[120:123], v[36:39], v[176:179]\n"
"v_cvt_pk_bf16_f32 v33, v42, v43\n"
"v_add_f32 v17, v17, v41\n"
"v_mfma_f32_16x16x32_bf16 v[180:183], a[124:127], v[36:39], v[180:183]\n"
"s_waitcnt lgkmcnt(13)\n"
"v_add_f32 v17, v17, v42\n"
"v_add_f32 v17, v17, v43\n"
"v_mfma_f32_16x16x32_bf16 v[48:51], a[0:3], v[56:59], 0\n"
"v_exp_f32 v46, v46\n"
"v_mfma_f32_16x16x32_bf16 v[48:51], a[4:7], v[60:63], v[48:51]\n"
"v_exp_f32 v47, v47\n"
"v_mfma_f32_16x16x32_bf16 v[48:51], a[8:11], v[64:67], v[48:51]\n"
"v_cvt_pk_bf16_f32 v34, v44, v45\n"
"v_add_f32 v17, v17, v44\n"
"v_mfma_f32_16x16x32_bf16 v[48:51], a[12:15], v[68:71], v[48:51]\n"
"v_cvt_pk_bf16_f32 v35, v46, v47\n"
"v_add_f32 v17, v17, v45\n"
"v_mfma_f32_16x16x32_bf16 v[52:55], a[16:19], v[56:59], 0\n"
"s_add_u32 s77, s71, 4096\n"
"ds_read_b64_tr_b16 a[90:91], v248 offset:53504\n"
"v_add_f32 v17, v17, v46\n"
"v_mfma_f32_16x16x32_bf16 v[52:55], a[20:23], v[60:63], v[52:55]\n"
"s_add_u32 m0, s68, 102400\n"
"ds_read_b64_tr_b16 a[92:93], v249 offset:49408\n"
"v_add_f32 v17, v17, v47\n"
"v_mfma_f32_16x16x32_bf16 v[52:55], a[24:27], v[64:67], v[52:55]\n"
"buffer_load_dwordx4 v10, s[88:91], s77 offen nt lds\n"
"ds_read_b64_tr_b16 a[94:95], v249 offset:53504\n"
"v_mfma_f32_16x16x32_bf16 v[52:55], a[28:31], v[68:71], v[52:55]\n"
"ds_read_b128 a[32:35], v3 offset:49152\n"
"v_exp_f32 v48, v48\n"
"v_mfma_f32_16x16x32_bf16 v[184:187], a[96:99], v[32:35], v[184:187]\n"
"v_exp_f32 v49, v49\n"
"v_mfma_f32_16x16x32_bf16 v[188:191], a[100:103], v[32:35], v[188:191]\n"
"ds_read_b128 a[36:39], v3 offset:49408\n"
"v_exp_f32 v50, v50\n"
"v_mfma_f32_16x16x32_bf16 v[192:195], a[104:107], v[32:35], v[192:195]\n"
"v_exp_f32 v51, v51\n"
"v_mfma_f32_16x16x32_bf16 v[196:199], a[108:111], v[32:35], v[196:199]\n"
"ds_read_b128 a[40:43], v4 offset:49152\n"
"v_cvt_pk_bf16_f32 v36, v48, v49\n"
"v_mfma_f32_16x16x32_bf16 v[200:203], a[112:115], v[32:35], v[200:203]\n"
"ds_read_b128 a[44:47], v4 offset:49408\n"
"v_exp_f32 v52, v52\n"
"v_mfma_f32_16x16x32_bf16 v[204:207], a[116:119], v[32:35], v[204:207]\n"
"v_exp_f32 v53, v53\n"
"v_mfma_f32_16x16x32_bf16 v[208:211], a[120:123], v[32:35], v[208:211]\n"
"ds_read_b128 a[48:51], v3 offset:53248\n"
"v_cvt_pk_bf16_f32 v37, v50, v51\n"
"v_mfma_f32_16x16x32_bf16 v[212:215], a[124:127], v[32:35], v[212:215]\n"
"ds_read_b128 a[52:55], v3 offset:53504\n"
"ds_read_b128 a[56:59], v4 offset:53248\n"
"v_mfma_f32_16x16x32_bf16 v[40:43], a[0:3], v[72:75], 0\n"
"s_add_u32 s76, s71, 8192\n"
"ds_read_b128 a[60:63], v4 offset:53504\n"
"v_exp_f32 v54, v54\n"
"v_mfma_f32_16x16x32_bf16 v[40:43], a[4:7], v[76:79], v[40:43]\n"
"s_add_u32 m0, s68, 106496\n"
"v_exp_f32 v55, v55\n"
"v_mfma_f32_16x16x32_bf16 v[40:43], a[8:11], v[80:83], v[40:43]\n"
"buffer_load_dwordx4 v10, s[88:91], s76 offen nt lds\n"
"v_cvt_pk_bf16_f32 v38, v52, v53\n"
"v_mfma_f32_16x16x32_bf16 v[40:43], a[12:15], v[84:87], v[40:43]\n"
"ds_read_b64_tr_b16 a[96:97], v7 offset:49152\n"
"v_cvt_pk_bf16_f32 v39, v54, v55\n"
"v_mfma_f32_16x16x32_bf16 v[44:47], a[16:19], v[72:75], 0\n"
"ds_read_b64_tr_b16 a[98:99], v7 offset:53248\n"
"ds_read_b64_tr_b16 a[100:101], v8 offset:49152\n"
"v_mfma_f32_16x16x32_bf16 v[44:47], a[20:23], v[76:79], v[44:47]\n"
"ds_read_b64_tr_b16 a[102:103], v8 offset:53248\n"
"ds_read_b64_tr_b16 a[104:105], v7 offset:49408\n"
"v_mfma_f32_16x16x32_bf16 v[44:47], a[24:27], v[80:83], v[44:47]\n"
"ds_read_b64_tr_b16 a[106:107], v7 offset:53504\n"
"ds_read_b64_tr_b16 a[108:109], v8 offset:49408\n"
"v_mfma_f32_16x16x32_bf16 v[44:47], a[28:31], v[84:87], v[44:47]\n"
"s_waitcnt lgkmcnt(15)\n"
"v_exp_f32 v40, v40\n"
"v_mfma_f32_16x16x32_bf16 v[120:123], a[64:67], v[36:39], v[120:123]\n"
"v_exp_f32 v41, v41\n"
"v_mfma_f32_16x16x32_bf16 v[124:127], a[68:71], v[36:39], v[124:127]\n"
"v_exp_f32 v42, v42\n"
"v_mfma_f32_16x16x32_bf16 v[128:131], a[72:75], v[36:39], v[128:131]\n"
"v_exp_f32 v43, v43\n"
"v_mfma_f32_16x16x32_bf16 v[132:135], a[76:79], v[36:39], v[132:135]\n"
"v_cvt_pk_bf16_f32 v32, v40, v41\n"
"v_add_f32 v16, v16, v40\n"
"v_mfma_f32_16x16x32_bf16 v[136:139], a[80:83], v[36:39], v[136:139]\n"
"v_exp_f32 v44, v44\n"
"v_mfma_f32_16x16x32_bf16 v[140:143], a[84:87], v[36:39], v[140:143]\n"
"v_exp_f32 v45, v45\n"
"v_mfma_f32_16x16x32_bf16 v[144:147], a[88:91], v[36:39], v[144:147]\n"
"s_add_u32 s77, s71, 12288\n"
"v_cvt_pk_bf16_f32 v33, v42, v43\n"
"v_add_f32 v16, v16, v41\n"
"v_mfma_f32_16x16x32_bf16 v[148:151], a[92:95], v[36:39], v[148:151]\n"
"s_add_u32 m0, s68, 110592\n"
"v_add_f32 v16, v16, v42\n"
"v_add_f32 v16, v16, v43\n"
"v_mfma_f32_16x16x32_bf16 v[24:27], v[20:23], v[36:39], v[24:27]\n"
"buffer_load_dwordx4 v10, s[88:91], s77 offen nt lds\n"
"ds_read_b64_tr_b16 a[110:111], v8 offset:53504\n"
"v_mfma_f32_16x16x32_bf16 v[48:51], a[0:3], v[88:91], 0\n"
"v_exp_f32 v46, v46\n"
"v_mfma_f32_16x16x32_bf16 v[48:51], a[4:7], v[92:95], v[48:51]\n"
"v_exp_f32 v47, v47\n"
"v_mfma_f32_16x16x32_bf16 v[48:51], a[8:11], v[96:99], v[48:51]\n"
"s_mov_b32 s70, 1572864\n"
"v_cvt_pk_bf16_f32 v34, v44, v45\n"
"v_add_f32 v16, v16, v44\n"
"v_mfma_f32_16x16x32_bf16 v[48:51], a[12:15], v[100:103], v[48:51]\n"
"v_cvt_pk_bf16_f32 v35, v46, v47\n"
"v_add_f32 v16, v16, v45\n"
"v_mfma_f32_16x16x32_bf16 v[52:55], a[16:19], v[88:91], 0\n"
"buffer_load_dword v0, v254, s[40:43], s70 offen\n"
"v_add_f32 v16, v16, v46\n"
"v_mfma_f32_16x16x32_bf16 v[52:55], a[20:23], v[92:95], v[52:55]\n"
"ds_read_b64_tr_b16 a[112:113], v250 offset:49152\n"
"v_add_f32 v16, v16, v47\n"
"v_mfma_f32_16x16x32_bf16 v[52:55], a[24:27], v[96:99], v[52:55]\n"
"ds_read_b64_tr_b16 a[114:115], v250 offset:53248\n"
"ds_read_b64_tr_b16 a[116:117], v251 offset:49152\n"
"v_mfma_f32_16x16x32_bf16 v[52:55], a[28:31], v[100:103], v[52:55]\n"
"v_exp_f32 v48, v48\n"
"v_mfma_f32_16x16x32_bf16 v[152:155], a[64:67], v[32:35], v[152:155]\n"
"v_exp_f32 v49, v49\n"
"v_mfma_f32_16x16x32_bf16 v[156:159], a[68:71], v[32:35], v[156:159]\n"
"v_exp_f32 v50, v50\n"
"v_mfma_f32_16x16x32_bf16 v[160:163], a[72:75], v[32:35], v[160:163]\n"
"v_exp_f32 v51, v51\n"
"v_mfma_f32_16x16x32_bf16 v[164:167], a[76:79], v[32:35], v[164:167]\n"
"v_cvt_pk_bf16_f32 v36, v48, v49\n"
"v_add_f32 v17, v17, v48\n"
"v_mfma_f32_16x16x32_bf16 v[168:171], a[80:83], v[32:35], v[168:171]\n"
"v_exp_f32 v52, v52\n"
"v_mfma_f32_16x16x32_bf16 v[172:175], a[84:87], v[32:35], v[172:175]\n"
"v_exp_f32 v53, v53\n"
"v_mfma_f32_16x16x32_bf16 v[176:179], a[88:91], v[32:35], v[176:179]\n"
"v_cvt_pk_bf16_f32 v37, v50, v51\n"
"v_add_f32 v17, v17, v49\n"
"v_mfma_f32_16x16x32_bf16 v[180:183], a[92:95], v[32:35], v[180:183]\n"
"v_add_f32 v17, v17, v50\n"
"v_add_f32 v17, v17, v51\n"
"v_mfma_f32_16x16x32_bf16 v[40:43], a[0:3], v[104:107], 0\n"
"v_exp_f32 v54, v54\n"
"v_mfma_f32_16x16x32_bf16 v[40:43], a[4:7], v[108:111], v[40:43]\n"
"v_exp_f32 v55, v55\n"
"v_mfma_f32_16x16x32_bf16 v[40:43], a[8:11], v[112:115], v[40:43]\n"
"v_cvt_pk_bf16_f32 v38, v52, v53\n"
"v_add_f32 v17, v17, v52\n"
"v_mfma_f32_16x16x32_bf16 v[40:43], a[12:15], v[116:119], v[40:43]\n"
"v_cvt_pk_bf16_f32 v39, v54, v55\n"
"v_add_f32 v17, v17, v53\n"
"v_mfma_f32_16x16x32_bf16 v[44:47], a[16:19], v[104:107], 0\n"
"ds_read_b64_tr_b16 a[118:119], v251 offset:53248\n"
"v_add_f32 v17, v17, v54\n"
"v_mfma_f32_16x16x32_bf16 v[44:47], a[20:23], v[108:111], v[44:47]\n"
"ds_read_b64_tr_b16 a[120:121], v250 offset:49408\n"
"v_add_f32 v17, v17, v55\n"
"v_mfma_f32_16x16x32_bf16 v[44:47], a[24:27], v[112:115], v[44:47]\n"
"ds_read_b64_tr_b16 a[122:123], v250 offset:53504\n"
"ds_read_b64_tr_b16 a[124:125], v251 offset:49408\n"
"v_mfma_f32_16x16x32_bf16 v[44:47], a[28:31], v[116:119], v[44:47]\n"
"v_exp_f32 v40, v40\n"
"v_mfma_f32_16x16x32_bf16 v[184:187], a[64:67], v[36:39], v[184:187]\n"
"v_exp_f32 v41, v41\n"
"v_mfma_f32_16x16x32_bf16 v[188:191], a[68:71], v[36:39], v[188:191]\n"
"v_exp_f32 v42, v42\n"
"v_mfma_f32_16x16x32_bf16 v[192:195], a[72:75], v[36:39], v[192:195]\n"
"v_exp_f32 v43, v43\n"
"v_mfma_f32_16x16x32_bf16 v[196:199], a[76:79], v[36:39], v[196:199]\n"
"ds_read_b64_tr_b16 a[126:127], v251 offset:53504\n"
"v_cvt_pk_bf16_f32 v32, v40, v41\n"
"v_mfma_f32_16x16x32_bf16 v[200:203], a[80:83], v[36:39], v[200:203]\n"
"v_exp_f32 v44, v44\n"
"v_mfma_f32_16x16x32_bf16 v[204:207], a[84:87], v[36:39], v[204:207]\n"
"v_exp_f32 v45, v45\n"
"v_mfma_f32_16x16x32_bf16 v[208:211], a[88:91], v[36:39], v[208:211]\n"
"v_cvt_pk_bf16_f32 v33, v42, v43\n"
"v_mfma_f32_16x16x32_bf16 v[212:215], a[92:95], v[36:39], v[212:215]\n"
"s_waitcnt vmcnt(20) lgkmcnt(0)\n"
"s_barrier\n"
"v_mfma_f32_16x16x32_bf16 v[48:51], a[32:35], v[56:59], 0\n"
"s_add_u32 s61, s75, 11\n"
"v_exp_f32 v46, v46\n"
"v_mfma_f32_16x16x32_bf16 v[48:51], a[36:39], v[60:63], v[48:51]\n"
"s_and_b32 s61, s61, 15\n"
"v_exp_f32 v47, v47\n"
"v_mfma_f32_16x16x32_bf16 v[48:51], a[40:43], v[64:67], v[48:51]\n"
"s_lshl_b32 s61, s61, 14\n"
"ds_read_b128 a[0:3], v1 offset:0\n"
"v_cvt_pk_bf16_f32 v34, v44, v45\n"
"v_mfma_f32_16x16x32_bf16 v[48:51], a[44:47], v[68:71], v[48:51]\n"
"s_add_u32 s71, s68, s61\n"
"ds_read_b128 a[4:7], v1 offset:256\n"
"v_cvt_pk_bf16_f32 v35, v46, v47\n"
"v_mfma_f32_16x16x32_bf16 v[52:55], a[48:51], v[56:59], 0\n"
"s_add_u32 s76, s71, 0\n"
"ds_read_b128 a[8:11], v2 offset:0\n"
"v_mfma_f32_16x16x32_bf16 v[52:55], a[52:55], v[60:63], v[52:55]\n"
"s_add_u32 m0, s68, 49152\n"
"ds_read_b128 a[12:15], v2 offset:256\n"
"ds_read_b128 a[16:19], v1 offset:4096\n"
"v_mfma_f32_16x16x32_bf16 v[52:55], a[56:59], v[64:67], v[52:55]\n"
"buffer_load_dwordx4 v9, s[28:31], s76 offen nt lds\n"
"ds_read_b128 a[20:23], v1 offset:4352\n"
"v_mfma_f32_16x16x32_bf16 v[52:55], a[60:63], v[68:71], v[52:55]\n"
"v_exp_f32 v48, v48\n"
"v_mfma_f32_16x16x32_bf16 v[232:235], a[80:83], v[32:35], v[232:235]\n"
"v_exp_f32 v49, v49\n"
"v_mfma_f32_16x16x32_bf16 v[236:239], a[84:87], v[32:35], v[236:239]\n"
"v_exp_f32 v50, v50\n"
"v_mfma_f32_16x16x32_bf16 v[240:243], a[88:91], v[32:35], v[240:243]\n"
"v_exp_f32 v51, v51\n"
"v_mfma_f32_16x16x32_bf16 v[244:247], a[92:95], v[32:35], v[244:247]\n"
"v_cvt_pk_bf16_f32 v36, v48, v49\n"
"v_mfma_f32_16x16x32_bf16 v[216:219], a[64:67], v[32:35], v[216:219]\n"
"v_exp_f32 v52, v52\n"
"v_mfma_f32_16x16x32_bf16 v[220:223], a[68:71], v[32:35], v[220:223]\n"
"v_exp_f32 v53, v53\n"
"v_mfma_f32_16x16x32_bf16 v[224:227], a[72:75], v[32:35], v[224:227]\n"
"s_add_u32 s77, s71, 4096\n"
"ds_read_b128 a[24:27], v2 offset:4096\n"
"v_cvt_pk_bf16_f32 v37, v50, v51\n"
"v_mfma_f32_16x16x32_bf16 v[228:231], a[76:79], v[32:35], v[228:231]\n"
"s_add_u32 m0, s68, 53248\n"
"ds_read_b128 a[28:31], v2 offset:4352\n"
"v_mfma_f32_16x16x32_bf16 v[28:31], v[20:23], v[32:35], v[28:31]\n"
"buffer_load_dwordx4 v9, s[28:31], s77 offen nt lds\n"
"ds_read_b64_tr_b16 a[64:65], v5 offset:0\n"
"v_mfma_f32_16x16x32_bf16 v[40:43], a[32:35], v[72:75], 0\n"
"v_exp_f32 v54, v54\n"
"v_mfma_f32_16x16x32_bf16 v[40:43], a[36:39], v[76:79], v[40:43]\n"
"v_exp_f32 v55, v55\n"
"v_mfma_f32_16x16x32_bf16 v[40:43], a[40:43], v[80:83], v[40:43]\n"
"ds_read_b64_tr_b16 a[66:67], v5 offset:4096\n"
"v_cvt_pk_bf16_f32 v38, v52, v53\n"
"v_mfma_f32_16x16x32_bf16 v[40:43], a[44:47], v[84:87], v[40:43]\n"
"ds_read_b64_tr_b16 a[68:69], v6 offset:0\n"
"v_cvt_pk_bf16_f32 v39, v54, v55\n"
"v_mfma_f32_16x16x32_bf16 v[44:47], a[48:51], v[72:75], 0\n"
"ds_read_b64_tr_b16 a[70:71], v6 offset:4096\n"
"ds_read_b64_tr_b16 a[72:73], v5 offset:256\n"
"v_mfma_f32_16x16x32_bf16 v[44:47], a[52:55], v[76:79], v[44:47]\n"
"ds_read_b64_tr_b16 a[74:75], v5 offset:4352\n"
"ds_read_b64_tr_b16 a[76:77], v6 offset:256\n"
"v_mfma_f32_16x16x32_bf16 v[44:47], a[56:59], v[80:83], v[44:47]\n"
"ds_read_b64_tr_b16 a[78:79], v6 offset:4352\n"
"v_mfma_f32_16x16x32_bf16 v[44:47], a[60:63], v[84:87], v[44:47]\n"
"s_add_u32 s76, s71, 8192\n"
"v_exp_f32 v40, v40\n"
"v_mfma_f32_16x16x32_bf16 v[120:123], a[96:99], v[36:39], v[120:123]\n"
"s_add_u32 m0, s68, 57344\n"
"v_exp_f32 v41, v41\n"
"v_mfma_f32_16x16x32_bf16 v[124:127], a[100:103], v[36:39], v[124:127]\n"
"buffer_load_dwordx4 v9, s[28:31], s76 offen nt lds\n"
"v_exp_f32 v42, v42\n"
"v_mfma_f32_16x16x32_bf16 v[128:131], a[104:107], v[36:39], v[128:131]\n"
"v_exp_f32 v43, v43\n"
"v_mfma_f32_16x16x32_bf16 v[132:135], a[108:111], v[36:39], v[132:135]\n"
"v_cvt_pk_bf16_f32 v32, v40, v41\n"
"v_add_f32 v16, v16, v40\n"
"v_mfma_f32_16x16x32_bf16 v[136:139], a[112:115], v[36:39], v[136:139]\n"
"v_exp_f32 v44, v44\n"
"v_mfma_f32_16x16x32_bf16 v[140:143], a[116:119], v[36:39], v[140:143]\n"
"v_exp_f32 v45, v45\n"
"v_mfma_f32_16x16x32_bf16 v[144:147], a[120:123], v[36:39], v[144:147]\n"
"v_cvt_pk_bf16_f32 v33, v42, v43\n"
"v_add_f32 v16, v16, v41\n"
"v_mfma_f32_16x16x32_bf16 v[148:151], a[124:127], v[36:39], v[148:151]\n"
"v_add_f32 v16, v16, v42\n"
"v_add_f32 v16, v16, v43\n"
"v_mfma_f32_16x16x32_bf16 v[24:27], v[20:23], v[36:39], v[24:27]\n"
"ds_read_b64_tr_b16 a[80:81], v248 offset:0\n"
"ds_read_b64_tr_b16 a[82:83], v248 offset:4096\n"
"v_mfma_f32_16x16x32_bf16 v[48:51], a[32:35], v[88:91], 0\n"
"v_exp_f32 v46, v46\n"
"v_mfma_f32_16x16x32_bf16 v[48:51], a[36:39], v[92:95], v[48:51]\n"
"v_exp_f32 v47, v47\n"
"v_mfma_f32_16x16x32_bf16 v[48:51], a[40:43], v[96:99], v[48:51]\n"
"s_add_u32 s77, s71, 12288\n"
"v_cvt_pk_bf16_f32 v34, v44, v45\n"
"v_add_f32 v16, v16, v44\n"
"v_mfma_f32_16x16x32_bf16 v[48:51], a[44:47], v[100:103], v[48:51]\n"
"s_add_u32 m0, s68, 61440\n"
"v_cvt_pk_bf16_f32 v35, v46, v47\n"
"v_add_f32 v16, v16, v45\n"
"v_mfma_f32_16x16x32_bf16 v[52:55], a[48:51], v[88:91], 0\n"
"buffer_load_dwordx4 v9, s[28:31], s77 offen nt lds\n"
"v_add_f32 v16, v16, v46\n"
"v_mfma_f32_16x16x32_bf16 v[52:55], a[52:55], v[92:95], v[52:55]\n"
"ds_read_b64_tr_b16 a[84:85], v249 offset:0\n"
"v_add_f32 v16, v16, v47\n"
"v_mfma_f32_16x16x32_bf16 v[52:55], a[56:59], v[96:99], v[52:55]\n"
"ds_read_b64_tr_b16 a[86:87], v249 offset:4096\n"
"ds_read_b64_tr_b16 a[88:89], v248 offset:256\n"
"v_mfma_f32_16x16x32_bf16 v[52:55], a[60:63], v[100:103], v[52:55]\n"
"v_exp_f32 v48, v48\n"
"v_mfma_f32_16x16x32_bf16 v[152:155], a[96:99], v[32:35], v[152:155]\n"
"v_exp_f32 v49, v49\n"
"v_mfma_f32_16x16x32_bf16 v[156:159], a[100:103], v[32:35], v[156:159]\n"
"v_exp_f32 v50, v50\n"
"v_mfma_f32_16x16x32_bf16 v[160:163], a[104:107], v[32:35], v[160:163]\n"
"s_add_u32 s76, s71, 0\n"
"v_exp_f32 v51, v51\n"
"v_mfma_f32_16x16x32_bf16 v[164:167], a[108:111], v[32:35], v[164:167]\n"
"s_add_u32 m0, s68, 114688\n"
"v_cvt_pk_bf16_f32 v36, v48, v49\n"
"v_add_f32 v17, v17, v48\n"
"v_mfma_f32_16x16x32_bf16 v[168:171], a[112:115], v[32:35], v[168:171]\n"
"buffer_load_dwordx4 v10, s[88:91], s76 offen nt lds\n"
"v_exp_f32 v52, v52\n"
"v_mfma_f32_16x16x32_bf16 v[172:175], a[116:119], v[32:35], v[172:175]\n"
"v_exp_f32 v53, v53\n"
"v_mfma_f32_16x16x32_bf16 v[176:179], a[120:123], v[32:35], v[176:179]\n"
"v_cvt_pk_bf16_f32 v37, v50, v51\n"
"v_add_f32 v17, v17, v49\n"
"v_mfma_f32_16x16x32_bf16 v[180:183], a[124:127], v[32:35], v[180:183]\n"
"s_waitcnt lgkmcnt(13)\n"
"v_add_f32 v17, v17, v50\n"
"v_add_f32 v17, v17, v51\n"
"v_mfma_f32_16x16x32_bf16 v[40:43], a[0:3], v[56:59], 0\n"
"v_exp_f32 v54, v54\n"
"v_mfma_f32_16x16x32_bf16 v[40:43], a[4:7], v[60:63], v[40:43]\n"
"v_exp_f32 v55, v55\n"
"v_mfma_f32_16x16x32_bf16 v[40:43], a[8:11], v[64:67], v[40:43]\n"
"v_cvt_pk_bf16_f32 v38, v52, v53\n"
"v_add_f32 v17, v17, v52\n"
"v_mfma_f32_16x16x32_bf16 v[40:43], a[12:15], v[68:71], v[40:43]\n"
"v_cvt_pk_bf16_f32 v39, v54, v55\n"
"v_add_f32 v17, v17, v53\n"
"v_mfma_f32_16x16x32_bf16 v[44:47], a[16:19], v[56:59], 0\n"
"s_add_u32 s77, s71, 4096\n"
"ds_read_b64_tr_b16 a[90:91], v248 offset:4352\n"
"v_add_f32 v17, v17, v54\n"
"v_mfma_f32_16x16x32_bf16 v[44:47], a[20:23], v[60:63], v[44:47]\n"
"s_add_u32 m0, s68, 118784\n"
"ds_read_b64_tr_b16 a[92:93], v249 offset:256\n"
"v_add_f32 v17, v17, v55\n"
"v_mfma_f32_16x16x32_bf16 v[44:47], a[24:27], v[64:67], v[44:47]\n"
"buffer_load_dwordx4 v10, s[88:91], s77 offen nt lds\n"
"ds_read_b64_tr_b16 a[94:95], v249 offset:4352\n"
"v_mfma_f32_16x16x32_bf16 v[44:47], a[28:31], v[68:71], v[44:47]\n"
"ds_read_b128 a[32:35], v3 offset:0\n"
"v_exp_f32 v40, v40\n"
"v_mfma_f32_16x16x32_bf16 v[184:187], a[96:99], v[36:39], v[184:187]\n"
"v_exp_f32 v41, v41\n"
"v_mfma_f32_16x16x32_bf16 v[188:191], a[100:103], v[36:39], v[188:191]\n"
"ds_read_b128 a[36:39], v3 offset:256\n"
"v_exp_f32 v42, v42\n"
"v_mfma_f32_16x16x32_bf16 v[192:195], a[104:107], v[36:39], v[192:195]\n"
"v_exp_f32 v43, v43\n"
"v_mfma_f32_16x16x32_bf16 v[196:199], a[108:111], v[36:39], v[196:199]\n"
"ds_read_b128 a[40:43], v4 offset:0\n"
"v_cvt_pk_bf16_f32 v32, v40, v41\n"
"v_mfma_f32_16x16x32_bf16 v[200:203], a[112:115], v[36:39], v[200:203]\n"
"ds_read_b128 a[44:47], v4 offset:256\n"
"v_exp_f32 v44, v44\n"
"v_mfma_f32_16x16x32_bf16 v[204:207], a[116:119], v[36:39], v[204:207]\n"
"v_exp_f32 v45, v45\n"
"v_mfma_f32_16x16x32_bf16 v[208:211], a[120:123], v[36:39], v[208:211]\n"
"ds_read_b128 a[48:51], v3 offset:4096\n"
"v_cvt_pk_bf16_f32 v33, v42, v43\n"
"v_mfma_f32_16x16x32_bf16 v[212:215], a[124:127], v[36:39], v[212:215]\n"
"ds_read_b128 a[52:55], v3 offset:4352\n"
"ds_read_b128 a[56:59], v4 offset:4096\n"
"v_mfma_f32_16x16x32_bf16 v[48:51], a[0:3], v[72:75], 0\n"
"s_add_u32 s76, s71, 8192\n"
"ds_read_b128 a[60:63], v4 offset:4352\n"
"v_exp_f32 v46, v46\n"
"v_mfma_f32_16x16x32_bf16 v[48:51], a[4:7], v[76:79], v[48:51]\n"
"s_add_u32 m0, s68, 122880\n"
"v_exp_f32 v47, v47\n"
"v_mfma_f32_16x16x32_bf16 v[48:51], a[8:11], v[80:83], v[48:51]\n"
"buffer_load_dwordx4 v10, s[88:91], s76 offen nt lds\n"
"v_cvt_pk_bf16_f32 v34, v44, v45\n"
"v_mfma_f32_16x16x32_bf16 v[48:51], a[12:15], v[84:87], v[48:51]\n"
"ds_read_b64_tr_b16 a[96:97], v7 offset:0\n"
"v_cvt_pk_bf16_f32 v35, v46, v47\n"
"v_mfma_f32_16x16x32_bf16 v[52:55], a[16:19], v[72:75], 0\n"
"ds_read_b64_tr_b16 a[98:99], v7 offset:4096\n"
"ds_read_b64_tr_b16 a[100:101], v8 offset:0\n"
"v_mfma_f32_16x16x32_bf16 v[52:55], a[20:23], v[76:79], v[52:55]\n"
"ds_read_b64_tr_b16 a[102:103], v8 offset:4096\n"
"ds_read_b64_tr_b16 a[104:105], v7 offset:256\n"
"v_mfma_f32_16x16x32_bf16 v[52:55], a[24:27], v[80:83], v[52:55]\n"
"ds_read_b64_tr_b16 a[106:107], v7 offset:4352\n"
"ds_read_b64_tr_b16 a[108:109], v8 offset:256\n"
"v_mfma_f32_16x16x32_bf16 v[52:55], a[28:31], v[84:87], v[52:55]\n"
"s_waitcnt lgkmcnt(15)\n"
"v_exp_f32 v48, v48\n"
"v_mfma_f32_16x16x32_bf16 v[120:123], a[64:67], v[32:35], v[120:123]\n"
"v_exp_f32 v49, v49\n"
"v_mfma_f32_16x16x32_bf16 v[124:127], a[68:71], v[32:35], v[124:127]\n"
"v_exp_f32 v50, v50\n"
"v_mfma_f32_16x16x32_bf16 v[128:131], a[72:75], v[32:35], v[128:131]\n"
"v_exp_f32 v51, v51\n"
"v_mfma_f32_16x16x32_bf16 v[132:135], a[76:79], v[32:35], v[132:135]\n"
"v_cvt_pk_bf16_f32 v36, v48, v49\n"
"v_add_f32 v16, v16, v48\n"
"v_mfma_f32_16x16x32_bf16 v[136:139], a[80:83], v[32:35], v[136:139]\n"
"v_exp_f32 v52, v52\n"
"v_mfma_f32_16x16x32_bf16 v[140:143], a[84:87], v[32:35], v[140:143]\n"
"v_exp_f32 v53, v53\n"
"v_mfma_f32_16x16x32_bf16 v[144:147], a[88:91], v[32:35], v[144:147]\n"
"s_add_u32 s77, s71, 12288\n"
"v_cvt_pk_bf16_f32 v37, v50, v51\n"
"v_add_f32 v16, v16, v49\n"
"v_mfma_f32_16x16x32_bf16 v[148:151], a[92:95], v[32:35], v[148:151]\n"
"s_add_u32 m0, s68, 126976\n"
"v_add_f32 v16, v16, v50\n"
"v_add_f32 v16, v16, v51\n"
"v_mfma_f32_16x16x32_bf16 v[24:27], v[20:23], v[32:35], v[24:27]\n"
"buffer_load_dwordx4 v10, s[88:91], s77 offen nt lds\n"
"ds_read_b64_tr_b16 a[110:111], v8 offset:4352\n"
"v_mfma_f32_16x16x32_bf16 v[40:43], a[0:3], v[88:91], 0\n"
"v_exp_f32 v54, v54\n"
"v_mfma_f32_16x16x32_bf16 v[40:43], a[4:7], v[92:95], v[40:43]\n"
"v_exp_f32 v55, v55\n"
"v_mfma_f32_16x16x32_bf16 v[40:43], a[8:11], v[96:99], v[40:43]\n"
"s_mov_b32 s70, 2097152\n"
"v_cvt_pk_bf16_f32 v38, v52, v53\n"
"v_add_f32 v16, v16, v52\n"
"v_mfma_f32_16x16x32_bf16 v[40:43], a[12:15], v[100:103], v[40:43]\n"
"v_cvt_pk_bf16_f32 v39, v54, v55\n"
"v_add_f32 v16, v16, v53\n"
"v_mfma_f32_16x16x32_bf16 v[44:47], a[16:19], v[88:91], 0\n"
"buffer_load_dword v0, v254, s[40:43], s70 offen\n"
"v_add_f32 v16, v16, v54\n"
"v_mfma_f32_16x16x32_bf16 v[44:47], a[20:23], v[92:95], v[44:47]\n"
"ds_read_b64_tr_b16 a[112:113], v250 offset:0\n"
"v_add_f32 v16, v16, v55\n"
"v_mfma_f32_16x16x32_bf16 v[44:47], a[24:27], v[96:99], v[44:47]\n"
"ds_read_b64_tr_b16 a[114:115], v250 offset:4096\n"
"ds_read_b64_tr_b16 a[116:117], v251 offset:0\n"
"v_mfma_f32_16x16x32_bf16 v[44:47], a[28:31], v[100:103], v[44:47]\n"
"v_exp_f32 v40, v40\n"
"v_mfma_f32_16x16x32_bf16 v[152:155], a[64:67], v[36:39], v[152:155]\n"
"v_exp_f32 v41, v41\n"
"v_mfma_f32_16x16x32_bf16 v[156:159], a[68:71], v[36:39], v[156:159]\n"
"v_exp_f32 v42, v42\n"
"v_mfma_f32_16x16x32_bf16 v[160:163], a[72:75], v[36:39], v[160:163]\n"
"v_exp_f32 v43, v43\n"
"v_mfma_f32_16x16x32_bf16 v[164:167], a[76:79], v[36:39], v[164:167]\n"
"v_cvt_pk_bf16_f32 v32, v40, v41\n"
"v_add_f32 v17, v17, v40\n"
"v_mfma_f32_16x16x32_bf16 v[168:171], a[80:83], v[36:39], v[168:171]\n"
"v_exp_f32 v44, v44\n"
"v_mfma_f32_16x16x32_bf16 v[172:175], a[84:87], v[36:39], v[172:175]\n"
"v_exp_f32 v45, v45\n"
"v_mfma_f32_16x16x32_bf16 v[176:179], a[88:91], v[36:39], v[176:179]\n"
"v_cvt_pk_bf16_f32 v33, v42, v43\n"
"v_add_f32 v17, v17, v41\n"
"v_mfma_f32_16x16x32_bf16 v[180:183], a[92:95], v[36:39], v[180:183]\n"
"v_add_f32 v17, v17, v42\n"
"v_add_f32 v17, v17, v43\n"
"v_mfma_f32_16x16x32_bf16 v[48:51], a[0:3], v[104:107], 0\n"
"v_exp_f32 v46, v46\n"
"v_mfma_f32_16x16x32_bf16 v[48:51], a[4:7], v[108:111], v[48:51]\n"
"v_exp_f32 v47, v47\n"
"v_mfma_f32_16x16x32_bf16 v[48:51], a[8:11], v[112:115], v[48:51]\n"
"v_cvt_pk_bf16_f32 v34, v44, v45\n"
"v_add_f32 v17, v17, v44\n"
"v_mfma_f32_16x16x32_bf16 v[48:51], a[12:15], v[116:119], v[48:51]\n"
"v_cvt_pk_bf16_f32 v35, v46, v47\n"
"v_add_f32 v17, v17, v45\n"
"v_mfma_f32_16x16x32_bf16 v[52:55], a[16:19], v[104:107], 0\n"
"ds_read_b64_tr_b16 a[118:119], v251 offset:4096\n"
"v_add_f32 v17, v17, v46\n"
"v_mfma_f32_16x16x32_bf16 v[52:55], a[20:23], v[108:111], v[52:55]\n"
"ds_read_b64_tr_b16 a[120:121], v250 offset:256\n"
"v_add_f32 v17, v17, v47\n"
"v_mfma_f32_16x16x32_bf16 v[52:55], a[24:27], v[112:115], v[52:55]\n"
"ds_read_b64_tr_b16 a[122:123], v250 offset:4352\n"
"ds_read_b64_tr_b16 a[124:125], v251 offset:256\n"
"v_mfma_f32_16x16x32_bf16 v[52:55], a[28:31], v[116:119], v[52:55]\n"
"v_exp_f32 v48, v48\n"
"v_mfma_f32_16x16x32_bf16 v[184:187], a[64:67], v[32:35], v[184:187]\n"
"v_exp_f32 v49, v49\n"
"v_mfma_f32_16x16x32_bf16 v[188:191], a[68:71], v[32:35], v[188:191]\n"
"v_exp_f32 v50, v50\n"
"v_mfma_f32_16x16x32_bf16 v[192:195], a[72:75], v[32:35], v[192:195]\n"
"v_exp_f32 v51, v51\n"
"v_mfma_f32_16x16x32_bf16 v[196:199], a[76:79], v[32:35], v[196:199]\n"
"ds_read_b64_tr_b16 a[126:127], v251 offset:4352\n"
"v_cvt_pk_bf16_f32 v36, v48, v49\n"
"v_mfma_f32_16x16x32_bf16 v[200:203], a[80:83], v[32:35], v[200:203]\n"
"v_exp_f32 v52, v52\n"
"v_mfma_f32_16x16x32_bf16 v[204:207], a[84:87], v[32:35], v[204:207]\n"
"v_exp_f32 v53, v53\n"
"v_mfma_f32_16x16x32_bf16 v[208:211], a[88:91], v[32:35], v[208:211]\n"
"v_cvt_pk_bf16_f32 v37, v50, v51\n"
"v_mfma_f32_16x16x32_bf16 v[212:215], a[92:95], v[32:35], v[212:215]\n"
"s_waitcnt vmcnt(20) lgkmcnt(0)\n"
"s_barrier\n"
"v_mfma_f32_16x16x32_bf16 v[40:43], a[32:35], v[56:59], 0\n"
"s_add_u32 s61, s75, 12\n"
"v_exp_f32 v54, v54\n"
"v_mfma_f32_16x16x32_bf16 v[40:43], a[36:39], v[60:63], v[40:43]\n"
"s_and_b32 s61, s61, 15\n"
"v_exp_f32 v55, v55\n"
"v_mfma_f32_16x16x32_bf16 v[40:43], a[40:43], v[64:67], v[40:43]\n"
"s_lshl_b32 s61, s61, 14\n"
"ds_read_b128 a[0:3], v1 offset:16384\n"
"v_cvt_pk_bf16_f32 v38, v52, v53\n"
"v_mfma_f32_16x16x32_bf16 v[40:43], a[44:47], v[68:71], v[40:43]\n"
"s_add_u32 s71, s68, s61\n"
"ds_read_b128 a[4:7], v1 offset:16640\n"
"v_cvt_pk_bf16_f32 v39, v54, v55\n"
"v_mfma_f32_16x16x32_bf16 v[44:47], a[48:51], v[56:59], 0\n"
"s_add_u32 s76, s71, 0\n"
"ds_read_b128 a[8:11], v2 offset:16384\n"
"v_mfma_f32_16x16x32_bf16 v[44:47], a[52:55], v[60:63], v[44:47]\n"
"s_add_u32 m0, s68, 0\n"
"ds_read_b128 a[12:15], v2 offset:16640\n"
"ds_read_b128 a[16:19], v1 offset:20480\n"
"v_mfma_f32_16x16x32_bf16 v[44:47], a[56:59], v[64:67], v[44:47]\n"
"buffer_load_dwordx4 v9, s[28:31], s76 offen nt lds\n"
"ds_read_b128 a[20:23], v1 offset:20736\n"
"v_mfma_f32_16x16x32_bf16 v[44:47], a[60:63], v[68:71], v[44:47]\n"
"v_exp_f32 v40, v40\n"
"v_mfma_f32_16x16x32_bf16 v[232:235], a[80:83], v[36:39], v[232:235]\n"
"v_exp_f32 v41, v41\n"
"v_mfma_f32_16x16x32_bf16 v[236:239], a[84:87], v[36:39], v[236:239]\n"
"v_exp_f32 v42, v42\n"
"v_mfma_f32_16x16x32_bf16 v[240:243], a[88:91], v[36:39], v[240:243]\n"
"v_exp_f32 v43, v43\n"
"v_mfma_f32_16x16x32_bf16 v[244:247], a[92:95], v[36:39], v[244:247]\n"
"v_cvt_pk_bf16_f32 v32, v40, v41\n"
"v_mfma_f32_16x16x32_bf16 v[216:219], a[64:67], v[36:39], v[216:219]\n"
"v_exp_f32 v44, v44\n"
"v_mfma_f32_16x16x32_bf16 v[220:223], a[68:71], v[36:39], v[220:223]\n"
"v_exp_f32 v45, v45\n"
"v_mfma_f32_16x16x32_bf16 v[224:227], a[72:75], v[36:39], v[224:227]\n"
"s_add_u32 s77, s71, 4096\n"
"ds_read_b128 a[24:27], v2 offset:20480\n"
"v_cvt_pk_bf16_f32 v33, v42, v43\n"
"v_mfma_f32_16x16x32_bf16 v[228:231], a[76:79], v[36:39], v[228:231]\n"
"s_add_u32 m0, s68, 4096\n"
"ds_read_b128 a[28:31], v2 offset:20736\n"
"v_mfma_f32_16x16x32_bf16 v[28:31], v[20:23], v[36:39], v[28:31]\n"
"buffer_load_dwordx4 v9, s[28:31], s77 offen nt lds\n"
"ds_read_b64_tr_b16 a[64:65], v5 offset:16384\n"
"v_mfma_f32_16x16x32_bf16 v[48:51], a[32:35], v[72:75], 0\n"
"v_exp_f32 v46, v46\n"
"v_mfma_f32_16x16x32_bf16 v[48:51], a[36:39], v[76:79], v[48:51]\n"
"v_exp_f32 v47, v47\n"
"v_mfma_f32_16x16x32_bf16 v[48:51], a[40:43], v[80:83], v[48:51]\n"
"ds_read_b64_tr_b16 a[66:67], v5 offset:20480\n"
"v_cvt_pk_bf16_f32 v34, v44, v45\n"
"v_mfma_f32_16x16x32_bf16 v[48:51], a[44:47], v[84:87], v[48:51]\n"
"ds_read_b64_tr_b16 a[68:69], v6 offset:16384\n"
"v_cvt_pk_bf16_f32 v35, v46, v47\n"
"v_mfma_f32_16x16x32_bf16 v[52:55], a[48:51], v[72:75], 0\n"
"ds_read_b64_tr_b16 a[70:71], v6 offset:20480\n"
"ds_read_b64_tr_b16 a[72:73], v5 offset:16640\n"
"v_mfma_f32_16x16x32_bf16 v[52:55], a[52:55], v[76:79], v[52:55]\n"
"ds_read_b64_tr_b16 a[74:75], v5 offset:20736\n"
"ds_read_b64_tr_b16 a[76:77], v6 offset:16640\n"
"v_mfma_f32_16x16x32_bf16 v[52:55], a[56:59], v[80:83], v[52:55]\n"
"ds_read_b64_tr_b16 a[78:79], v6 offset:20736\n"
"v_mfma_f32_16x16x32_bf16 v[52:55], a[60:63], v[84:87], v[52:55]\n"
"s_add_u32 s76, s71, 8192\n"
"v_exp_f32 v48, v48\n"
"v_mfma_f32_16x16x32_bf16 v[120:123], a[96:99], v[32:35], v[120:123]\n"
"s_add_u32 m0, s68, 8192\n"
"v_exp_f32 v49, v49\n"
"v_mfma_f32_16x16x32_bf16 v[124:127], a[100:103], v[32:35], v[124:127]\n"
"buffer_load_dwordx4 v9, s[28:31], s76 offen nt lds\n"
"v_exp_f32 v50, v50\n"
"v_mfma_f32_16x16x32_bf16 v[128:131], a[104:107], v[32:35], v[128:131]\n"
"v_exp_f32 v51, v51\n"
"v_mfma_f32_16x16x32_bf16 v[132:135], a[108:111], v[32:35], v[132:135]\n"
"v_cvt_pk_bf16_f32 v36, v48, v49\n"
"v_add_f32 v16, v16, v48\n"
"v_mfma_f32_16x16x32_bf16 v[136:139], a[112:115], v[32:35], v[136:139]\n"
"v_exp_f32 v52, v52\n"
"v_mfma_f32_16x16x32_bf16 v[140:143], a[116:119], v[32:35], v[140:143]\n"
"v_exp_f32 v53, v53\n"
"v_mfma_f32_16x16x32_bf16 v[144:147], a[120:123], v[32:35], v[144:147]\n"
"v_cvt_pk_bf16_f32 v37, v50, v51\n"
"v_add_f32 v16, v16, v49\n"
"v_mfma_f32_16x16x32_bf16 v[148:151], a[124:127], v[32:35], v[148:151]\n"
"v_add_f32 v16, v16, v50\n"
"v_add_f32 v16, v16, v51\n"
"v_mfma_f32_16x16x32_bf16 v[24:27], v[20:23], v[32:35], v[24:27]\n"
"ds_read_b64_tr_b16 a[80:81], v248 offset:16384\n"
"ds_read_b64_tr_b16 a[82:83], v248 offset:20480\n"
"v_mfma_f32_16x16x32_bf16 v[40:43], a[32:35], v[88:91], 0\n"
"v_exp_f32 v54, v54\n"
"v_mfma_f32_16x16x32_bf16 v[40:43], a[36:39], v[92:95], v[40:43]\n"
"v_exp_f32 v55, v55\n"
"v_mfma_f32_16x16x32_bf16 v[40:43], a[40:43], v[96:99], v[40:43]\n"
"s_add_u32 s77, s71, 12288\n"
"v_cvt_pk_bf16_f32 v38, v52, v53\n"
"v_add_f32 v16, v16, v52\n"
"v_mfma_f32_16x16x32_bf16 v[40:43], a[44:47], v[100:103], v[40:43]\n"
"s_add_u32 m0, s68, 12288\n"
"v_cvt_pk_bf16_f32 v39, v54, v55\n"
"v_add_f32 v16, v16, v53\n"
"v_mfma_f32_16x16x32_bf16 v[44:47], a[48:51], v[88:91], 0\n"
"buffer_load_dwordx4 v9, s[28:31], s77 offen nt lds\n"
"v_add_f32 v16, v16, v54\n"
"v_mfma_f32_16x16x32_bf16 v[44:47], a[52:55], v[92:95], v[44:47]\n"
"ds_read_b64_tr_b16 a[84:85], v249 offset:16384\n"
"v_add_f32 v16, v16, v55\n"
"v_mfma_f32_16x16x32_bf16 v[44:47], a[56:59], v[96:99], v[44:47]\n"
"ds_read_b64_tr_b16 a[86:87], v249 offset:20480\n"
"ds_read_b64_tr_b16 a[88:89], v248 offset:16640\n"
"v_mfma_f32_16x16x32_bf16 v[44:47], a[60:63], v[100:103], v[44:47]\n"
"v_exp_f32 v40, v40\n"
"v_mfma_f32_16x16x32_bf16 v[152:155], a[96:99], v[36:39], v[152:155]\n"
"v_exp_f32 v41, v41\n"
"v_mfma_f32_16x16x32_bf16 v[156:159], a[100:103], v[36:39], v[156:159]\n"
"v_exp_f32 v42, v42\n"
"v_mfma_f32_16x16x32_bf16 v[160:163], a[104:107], v[36:39], v[160:163]\n"
"s_add_u32 s76, s71, 0\n"
"v_exp_f32 v43, v43\n"
"v_mfma_f32_16x16x32_bf16 v[164:167], a[108:111], v[36:39], v[164:167]\n"
"s_add_u32 m0, s68, 65536\n"
"v_cvt_pk_bf16_f32 v32, v40, v41\n"
"v_add_f32 v17, v17, v40\n"
"v_mfma_f32_16x16x32_bf16 v[168:171], a[112:115], v[36:39], v[168:171]\n"
"buffer_load_dwordx4 v10, s[88:91], s76 offen nt lds\n"
"v_exp_f32 v44, v44\n"
"v_mfma_f32_16x16x32_bf16 v[172:175], a[116:119], v[36:39], v[172:175]\n"
"v_exp_f32 v45, v45\n"
"v_mfma_f32_16x16x32_bf16 v[176:179], a[120:123], v[36:39], v[176:179]\n"
"v_cvt_pk_bf16_f32 v33, v42, v43\n"
"v_add_f32 v17, v17, v41\n"
"v_mfma_f32_16x16x32_bf16 v[180:183], a[124:127], v[36:39], v[180:183]\n"
"s_waitcnt lgkmcnt(13)\n"
"v_add_f32 v17, v17, v42\n"
"v_add_f32 v17, v17, v43\n"
"v_mfma_f32_16x16x32_bf16 v[48:51], a[0:3], v[56:59], 0\n"
"v_exp_f32 v46, v46\n"
"v_mfma_f32_16x16x32_bf16 v[48:51], a[4:7], v[60:63], v[48:51]\n"
"v_exp_f32 v47, v47\n"
"v_mfma_f32_16x16x32_bf16 v[48:51], a[8:11], v[64:67], v[48:51]\n"
"v_cvt_pk_bf16_f32 v34, v44, v45\n"
"v_add_f32 v17, v17, v44\n"
"v_mfma_f32_16x16x32_bf16 v[48:51], a[12:15], v[68:71], v[48:51]\n"
"v_cvt_pk_bf16_f32 v35, v46, v47\n"
"v_add_f32 v17, v17, v45\n"
"v_mfma_f32_16x16x32_bf16 v[52:55], a[16:19], v[56:59], 0\n"
"s_add_u32 s77, s71, 4096\n"
"ds_read_b64_tr_b16 a[90:91], v248 offset:20736\n"
"v_add_f32 v17, v17, v46\n"
"v_mfma_f32_16x16x32_bf16 v[52:55], a[20:23], v[60:63], v[52:55]\n"
"s_add_u32 m0, s68, 69632\n"
"ds_read_b64_tr_b16 a[92:93], v249 offset:16640\n"
"v_add_f32 v17, v17, v47\n"
"v_mfma_f32_16x16x32_bf16 v[52:55], a[24:27], v[64:67], v[52:55]\n"
"buffer_load_dwordx4 v10, s[88:91], s77 offen nt lds\n"
"ds_read_b64_tr_b16 a[94:95], v249 offset:20736\n"
"v_mfma_f32_16x16x32_bf16 v[52:55], a[28:31], v[68:71], v[52:55]\n"
"ds_read_b128 a[32:35], v3 offset:16384\n"
"v_exp_f32 v48, v48\n"
"v_mfma_f32_16x16x32_bf16 v[184:187], a[96:99], v[32:35], v[184:187]\n"
"v_exp_f32 v49, v49\n"
"v_mfma_f32_16x16x32_bf16 v[188:191], a[100:103], v[32:35], v[188:191]\n"
"ds_read_b128 a[36:39], v3 offset:16640\n"
"v_exp_f32 v50, v50\n"
"v_mfma_f32_16x16x32_bf16 v[192:195], a[104:107], v[32:35], v[192:195]\n"
"v_exp_f32 v51, v51\n"
"v_mfma_f32_16x16x32_bf16 v[196:199], a[108:111], v[32:35], v[196:199]\n"
"ds_read_b128 a[40:43], v4 offset:16384\n"
"v_cvt_pk_bf16_f32 v36, v48, v49\n"
"v_mfma_f32_16x16x32_bf16 v[200:203], a[112:115], v[32:35], v[200:203]\n"
"ds_read_b128 a[44:47], v4 offset:16640\n"
"v_exp_f32 v52, v52\n"
"v_mfma_f32_16x16x32_bf16 v[204:207], a[116:119], v[32:35], v[204:207]\n"
"v_exp_f32 v53, v53\n"
"v_mfma_f32_16x16x32_bf16 v[208:211], a[120:123], v[32:35], v[208:211]\n"
"ds_read_b128 a[48:51], v3 offset:20480\n"
"v_cvt_pk_bf16_f32 v37, v50, v51\n"
"v_mfma_f32_16x16x32_bf16 v[212:215], a[124:127], v[32:35], v[212:215]\n"
"ds_read_b128 a[52:55], v3 offset:20736\n"
"ds_read_b128 a[56:59], v4 offset:20480\n"
"v_mfma_f32_16x16x32_bf16 v[40:43], a[0:3], v[72:75], 0\n"
"s_add_u32 s76, s71, 8192\n"
"ds_read_b128 a[60:63], v4 offset:20736\n"
"v_exp_f32 v54, v54\n"
"v_mfma_f32_16x16x32_bf16 v[40:43], a[4:7], v[76:79], v[40:43]\n"
"s_add_u32 m0, s68, 73728\n"
"v_exp_f32 v55, v55\n"
"v_mfma_f32_16x16x32_bf16 v[40:43], a[8:11], v[80:83], v[40:43]\n"
"buffer_load_dwordx4 v10, s[88:91], s76 offen nt lds\n"
"v_cvt_pk_bf16_f32 v38, v52, v53\n"
"v_mfma_f32_16x16x32_bf16 v[40:43], a[12:15], v[84:87], v[40:43]\n"
"ds_read_b64_tr_b16 a[96:97], v7 offset:16384\n"
"v_cvt_pk_bf16_f32 v39, v54, v55\n"
"v_mfma_f32_16x16x32_bf16 v[44:47], a[16:19], v[72:75], 0\n"
"ds_read_b64_tr_b16 a[98:99], v7 offset:20480\n"
"ds_read_b64_tr_b16 a[100:101], v8 offset:16384\n"
"v_mfma_f32_16x16x32_bf16 v[44:47], a[20:23], v[76:79], v[44:47]\n"
"ds_read_b64_tr_b16 a[102:103], v8 offset:20480\n"
"ds_read_b64_tr_b16 a[104:105], v7 offset:16640\n"
"v_mfma_f32_16x16x32_bf16 v[44:47], a[24:27], v[80:83], v[44:47]\n"
"ds_read_b64_tr_b16 a[106:107], v7 offset:20736\n"
"ds_read_b64_tr_b16 a[108:109], v8 offset:16640\n"
"v_mfma_f32_16x16x32_bf16 v[44:47], a[28:31], v[84:87], v[44:47]\n"
"s_waitcnt lgkmcnt(15)\n"
"v_exp_f32 v40, v40\n"
"v_mfma_f32_16x16x32_bf16 v[120:123], a[64:67], v[36:39], v[120:123]\n"
"v_exp_f32 v41, v41\n"
"v_mfma_f32_16x16x32_bf16 v[124:127], a[68:71], v[36:39], v[124:127]\n"
"v_exp_f32 v42, v42\n"
"v_mfma_f32_16x16x32_bf16 v[128:131], a[72:75], v[36:39], v[128:131]\n"
"v_exp_f32 v43, v43\n"
"v_mfma_f32_16x16x32_bf16 v[132:135], a[76:79], v[36:39], v[132:135]\n"
"v_cvt_pk_bf16_f32 v32, v40, v41\n"
"v_add_f32 v16, v16, v40\n"
"v_mfma_f32_16x16x32_bf16 v[136:139], a[80:83], v[36:39], v[136:139]\n"
"v_exp_f32 v44, v44\n"
"v_mfma_f32_16x16x32_bf16 v[140:143], a[84:87], v[36:39], v[140:143]\n"
"v_exp_f32 v45, v45\n"
"v_mfma_f32_16x16x32_bf16 v[144:147], a[88:91], v[36:39], v[144:147]\n"
"s_add_u32 s77, s71, 12288\n"
"v_cvt_pk_bf16_f32 v33, v42, v43\n"
"v_add_f32 v16, v16, v41\n"
"v_mfma_f32_16x16x32_bf16 v[148:151], a[92:95], v[36:39], v[148:151]\n"
"s_add_u32 m0, s68, 77824\n"
"v_add_f32 v16, v16, v42\n"
"v_add_f32 v16, v16, v43\n"
"v_mfma_f32_16x16x32_bf16 v[24:27], v[20:23], v[36:39], v[24:27]\n"
"buffer_load_dwordx4 v10, s[88:91], s77 offen nt lds\n"
"ds_read_b64_tr_b16 a[110:111], v8 offset:20736\n"
"v_mfma_f32_16x16x32_bf16 v[48:51], a[0:3], v[88:91], 0\n"
"v_exp_f32 v46, v46\n"
"v_mfma_f32_16x16x32_bf16 v[48:51], a[4:7], v[92:95], v[48:51]\n"
"v_exp_f32 v47, v47\n"
"v_mfma_f32_16x16x32_bf16 v[48:51], a[8:11], v[96:99], v[48:51]\n"
"s_mov_b32 s70, 2621440\n"
"v_cvt_pk_bf16_f32 v34, v44, v45\n"
"v_add_f32 v16, v16, v44\n"
"v_mfma_f32_16x16x32_bf16 v[48:51], a[12:15], v[100:103], v[48:51]\n"
"v_cvt_pk_bf16_f32 v35, v46, v47\n"
"v_add_f32 v16, v16, v45\n"
"v_mfma_f32_16x16x32_bf16 v[52:55], a[16:19], v[88:91], 0\n"
"buffer_load_dword v0, v254, s[40:43], s70 offen\n"
"v_add_f32 v16, v16, v46\n"
"v_mfma_f32_16x16x32_bf16 v[52:55], a[20:23], v[92:95], v[52:55]\n"
"ds_read_b64_tr_b16 a[112:113], v250 offset:16384\n"
"v_add_f32 v16, v16, v47\n"
"v_mfma_f32_16x16x32_bf16 v[52:55], a[24:27], v[96:99], v[52:55]\n"
"ds_read_b64_tr_b16 a[114:115], v250 offset:20480\n"
"ds_read_b64_tr_b16 a[116:117], v251 offset:16384\n"
"v_mfma_f32_16x16x32_bf16 v[52:55], a[28:31], v[100:103], v[52:55]\n"
"v_exp_f32 v48, v48\n"
"v_mfma_f32_16x16x32_bf16 v[152:155], a[64:67], v[32:35], v[152:155]\n"
"v_exp_f32 v49, v49\n"
"v_mfma_f32_16x16x32_bf16 v[156:159], a[68:71], v[32:35], v[156:159]\n"
"v_exp_f32 v50, v50\n"
"v_mfma_f32_16x16x32_bf16 v[160:163], a[72:75], v[32:35], v[160:163]\n"
"v_exp_f32 v51, v51\n"
"v_mfma_f32_16x16x32_bf16 v[164:167], a[76:79], v[32:35], v[164:167]\n"
"v_cvt_pk_bf16_f32 v36, v48, v49\n"
"v_add_f32 v17, v17, v48\n"
"v_mfma_f32_16x16x32_bf16 v[168:171], a[80:83], v[32:35], v[168:171]\n"
"v_exp_f32 v52, v52\n"
"v_mfma_f32_16x16x32_bf16 v[172:175], a[84:87], v[32:35], v[172:175]\n"
"v_exp_f32 v53, v53\n"
"v_mfma_f32_16x16x32_bf16 v[176:179], a[88:91], v[32:35], v[176:179]\n"
"v_cvt_pk_bf16_f32 v37, v50, v51\n"
"v_add_f32 v17, v17, v49\n"
"v_mfma_f32_16x16x32_bf16 v[180:183], a[92:95], v[32:35], v[180:183]\n"
"v_add_f32 v17, v17, v50\n"
"v_add_f32 v17, v17, v51\n"
"v_mfma_f32_16x16x32_bf16 v[40:43], a[0:3], v[104:107], 0\n"
"v_exp_f32 v54, v54\n"
"v_mfma_f32_16x16x32_bf16 v[40:43], a[4:7], v[108:111], v[40:43]\n"
"v_exp_f32 v55, v55\n"
"v_mfma_f32_16x16x32_bf16 v[40:43], a[8:11], v[112:115], v[40:43]\n"
"v_cvt_pk_bf16_f32 v38, v52, v53\n"
"v_add_f32 v17, v17, v52\n"
"v_mfma_f32_16x16x32_bf16 v[40:43], a[12:15], v[116:119], v[40:43]\n"
"v_cvt_pk_bf16_f32 v39, v54, v55\n"
"v_add_f32 v17, v17, v53\n"
"v_mfma_f32_16x16x32_bf16 v[44:47], a[16:19], v[104:107], 0\n"
"ds_read_b64_tr_b16 a[118:119], v251 offset:20480\n"
"v_add_f32 v17, v17, v54\n"
"v_mfma_f32_16x16x32_bf16 v[44:47], a[20:23], v[108:111], v[44:47]\n"
"ds_read_b64_tr_b16 a[120:121], v250 offset:16640\n"
"v_add_f32 v17, v17, v55\n"
"v_mfma_f32_16x16x32_bf16 v[44:47], a[24:27], v[112:115], v[44:47]\n"
"ds_read_b64_tr_b16 a[122:123], v250 offset:20736\n"
"ds_read_b64_tr_b16 a[124:125], v251 offset:16640\n"
"v_mfma_f32_16x16x32_bf16 v[44:47], a[28:31], v[116:119], v[44:47]\n"
"v_exp_f32 v40, v40\n"
"v_mfma_f32_16x16x32_bf16 v[184:187], a[64:67], v[36:39], v[184:187]\n"
"v_exp_f32 v41, v41\n"
"v_mfma_f32_16x16x32_bf16 v[188:191], a[68:71], v[36:39], v[188:191]\n"
"v_exp_f32 v42, v42\n"
"v_mfma_f32_16x16x32_bf16 v[192:195], a[72:75], v[36:39], v[192:195]\n"
"v_exp_f32 v43, v43\n"
"v_mfma_f32_16x16x32_bf16 v[196:199], a[76:79], v[36:39], v[196:199]\n"
"ds_read_b64_tr_b16 a[126:127], v251 offset:20736\n"
"v_cvt_pk_bf16_f32 v32, v40, v41\n"
"v_mfma_f32_16x16x32_bf16 v[200:203], a[80:83], v[36:39], v[200:203]\n"
"v_exp_f32 v44, v44\n"
"v_mfma_f32_16x16x32_bf16 v[204:207], a[84:87], v[36:39], v[204:207]\n"
"v_exp_f32 v45, v45\n"
"v_mfma_f32_16x16x32_bf16 v[208:211], a[88:91], v[36:39], v[208:211]\n"
"v_cvt_pk_bf16_f32 v33, v42, v43\n"
"v_mfma_f32_16x16x32_bf16 v[212:215], a[92:95], v[36:39], v[212:215]\n"
"s_waitcnt vmcnt(19) lgkmcnt(0)\n"
"s_barrier\n"
"v_mfma_f32_16x16x32_bf16 v[48:51], a[32:35], v[56:59], 0\n"
"s_add_u32 s61, s75, 13\n"
"v_exp_f32 v46, v46\n"
"v_mfma_f32_16x16x32_bf16 v[48:51], a[36:39], v[60:63], v[48:51]\n"
"s_and_b32 s61, s61, 15\n"
"v_exp_f32 v47, v47\n"
"v_mfma_f32_16x16x32_bf16 v[48:51], a[40:43], v[64:67], v[48:51]\n"
"s_lshl_b32 s61, s61, 14\n"
"ds_read_b128 a[0:3], v1 offset:32768\n"
"v_cvt_pk_bf16_f32 v34, v44, v45\n"
"v_mfma_f32_16x16x32_bf16 v[48:51], a[44:47], v[68:71], v[48:51]\n"
"s_add_u32 s71, s68, s61\n"
"ds_read_b128 a[4:7], v1 offset:33024\n"
"v_cvt_pk_bf16_f32 v35, v46, v47\n"
"v_mfma_f32_16x16x32_bf16 v[52:55], a[48:51], v[56:59], 0\n"
"s_add_u32 s76, s71, 0\n"
"ds_read_b128 a[8:11], v2 offset:32768\n"
"v_mfma_f32_16x16x32_bf16 v[52:55], a[52:55], v[60:63], v[52:55]\n"
"s_add_u32 m0, s68, 16384\n"
"ds_read_b128 a[12:15], v2 offset:33024\n"
"ds_read_b128 a[16:19], v1 offset:36864\n"
"v_mfma_f32_16x16x32_bf16 v[52:55], a[56:59], v[64:67], v[52:55]\n"
"buffer_load_dwordx4 v9, s[28:31], s76 offen nt lds\n"
"ds_read_b128 a[20:23], v1 offset:37120\n"
"v_mfma_f32_16x16x32_bf16 v[52:55], a[60:63], v[68:71], v[52:55]\n"
"v_exp_f32 v48, v48\n"
"v_mfma_f32_16x16x32_bf16 v[232:235], a[80:83], v[32:35], v[232:235]\n"
"v_exp_f32 v49, v49\n"
"v_mfma_f32_16x16x32_bf16 v[236:239], a[84:87], v[32:35], v[236:239]\n"
"v_exp_f32 v50, v50\n"
"v_mfma_f32_16x16x32_bf16 v[240:243], a[88:91], v[32:35], v[240:243]\n"
"v_exp_f32 v51, v51\n"
"v_mfma_f32_16x16x32_bf16 v[244:247], a[92:95], v[32:35], v[244:247]\n"
"v_cvt_pk_bf16_f32 v36, v48, v49\n"
"v_mfma_f32_16x16x32_bf16 v[216:219], a[64:67], v[32:35], v[216:219]\n"
"v_exp_f32 v52, v52\n"
"v_mfma_f32_16x16x32_bf16 v[220:223], a[68:71], v[32:35], v[220:223]\n"
"v_exp_f32 v53, v53\n"
"v_mfma_f32_16x16x32_bf16 v[224:227], a[72:75], v[32:35], v[224:227]\n"
"s_add_u32 s77, s71, 4096\n"
"ds_read_b128 a[24:27], v2 offset:36864\n"
"v_cvt_pk_bf16_f32 v37, v50, v51\n"
"v_mfma_f32_16x16x32_bf16 v[228:231], a[76:79], v[32:35], v[228:231]\n"
"s_add_u32 m0, s68, 20480\n"
"ds_read_b128 a[28:31], v2 offset:37120\n"
"v_mfma_f32_16x16x32_bf16 v[28:31], v[20:23], v[32:35], v[28:31]\n"
"buffer_load_dwordx4 v9, s[28:31], s77 offen nt lds\n"
"ds_read_b64_tr_b16 a[64:65], v5 offset:32768\n"
"v_mfma_f32_16x16x32_bf16 v[40:43], a[32:35], v[72:75], 0\n"
"v_exp_f32 v54, v54\n"
"v_mfma_f32_16x16x32_bf16 v[40:43], a[36:39], v[76:79], v[40:43]\n"
"v_exp_f32 v55, v55\n"
"v_mfma_f32_16x16x32_bf16 v[40:43], a[40:43], v[80:83], v[40:43]\n"
"ds_read_b64_tr_b16 a[66:67], v5 offset:36864\n"
"v_cvt_pk_bf16_f32 v38, v52, v53\n"
"v_mfma_f32_16x16x32_bf16 v[40:43], a[44:47], v[84:87], v[40:43]\n"
"ds_read_b64_tr_b16 a[68:69], v6 offset:32768\n"
"v_cvt_pk_bf16_f32 v39, v54, v55\n"
"v_mfma_f32_16x16x32_bf16 v[44:47], a[48:51], v[72:75], 0\n"
"ds_read_b64_tr_b16 a[70:71], v6 offset:36864\n"
"ds_read_b64_tr_b16 a[72:73], v5 offset:33024\n"
"v_mfma_f32_16x16x32_bf16 v[44:47], a[52:55], v[76:79], v[44:47]\n"
"ds_read_b64_tr_b16 a[74:75], v5 offset:37120\n"
"ds_read_b64_tr_b16 a[76:77], v6 offset:33024\n"
"v_mfma_f32_16x16x32_bf16 v[44:47], a[56:59], v[80:83], v[44:47]\n"
"ds_read_b64_tr_b16 a[78:79], v6 offset:37120\n"
"v_mfma_f32_16x16x32_bf16 v[44:47], a[60:63], v[84:87], v[44:47]\n"
"s_add_u32 s76, s71, 8192\n"
"v_exp_f32 v40, v40\n"
"v_mfma_f32_16x16x32_bf16 v[120:123], a[96:99], v[36:39], v[120:123]\n"
"s_add_u32 m0, s68, 24576\n"
"v_exp_f32 v41, v41\n"
"v_mfma_f32_16x16x32_bf16 v[124:127], a[100:103], v[36:39], v[124:127]\n"
"buffer_load_dwordx4 v9, s[28:31], s76 offen nt lds\n"
"v_exp_f32 v42, v42\n"
"v_mfma_f32_16x16x32_bf16 v[128:131], a[104:107], v[36:39], v[128:131]\n"
"v_exp_f32 v43, v43\n"
"v_mfma_f32_16x16x32_bf16 v[132:135], a[108:111], v[36:39], v[132:135]\n"
"v_cvt_pk_bf16_f32 v32, v40, v41\n"
"v_add_f32 v16, v16, v40\n"
"v_mfma_f32_16x16x32_bf16 v[136:139], a[112:115], v[36:39], v[136:139]\n"
"v_exp_f32 v44, v44\n"
"v_mfma_f32_16x16x32_bf16 v[140:143], a[116:119], v[36:39], v[140:143]\n"
"v_exp_f32 v45, v45\n"
"v_mfma_f32_16x16x32_bf16 v[144:147], a[120:123], v[36:39], v[144:147]\n"
"v_cvt_pk_bf16_f32 v33, v42, v43\n"
"v_add_f32 v16, v16, v41\n"
"v_mfma_f32_16x16x32_bf16 v[148:151], a[124:127], v[36:39], v[148:151]\n"
"v_add_f32 v16, v16, v42\n"
"v_add_f32 v16, v16, v43\n"
"v_mfma_f32_16x16x32_bf16 v[24:27], v[20:23], v[36:39], v[24:27]\n"
"ds_read_b64_tr_b16 a[80:81], v248 offset:32768\n"
"ds_read_b64_tr_b16 a[82:83], v248 offset:36864\n"
"v_mfma_f32_16x16x32_bf16 v[48:51], a[32:35], v[88:91], 0\n"
"v_exp_f32 v46, v46\n"
"v_mfma_f32_16x16x32_bf16 v[48:51], a[36:39], v[92:95], v[48:51]\n"
"v_exp_f32 v47, v47\n"
"v_mfma_f32_16x16x32_bf16 v[48:51], a[40:43], v[96:99], v[48:51]\n"
"s_add_u32 s77, s71, 12288\n"
"v_cvt_pk_bf16_f32 v34, v44, v45\n"
"v_add_f32 v16, v16, v44\n"
"v_mfma_f32_16x16x32_bf16 v[48:51], a[44:47], v[100:103], v[48:51]\n"
"s_add_u32 m0, s68, 28672\n"
"v_cvt_pk_bf16_f32 v35, v46, v47\n"
"v_add_f32 v16, v16, v45\n"
"v_mfma_f32_16x16x32_bf16 v[52:55], a[48:51], v[88:91], 0\n"
"buffer_load_dwordx4 v9, s[28:31], s77 offen nt lds\n"
"v_add_f32 v16, v16, v46\n"
"v_mfma_f32_16x16x32_bf16 v[52:55], a[52:55], v[92:95], v[52:55]\n"
"ds_read_b64_tr_b16 a[84:85], v249 offset:32768\n"
"v_add_f32 v16, v16, v47\n"
"v_mfma_f32_16x16x32_bf16 v[52:55], a[56:59], v[96:99], v[52:55]\n"
"ds_read_b64_tr_b16 a[86:87], v249 offset:36864\n"
"ds_read_b64_tr_b16 a[88:89], v248 offset:33024\n"
"v_mfma_f32_16x16x32_bf16 v[52:55], a[60:63], v[100:103], v[52:55]\n"
"v_exp_f32 v48, v48\n"
"v_mfma_f32_16x16x32_bf16 v[152:155], a[96:99], v[32:35], v[152:155]\n"
"v_exp_f32 v49, v49\n"
"v_mfma_f32_16x16x32_bf16 v[156:159], a[100:103], v[32:35], v[156:159]\n"
"v_exp_f32 v50, v50\n"
"v_mfma_f32_16x16x32_bf16 v[160:163], a[104:107], v[32:35], v[160:163]\n"
"s_add_u32 s76, s71, 0\n"
"v_exp_f32 v51, v51\n"
"v_mfma_f32_16x16x32_bf16 v[164:167], a[108:111], v[32:35], v[164:167]\n"
"s_add_u32 m0, s68, 81920\n"
"v_cvt_pk_bf16_f32 v36, v48, v49\n"
"v_add_f32 v17, v17, v48\n"
"v_mfma_f32_16x16x32_bf16 v[168:171], a[112:115], v[32:35], v[168:171]\n"
"buffer_load_dwordx4 v10, s[88:91], s76 offen nt lds\n"
"v_exp_f32 v52, v52\n"
"v_mfma_f32_16x16x32_bf16 v[172:175], a[116:119], v[32:35], v[172:175]\n"
"v_exp_f32 v53, v53\n"
"v_mfma_f32_16x16x32_bf16 v[176:179], a[120:123], v[32:35], v[176:179]\n"
"v_cvt_pk_bf16_f32 v37, v50, v51\n"
"v_add_f32 v17, v17, v49\n"
"v_mfma_f32_16x16x32_bf16 v[180:183], a[124:127], v[32:35], v[180:183]\n"
"s_waitcnt lgkmcnt(13)\n"
"v_add_f32 v17, v17, v50\n"
"v_add_f32 v17, v17, v51\n"
"v_mfma_f32_16x16x32_bf16 v[40:43], a[0:3], v[56:59], 0\n"
"v_exp_f32 v54, v54\n"
"v_mfma_f32_16x16x32_bf16 v[40:43], a[4:7], v[60:63], v[40:43]\n"
"v_exp_f32 v55, v55\n"
"v_mfma_f32_16x16x32_bf16 v[40:43], a[8:11], v[64:67], v[40:43]\n"
"v_cvt_pk_bf16_f32 v38, v52, v53\n"
"v_add_f32 v17, v17, v52\n"
"v_mfma_f32_16x16x32_bf16 v[40:43], a[12:15], v[68:71], v[40:43]\n"
"v_cvt_pk_bf16_f32 v39, v54, v55\n"
"v_add_f32 v17, v17, v53\n"
"v_mfma_f32_16x16x32_bf16 v[44:47], a[16:19], v[56:59], 0\n"
"s_add_u32 s77, s71, 4096\n"
"ds_read_b64_tr_b16 a[90:91], v248 offset:37120\n"
"v_add_f32 v17, v17, v54\n"
"v_mfma_f32_16x16x32_bf16 v[44:47], a[20:23], v[60:63], v[44:47]\n"
"s_add_u32 m0, s68, 86016\n"
"ds_read_b64_tr_b16 a[92:93], v249 offset:33024\n"
"v_add_f32 v17, v17, v55\n"
"v_mfma_f32_16x16x32_bf16 v[44:47], a[24:27], v[64:67], v[44:47]\n"
"buffer_load_dwordx4 v10, s[88:91], s77 offen nt lds\n"
"ds_read_b64_tr_b16 a[94:95], v249 offset:37120\n"
"v_mfma_f32_16x16x32_bf16 v[44:47], a[28:31], v[68:71], v[44:47]\n"
"ds_read_b128 a[32:35], v3 offset:32768\n"
"v_exp_f32 v40, v40\n"
"v_mfma_f32_16x16x32_bf16 v[184:187], a[96:99], v[36:39], v[184:187]\n"
"v_exp_f32 v41, v41\n"
"v_mfma_f32_16x16x32_bf16 v[188:191], a[100:103], v[36:39], v[188:191]\n"
"ds_read_b128 a[36:39], v3 offset:33024\n"
"v_exp_f32 v42, v42\n"
"v_mfma_f32_16x16x32_bf16 v[192:195], a[104:107], v[36:39], v[192:195]\n"
"v_exp_f32 v43, v43\n"
"v_mfma_f32_16x16x32_bf16 v[196:199], a[108:111], v[36:39], v[196:199]\n"
"ds_read_b128 a[40:43], v4 offset:32768\n"
"v_cvt_pk_bf16_f32 v32, v40, v41\n"
"v_mfma_f32_16x16x32_bf16 v[200:203], a[112:115], v[36:39], v[200:203]\n"
"ds_read_b128 a[44:47], v4 offset:33024\n"
"v_exp_f32 v44, v44\n"
"v_mfma_f32_16x16x32_bf16 v[204:207], a[116:119], v[36:39], v[204:207]\n"
"v_exp_f32 v45, v45\n"
"v_mfma_f32_16x16x32_bf16 v[208:211], a[120:123], v[36:39], v[208:211]\n"
"ds_read_b128 a[48:51], v3 offset:36864\n"
"v_cvt_pk_bf16_f32 v33, v42, v43\n"
"v_mfma_f32_16x16x32_bf16 v[212:215], a[124:127], v[36:39], v[212:215]\n"
"ds_read_b128 a[52:55], v3 offset:37120\n"
"ds_read_b128 a[56:59], v4 offset:36864\n"
"v_mfma_f32_16x16x32_bf16 v[48:51], a[0:3], v[72:75], 0\n"
"s_add_u32 s76, s71, 8192\n"
"ds_read_b128 a[60:63], v4 offset:37120\n"
"v_exp_f32 v46, v46\n"
"v_mfma_f32_16x16x32_bf16 v[48:51], a[4:7], v[76:79], v[48:51]\n"
"s_add_u32 m0, s68, 90112\n"
"v_exp_f32 v47, v47\n"
"v_mfma_f32_16x16x32_bf16 v[48:51], a[8:11], v[80:83], v[48:51]\n"
"buffer_load_dwordx4 v10, s[88:91], s76 offen nt lds\n"
"v_cvt_pk_bf16_f32 v34, v44, v45\n"
"v_mfma_f32_16x16x32_bf16 v[48:51], a[12:15], v[84:87], v[48:51]\n"
"ds_read_b64_tr_b16 a[96:97], v7 offset:32768\n"
"v_cvt_pk_bf16_f32 v35, v46, v47\n"
"v_mfma_f32_16x16x32_bf16 v[52:55], a[16:19], v[72:75], 0\n"
"ds_read_b64_tr_b16 a[98:99], v7 offset:36864\n"
"ds_read_b64_tr_b16 a[100:101], v8 offset:32768\n"
"v_mfma_f32_16x16x32_bf16 v[52:55], a[20:23], v[76:79], v[52:55]\n"
"ds_read_b64_tr_b16 a[102:103], v8 offset:36864\n"
"ds_read_b64_tr_b16 a[104:105], v7 offset:33024\n"
"v_mfma_f32_16x16x32_bf16 v[52:55], a[24:27], v[80:83], v[52:55]\n"
"ds_read_b64_tr_b16 a[106:107], v7 offset:37120\n"
"ds_read_b64_tr_b16 a[108:109], v8 offset:33024\n"
"v_mfma_f32_16x16x32_bf16 v[52:55], a[28:31], v[84:87], v[52:55]\n"
"s_waitcnt lgkmcnt(15)\n"
"v_exp_f32 v48, v48\n"
"v_mfma_f32_16x16x32_bf16 v[120:123], a[64:67], v[32:35], v[120:123]\n"
"v_exp_f32 v49, v49\n"
"v_mfma_f32_16x16x32_bf16 v[124:127], a[68:71], v[32:35], v[124:127]\n"
"v_exp_f32 v50, v50\n"
"v_mfma_f32_16x16x32_bf16 v[128:131], a[72:75], v[32:35], v[128:131]\n"
"v_exp_f32 v51, v51\n"
"v_mfma_f32_16x16x32_bf16 v[132:135], a[76:79], v[32:35], v[132:135]\n"
"v_cvt_pk_bf16_f32 v36, v48, v49\n"
"v_add_f32 v16, v16, v48\n"
"v_mfma_f32_16x16x32_bf16 v[136:139], a[80:83], v[32:35], v[136:139]\n"
"v_exp_f32 v52, v52\n"
"v_mfma_f32_16x16x32_bf16 v[140:143], a[84:87], v[32:35], v[140:143]\n"
"v_exp_f32 v53, v53\n"
"v_mfma_f32_16x16x32_bf16 v[144:147], a[88:91], v[32:35], v[144:147]\n"
"s_add_u32 s77, s71, 12288\n"
"v_cvt_pk_bf16_f32 v37, v50, v51\n"
"v_add_f32 v16, v16, v49\n"
"v_mfma_f32_16x16x32_bf16 v[148:151], a[92:95], v[32:35], v[148:151]\n"
"s_add_u32 m0, s68, 94208\n"
"v_add_f32 v16, v16, v50\n"
"v_add_f32 v16, v16, v51\n"
"v_mfma_f32_16x16x32_bf16 v[24:27], v[20:23], v[32:35], v[24:27]\n"
"buffer_load_dwordx4 v10, s[88:91], s77 offen nt lds\n"
"ds_read_b64_tr_b16 a[110:111], v8 offset:37120\n"
"v_mfma_f32_16x16x32_bf16 v[40:43], a[0:3], v[88:91], 0\n"
"v_exp_f32 v54, v54\n"
"v_mfma_f32_16x16x32_bf16 v[40:43], a[4:7], v[92:95], v[40:43]\n"
"v_exp_f32 v55, v55\n"
"v_mfma_f32_16x16x32_bf16 v[40:43], a[8:11], v[96:99], v[40:43]\n"
"s_mov_b32 s70, 3145728\n"
"v_cvt_pk_bf16_f32 v38, v52, v53\n"
"v_add_f32 v16, v16, v52\n"
"v_mfma_f32_16x16x32_bf16 v[40:43], a[12:15], v[100:103], v[40:43]\n"
"v_cvt_pk_bf16_f32 v39, v54, v55\n"
"v_add_f32 v16, v16, v53\n"
"v_mfma_f32_16x16x32_bf16 v[44:47], a[16:19], v[88:91], 0\n"
"buffer_load_dword v0, v254, s[40:43], s70 offen\n"
"v_add_f32 v16, v16, v54\n"
"v_mfma_f32_16x16x32_bf16 v[44:47], a[20:23], v[92:95], v[44:47]\n"
"ds_read_b64_tr_b16 a[112:113], v250 offset:32768\n"
"v_add_f32 v16, v16, v55\n"
"v_mfma_f32_16x16x32_bf16 v[44:47], a[24:27], v[96:99], v[44:47]\n"
"ds_read_b64_tr_b16 a[114:115], v250 offset:36864\n"
"ds_read_b64_tr_b16 a[116:117], v251 offset:32768\n"
"v_mfma_f32_16x16x32_bf16 v[44:47], a[28:31], v[100:103], v[44:47]\n"
"v_exp_f32 v40, v40\n"
"v_mfma_f32_16x16x32_bf16 v[152:155], a[64:67], v[36:39], v[152:155]\n"
"v_exp_f32 v41, v41\n"
"v_mfma_f32_16x16x32_bf16 v[156:159], a[68:71], v[36:39], v[156:159]\n"
"v_exp_f32 v42, v42\n"
"v_mfma_f32_16x16x32_bf16 v[160:163], a[72:75], v[36:39], v[160:163]\n"
"v_exp_f32 v43, v43\n"
"v_mfma_f32_16x16x32_bf16 v[164:167], a[76:79], v[36:39], v[164:167]\n"
"v_cvt_pk_bf16_f32 v32, v40, v41\n"
"v_add_f32 v17, v17, v40\n"
"v_mfma_f32_16x16x32_bf16 v[168:171], a[80:83], v[36:39], v[168:171]\n"
"v_exp_f32 v44, v44\n"
"v_mfma_f32_16x16x32_bf16 v[172:175], a[84:87], v[36:39], v[172:175]\n"
"v_exp_f32 v45, v45\n"
"v_mfma_f32_16x16x32_bf16 v[176:179], a[88:91], v[36:39], v[176:179]\n"
"v_cvt_pk_bf16_f32 v33, v42, v43\n"
"v_add_f32 v17, v17, v41\n"
"v_mfma_f32_16x16x32_bf16 v[180:183], a[92:95], v[36:39], v[180:183]\n"
"v_add_f32 v17, v17, v42\n"
"v_add_f32 v17, v17, v43\n"
"v_mfma_f32_16x16x32_bf16 v[48:51], a[0:3], v[104:107], 0\n"
"v_exp_f32 v46, v46\n"
"v_mfma_f32_16x16x32_bf16 v[48:51], a[4:7], v[108:111], v[48:51]\n"
"v_exp_f32 v47, v47\n"
"v_mfma_f32_16x16x32_bf16 v[48:51], a[8:11], v[112:115], v[48:51]\n"
"v_cvt_pk_bf16_f32 v34, v44, v45\n"
"v_add_f32 v17, v17, v44\n"
"v_mfma_f32_16x16x32_bf16 v[48:51], a[12:15], v[116:119], v[48:51]\n"
"v_cvt_pk_bf16_f32 v35, v46, v47\n"
"v_add_f32 v17, v17, v45\n"
"v_mfma_f32_16x16x32_bf16 v[52:55], a[16:19], v[104:107], 0\n"
"ds_read_b64_tr_b16 a[118:119], v251 offset:36864\n"
"v_add_f32 v17, v17, v46\n"
"v_mfma_f32_16x16x32_bf16 v[52:55], a[20:23], v[108:111], v[52:55]\n"
"ds_read_b64_tr_b16 a[120:121], v250 offset:33024\n"
"v_add_f32 v17, v17, v47\n"
"v_mfma_f32_16x16x32_bf16 v[52:55], a[24:27], v[112:115], v[52:55]\n"
"ds_read_b64_tr_b16 a[122:123], v250 offset:37120\n"
"ds_read_b64_tr_b16 a[124:125], v251 offset:33024\n"
"v_mfma_f32_16x16x32_bf16 v[52:55], a[28:31], v[116:119], v[52:55]\n"
"v_exp_f32 v48, v48\n"
"v_mfma_f32_16x16x32_bf16 v[184:187], a[64:67], v[32:35], v[184:187]\n"
"v_exp_f32 v49, v49\n"
"v_mfma_f32_16x16x32_bf16 v[188:191], a[68:71], v[32:35], v[188:191]\n"
"v_exp_f32 v50, v50\n"
"v_mfma_f32_16x16x32_bf16 v[192:195], a[72:75], v[32:35], v[192:195]\n"
"v_exp_f32 v51, v51\n"
"v_mfma_f32_16x16x32_bf16 v[196:199], a[76:79], v[32:35], v[196:199]\n"
"ds_read_b64_tr_b16 a[126:127], v251 offset:37120\n"
"v_cvt_pk_bf16_f32 v36, v48, v49\n"
"v_mfma_f32_16x16x32_bf16 v[200:203], a[80:83], v[32:35], v[200:203]\n"
"v_exp_f32 v52, v52\n"
"v_mfma_f32_16x16x32_bf16 v[204:207], a[84:87], v[32:35], v[204:207]\n"
"v_exp_f32 v53, v53\n"
"v_mfma_f32_16x16x32_bf16 v[208:211], a[88:91], v[32:35], v[208:211]\n"
"v_cvt_pk_bf16_f32 v37, v50, v51\n"
"v_mfma_f32_16x16x32_bf16 v[212:215], a[92:95], v[32:35], v[212:215]\n"
"s_waitcnt vmcnt(19) lgkmcnt(0)\n"
"s_barrier\n"
"v_mfma_f32_16x16x32_bf16 v[40:43], a[32:35], v[56:59], 0\n"
"s_add_u32 s61, s75, 14\n"
"v_exp_f32 v54, v54\n"
"v_mfma_f32_16x16x32_bf16 v[40:43], a[36:39], v[60:63], v[40:43]\n"
"s_and_b32 s61, s61, 15\n"
"v_exp_f32 v55, v55\n"
"v_mfma_f32_16x16x32_bf16 v[40:43], a[40:43], v[64:67], v[40:43]\n"
"s_lshl_b32 s61, s61, 14\n"
"ds_read_b128 a[0:3], v1 offset:49152\n"
"v_cvt_pk_bf16_f32 v38, v52, v53\n"
"v_mfma_f32_16x16x32_bf16 v[40:43], a[44:47], v[68:71], v[40:43]\n"
"s_add_u32 s71, s68, s61\n"
"ds_read_b128 a[4:7], v1 offset:49408\n"
"v_cvt_pk_bf16_f32 v39, v54, v55\n"
"v_mfma_f32_16x16x32_bf16 v[44:47], a[48:51], v[56:59], 0\n"
"s_add_u32 s76, s71, 0\n"
"ds_read_b128 a[8:11], v2 offset:49152\n"
"v_mfma_f32_16x16x32_bf16 v[44:47], a[52:55], v[60:63], v[44:47]\n"
"s_add_u32 m0, s68, 32768\n"
"ds_read_b128 a[12:15], v2 offset:49408\n"
"ds_read_b128 a[16:19], v1 offset:53248\n"
"v_mfma_f32_16x16x32_bf16 v[44:47], a[56:59], v[64:67], v[44:47]\n"
"buffer_load_dwordx4 v9, s[28:31], s76 offen nt lds\n"
"ds_read_b128 a[20:23], v1 offset:53504\n"
"v_mfma_f32_16x16x32_bf16 v[44:47], a[60:63], v[68:71], v[44:47]\n"
"v_exp_f32 v40, v40\n"
"v_mfma_f32_16x16x32_bf16 v[232:235], a[80:83], v[36:39], v[232:235]\n"
"v_exp_f32 v41, v41\n"
"v_mfma_f32_16x16x32_bf16 v[236:239], a[84:87], v[36:39], v[236:239]\n"
"v_exp_f32 v42, v42\n"
"v_mfma_f32_16x16x32_bf16 v[240:243], a[88:91], v[36:39], v[240:243]\n"
"v_exp_f32 v43, v43\n"
"v_mfma_f32_16x16x32_bf16 v[244:247], a[92:95], v[36:39], v[244:247]\n"
"v_cvt_pk_bf16_f32 v32, v40, v41\n"
"v_mfma_f32_16x16x32_bf16 v[216:219], a[64:67], v[36:39], v[216:219]\n"
"v_exp_f32 v44, v44\n"
"v_mfma_f32_16x16x32_bf16 v[220:223], a[68:71], v[36:39], v[220:223]\n"
"v_exp_f32 v45, v45\n"
"v_mfma_f32_16x16x32_bf16 v[224:227], a[72:75], v[36:39], v[224:227]\n"
"s_add_u32 s77, s71, 4096\n"
"ds_read_b128 a[24:27], v2 offset:53248\n"
"v_cvt_pk_bf16_f32 v33, v42, v43\n"
"v_mfma_f32_16x16x32_bf16 v[228:231], a[76:79], v[36:39], v[228:231]\n"
"s_add_u32 m0, s68, 36864\n"
"ds_read_b128 a[28:31], v2 offset:53504\n"
"v_mfma_f32_16x16x32_bf16 v[28:31], v[20:23], v[36:39], v[28:31]\n"
"buffer_load_dwordx4 v9, s[28:31], s77 offen nt lds\n"
"ds_read_b64_tr_b16 a[64:65], v5 offset:49152\n"
"v_mfma_f32_16x16x32_bf16 v[48:51], a[32:35], v[72:75], 0\n"
"v_exp_f32 v46, v46\n"
"v_mfma_f32_16x16x32_bf16 v[48:51], a[36:39], v[76:79], v[48:51]\n"
"v_exp_f32 v47, v47\n"
"v_mfma_f32_16x16x32_bf16 v[48:51], a[40:43], v[80:83], v[48:51]\n"
"ds_read_b64_tr_b16 a[66:67], v5 offset:53248\n"
"v_cvt_pk_bf16_f32 v34, v44, v45\n"
"v_mfma_f32_16x16x32_bf16 v[48:51], a[44:47], v[84:87], v[48:51]\n"
"ds_read_b64_tr_b16 a[68:69], v6 offset:49152\n"
"v_cvt_pk_bf16_f32 v35, v46, v47\n"
"v_mfma_f32_16x16x32_bf16 v[52:55], a[48:51], v[72:75], 0\n"
"ds_read_b64_tr_b16 a[70:71], v6 offset:53248\n"
"ds_read_b64_tr_b16 a[72:73], v5 offset:49408\n"
"v_mfma_f32_16x16x32_bf16 v[52:55], a[52:55], v[76:79], v[52:55]\n"
"ds_read_b64_tr_b16 a[74:75], v5 offset:53504\n"
"ds_read_b64_tr_b16 a[76:77], v6 offset:49408\n"
"v_mfma_f32_16x16x32_bf16 v[52:55], a[56:59], v[80:83], v[52:55]\n"
"ds_read_b64_tr_b16 a[78:79], v6 offset:53504\n"
"v_mfma_f32_16x16x32_bf16 v[52:55], a[60:63], v[84:87], v[52:55]\n"
"s_add_u32 s76, s71, 8192\n"
"v_exp_f32 v48, v48\n"
"v_mfma_f32_16x16x32_bf16 v[120:123], a[96:99], v[32:35], v[120:123]\n"
"s_add_u32 m0, s68, 40960\n"
"v_exp_f32 v49, v49\n"
"v_mfma_f32_16x16x32_bf16 v[124:127], a[100:103], v[32:35], v[124:127]\n"
"buffer_load_dwordx4 v9, s[28:31], s76 offen nt lds\n"
"v_exp_f32 v50, v50\n"
"v_mfma_f32_16x16x32_bf16 v[128:131], a[104:107], v[32:35], v[128:131]\n"
"v_exp_f32 v51, v51\n"
"v_mfma_f32_16x16x32_bf16 v[132:135], a[108:111], v[32:35], v[132:135]\n"
"v_cvt_pk_bf16_f32 v36, v48, v49\n"
"v_add_f32 v16, v16, v48\n"
"v_mfma_f32_16x16x32_bf16 v[136:139], a[112:115], v[32:35], v[136:139]\n"
"v_exp_f32 v52, v52\n"
"v_mfma_f32_16x16x32_bf16 v[140:143], a[116:119], v[32:35], v[140:143]\n"
"v_exp_f32 v53, v53\n"
"v_mfma_f32_16x16x32_bf16 v[144:147], a[120:123], v[32:35], v[144:147]\n"
"v_cvt_pk_bf16_f32 v37, v50, v51\n"
"v_add_f32 v16, v16, v49\n"
"v_mfma_f32_16x16x32_bf16 v[148:151], a[124:127], v[32:35], v[148:151]\n"
"v_add_f32 v16, v16, v50\n"
"v_add_f32 v16, v16, v51\n"
"v_mfma_f32_16x16x32_bf16 v[24:27], v[20:23], v[32:35], v[24:27]\n"
"ds_read_b64_tr_b16 a[80:81], v248 offset:49152\n"
"ds_read_b64_tr_b16 a[82:83], v248 offset:53248\n"
"v_mfma_f32_16x16x32_bf16 v[40:43], a[32:35], v[88:91], 0\n"
"v_exp_f32 v54, v54\n"
"v_mfma_f32_16x16x32_bf16 v[40:43], a[36:39], v[92:95], v[40:43]\n"
"v_exp_f32 v55, v55\n"
"v_mfma_f32_16x16x32_bf16 v[40:43], a[40:43], v[96:99], v[40:43]\n"
"s_add_u32 s77, s71, 12288\n"
"v_cvt_pk_bf16_f32 v38, v52, v53\n"
"v_add_f32 v16, v16, v52\n"
"v_mfma_f32_16x16x32_bf16 v[40:43], a[44:47], v[100:103], v[40:43]\n"
"s_add_u32 m0, s68, 45056\n"
"v_cvt_pk_bf16_f32 v39, v54, v55\n"
"v_add_f32 v16, v16, v53\n"
"v_mfma_f32_16x16x32_bf16 v[44:47], a[48:51], v[88:91], 0\n"
"buffer_load_dwordx4 v9, s[28:31], s77 offen nt lds\n"
"v_add_f32 v16, v16, v54\n"
"v_mfma_f32_16x16x32_bf16 v[44:47], a[52:55], v[92:95], v[44:47]\n"
"ds_read_b64_tr_b16 a[84:85], v249 offset:49152\n"
"v_add_f32 v16, v16, v55\n"
"v_mfma_f32_16x16x32_bf16 v[44:47], a[56:59], v[96:99], v[44:47]\n"
"ds_read_b64_tr_b16 a[86:87], v249 offset:53248\n"
"ds_read_b64_tr_b16 a[88:89], v248 offset:49408\n"
"v_mfma_f32_16x16x32_bf16 v[44:47], a[60:63], v[100:103], v[44:47]\n"
"v_exp_f32 v40, v40\n"
"v_mfma_f32_16x16x32_bf16 v[152:155], a[96:99], v[36:39], v[152:155]\n"
"v_exp_f32 v41, v41\n"
"v_mfma_f32_16x16x32_bf16 v[156:159], a[100:103], v[36:39], v[156:159]\n"
"v_exp_f32 v42, v42\n"
"v_mfma_f32_16x16x32_bf16 v[160:163], a[104:107], v[36:39], v[160:163]\n"
"s_add_u32 s76, s71, 0\n"
"v_exp_f32 v43, v43\n"
"v_mfma_f32_16x16x32_bf16 v[164:167], a[108:111], v[36:39], v[164:167]\n"
"s_add_u32 m0, s68, 98304\n"
"v_cvt_pk_bf16_f32 v32, v40, v41\n"
"v_add_f32 v17, v17, v40\n"
"v_mfma_f32_16x16x32_bf16 v[168:171], a[112:115], v[36:39], v[168:171]\n"
"buffer_load_dwordx4 v10, s[88:91], s76 offen nt lds\n"
"v_exp_f32 v44, v44\n"
"v_mfma_f32_16x16x32_bf16 v[172:175], a[116:119], v[36:39], v[172:175]\n"
"v_exp_f32 v45, v45\n"
"v_mfma_f32_16x16x32_bf16 v[176:179], a[120:123], v[36:39], v[176:179]\n"
"v_cvt_pk_bf16_f32 v33, v42, v43\n"
"v_add_f32 v17, v17, v41\n"
"v_mfma_f32_16x16x32_bf16 v[180:183], a[124:127], v[36:39], v[180:183]\n"
"s_waitcnt lgkmcnt(13)\n"
"v_add_f32 v17, v17, v42\n"
"v_add_f32 v17, v17, v43\n"
"v_mfma_f32_16x16x32_bf16 v[48:51], a[0:3], v[56:59], 0\n"
"v_exp_f32 v46, v46\n"
"v_mfma_f32_16x16x32_bf16 v[48:51], a[4:7], v[60:63], v[48:51]\n"
"v_exp_f32 v47, v47\n"
"v_mfma_f32_16x16x32_bf16 v[48:51], a[8:11], v[64:67], v[48:51]\n"
"v_cvt_pk_bf16_f32 v34, v44, v45\n"
"v_add_f32 v17, v17, v44\n"
"v_mfma_f32_16x16x32_bf16 v[48:51], a[12:15], v[68:71], v[48:51]\n"
"v_cvt_pk_bf16_f32 v35, v46, v47\n"
"v_add_f32 v17, v17, v45\n"
"v_mfma_f32_16x16x32_bf16 v[52:55], a[16:19], v[56:59], 0\n"
"s_add_u32 s77, s71, 4096\n"
"ds_read_b64_tr_b16 a[90:91], v248 offset:53504\n"
"v_add_f32 v17, v17, v46\n"
"v_mfma_f32_16x16x32_bf16 v[52:55], a[20:23], v[60:63], v[52:55]\n"
"s_add_u32 m0, s68, 102400\n"
"ds_read_b64_tr_b16 a[92:93], v249 offset:49408\n"
"v_add_f32 v17, v17, v47\n"
"v_mfma_f32_16x16x32_bf16 v[52:55], a[24:27], v[64:67], v[52:55]\n"
"buffer_load_dwordx4 v10, s[88:91], s77 offen nt lds\n"
"ds_read_b64_tr_b16 a[94:95], v249 offset:53504\n"
"v_mfma_f32_16x16x32_bf16 v[52:55], a[28:31], v[68:71], v[52:55]\n"
"ds_read_b128 a[32:35], v3 offset:49152\n"
"v_exp_f32 v48, v48\n"
"v_mfma_f32_16x16x32_bf16 v[184:187], a[96:99], v[32:35], v[184:187]\n"
"v_exp_f32 v49, v49\n"
"v_mfma_f32_16x16x32_bf16 v[188:191], a[100:103], v[32:35], v[188:191]\n"
"ds_read_b128 a[36:39], v3 offset:49408\n"
"v_exp_f32 v50, v50\n"
"v_mfma_f32_16x16x32_bf16 v[192:195], a[104:107], v[32:35], v[192:195]\n"
"v_exp_f32 v51, v51\n"
"v_mfma_f32_16x16x32_bf16 v[196:199], a[108:111], v[32:35], v[196:199]\n"
"ds_read_b128 a[40:43], v4 offset:49152\n"
"v_cvt_pk_bf16_f32 v36, v48, v49\n"
"v_mfma_f32_16x16x32_bf16 v[200:203], a[112:115], v[32:35], v[200:203]\n"
"ds_read_b128 a[44:47], v4 offset:49408\n"
"v_exp_f32 v52, v52\n"
"v_mfma_f32_16x16x32_bf16 v[204:207], a[116:119], v[32:35], v[204:207]\n"
"v_exp_f32 v53, v53\n"
"v_mfma_f32_16x16x32_bf16 v[208:211], a[120:123], v[32:35], v[208:211]\n"
"ds_read_b128 a[48:51], v3 offset:53248\n"
"v_cvt_pk_bf16_f32 v37, v50, v51\n"
"v_mfma_f32_16x16x32_bf16 v[212:215], a[124:127], v[32:35], v[212:215]\n"
"ds_read_b128 a[52:55], v3 offset:53504\n"
"ds_read_b128 a[56:59], v4 offset:53248\n"
"v_mfma_f32_16x16x32_bf16 v[40:43], a[0:3], v[72:75], 0\n"
"s_add_u32 s76, s71, 8192\n"
"ds_read_b128 a[60:63], v4 offset:53504\n"
"v_exp_f32 v54, v54\n"
"v_mfma_f32_16x16x32_bf16 v[40:43], a[4:7], v[76:79], v[40:43]\n"
"s_add_u32 m0, s68, 106496\n"
"v_exp_f32 v55, v55\n"
"v_mfma_f32_16x16x32_bf16 v[40:43], a[8:11], v[80:83], v[40:43]\n"
"buffer_load_dwordx4 v10, s[88:91], s76 offen nt lds\n"
"v_cvt_pk_bf16_f32 v38, v52, v53\n"
"v_mfma_f32_16x16x32_bf16 v[40:43], a[12:15], v[84:87], v[40:43]\n"
"ds_read_b64_tr_b16 a[96:97], v7 offset:49152\n"
"v_cvt_pk_bf16_f32 v39, v54, v55\n"
"v_mfma_f32_16x16x32_bf16 v[44:47], a[16:19], v[72:75], 0\n"
"ds_read_b64_tr_b16 a[98:99], v7 offset:53248\n"
"ds_read_b64_tr_b16 a[100:101], v8 offset:49152\n"
"v_mfma_f32_16x16x32_bf16 v[44:47], a[20:23], v[76:79], v[44:47]\n"
"ds_read_b64_tr_b16 a[102:103], v8 offset:53248\n"
"ds_read_b64_tr_b16 a[104:105], v7 offset:49408\n"
"v_mfma_f32_16x16x32_bf16 v[44:47], a[24:27], v[80:83], v[44:47]\n"
"ds_read_b64_tr_b16 a[106:107], v7 offset:53504\n"
"ds_read_b64_tr_b16 a[108:109], v8 offset:49408\n"
"v_mfma_f32_16x16x32_bf16 v[44:47], a[28:31], v[84:87], v[44:47]\n"
"s_waitcnt lgkmcnt(15)\n"
"v_exp_f32 v40, v40\n"
"v_mfma_f32_16x16x32_bf16 v[120:123], a[64:67], v[36:39], v[120:123]\n"
"v_exp_f32 v41, v41\n"
"v_mfma_f32_16x16x32_bf16 v[124:127], a[68:71], v[36:39], v[124:127]\n"
"v_exp_f32 v42, v42\n"
"v_mfma_f32_16x16x32_bf16 v[128:131], a[72:75], v[36:39], v[128:131]\n"
"v_exp_f32 v43, v43\n"
"v_mfma_f32_16x16x32_bf16 v[132:135], a[76:79], v[36:39], v[132:135]\n"
"v_cvt_pk_bf16_f32 v32, v40, v41\n"
"v_add_f32 v16, v16, v40\n"
"v_mfma_f32_16x16x32_bf16 v[136:139], a[80:83], v[36:39], v[136:139]\n"
"v_exp_f32 v44, v44\n"
"v_mfma_f32_16x16x32_bf16 v[140:143], a[84:87], v[36:39], v[140:143]\n"
"v_exp_f32 v45, v45\n"
"v_mfma_f32_16x16x32_bf16 v[144:147], a[88:91], v[36:39], v[144:147]\n"
"s_add_u32 s77, s71, 12288\n"
"v_cvt_pk_bf16_f32 v33, v42, v43\n"
"v_add_f32 v16, v16, v41\n"
"v_mfma_f32_16x16x32_bf16 v[148:151], a[92:95], v[36:39], v[148:151]\n"
"s_add_u32 m0, s68, 110592\n"
"v_add_f32 v16, v16, v42\n"
"v_add_f32 v16, v16, v43\n"
"v_mfma_f32_16x16x32_bf16 v[24:27], v[20:23], v[36:39], v[24:27]\n"
"buffer_load_dwordx4 v10, s[88:91], s77 offen nt lds\n"
"ds_read_b64_tr_b16 a[110:111], v8 offset:53504\n"
"v_mfma_f32_16x16x32_bf16 v[48:51], a[0:3], v[88:91], 0\n"
"v_exp_f32 v46, v46\n"
"v_mfma_f32_16x16x32_bf16 v[48:51], a[4:7], v[92:95], v[48:51]\n"
"v_exp_f32 v47, v47\n"
"v_mfma_f32_16x16x32_bf16 v[48:51], a[8:11], v[96:99], v[48:51]\n"
"s_mov_b32 s70, 3670016\n"
"v_cvt_pk_bf16_f32 v34, v44, v45\n"
"v_add_f32 v16, v16, v44\n"
"v_mfma_f32_16x16x32_bf16 v[48:51], a[12:15], v[100:103], v[48:51]\n"
"v_cvt_pk_bf16_f32 v35, v46, v47\n"
"v_add_f32 v16, v16, v45\n"
"v_mfma_f32_16x16x32_bf16 v[52:55], a[16:19], v[88:91], 0\n"
"buffer_load_dword v0, v254, s[40:43], s70 offen\n"
"v_add_f32 v16, v16, v46\n"
"v_mfma_f32_16x16x32_bf16 v[52:55], a[20:23], v[92:95], v[52:55]\n"
"ds_read_b64_tr_b16 a[112:113], v250 offset:49152\n"
"v_add_f32 v16, v16, v47\n"
"v_mfma_f32_16x16x32_bf16 v[52:55], a[24:27], v[96:99], v[52:55]\n"
"ds_read_b64_tr_b16 a[114:115], v250 offset:53248\n"
"ds_read_b64_tr_b16 a[116:117], v251 offset:49152\n"
"v_mfma_f32_16x16x32_bf16 v[52:55], a[28:31], v[100:103], v[52:55]\n"
"v_exp_f32 v48, v48\n"
"v_mfma_f32_16x16x32_bf16 v[152:155], a[64:67], v[32:35], v[152:155]\n"
"v_exp_f32 v49, v49\n"
"v_mfma_f32_16x16x32_bf16 v[156:159], a[68:71], v[32:35], v[156:159]\n"
"v_exp_f32 v50, v50\n"
"v_mfma_f32_16x16x32_bf16 v[160:163], a[72:75], v[32:35], v[160:163]\n"
"v_exp_f32 v51, v51\n"
"v_mfma_f32_16x16x32_bf16 v[164:167], a[76:79], v[32:35], v[164:167]\n"
"v_cvt_pk_bf16_f32 v36, v48, v49\n"
"v_add_f32 v17, v17, v48\n"
"v_mfma_f32_16x16x32_bf16 v[168:171], a[80:83], v[32:35], v[168:171]\n"
"v_exp_f32 v52, v52\n"
"v_mfma_f32_16x16x32_bf16 v[172:175], a[84:87], v[32:35], v[172:175]\n"
"v_exp_f32 v53, v53\n"
"v_mfma_f32_16x16x32_bf16 v[176:179], a[88:91], v[32:35], v[176:179]\n"
"v_cvt_pk_bf16_f32 v37, v50, v51\n"
"v_add_f32 v17, v17, v49\n"
"v_mfma_f32_16x16x32_bf16 v[180:183], a[92:95], v[32:35], v[180:183]\n"
"v_add_f32 v17, v17, v50\n"
"v_add_f32 v17, v17, v51\n"
"v_mfma_f32_16x16x32_bf16 v[40:43], a[0:3], v[104:107], 0\n"
"v_exp_f32 v54, v54\n"
"v_mfma_f32_16x16x32_bf16 v[40:43], a[4:7], v[108:111], v[40:43]\n"
"v_exp_f32 v55, v55\n"
"v_mfma_f32_16x16x32_bf16 v[40:43], a[8:11], v[112:115], v[40:43]\n"
"v_cvt_pk_bf16_f32 v38, v52, v53\n"
"v_add_f32 v17, v17, v52\n"
"v_mfma_f32_16x16x32_bf16 v[40:43], a[12:15], v[116:119], v[40:43]\n"
"v_cvt_pk_bf16_f32 v39, v54, v55\n"
"v_add_f32 v17, v17, v53\n"
"v_mfma_f32_16x16x32_bf16 v[44:47], a[16:19], v[104:107], 0\n"
"ds_read_b64_tr_b16 a[118:119], v251 offset:53248\n"
"v_add_f32 v17, v17, v54\n"
"v_mfma_f32_16x16x32_bf16 v[44:47], a[20:23], v[108:111], v[44:47]\n"
"ds_read_b64_tr_b16 a[120:121], v250 offset:49408\n"
"v_add_f32 v17, v17, v55\n"
"v_mfma_f32_16x16x32_bf16 v[44:47], a[24:27], v[112:115], v[44:47]\n"
"ds_read_b64_tr_b16 a[122:123], v250 offset:53504\n"
"ds_read_b64_tr_b16 a[124:125], v251 offset:49408\n"
"v_mfma_f32_16x16x32_bf16 v[44:47], a[28:31], v[116:119], v[44:47]\n"
"v_exp_f32 v40, v40\n"
"v_mfma_f32_16x16x32_bf16 v[184:187], a[64:67], v[36:39], v[184:187]\n"
"v_exp_f32 v41, v41\n"
"v_mfma_f32_16x16x32_bf16 v[188:191], a[68:71], v[36:39], v[188:191]\n"
"v_exp_f32 v42, v42\n"
"v_mfma_f32_16x16x32_bf16 v[192:195], a[72:75], v[36:39], v[192:195]\n"
"v_exp_f32 v43, v43\n"
"v_mfma_f32_16x16x32_bf16 v[196:199], a[76:79], v[36:39], v[196:199]\n"
"ds_read_b64_tr_b16 a[126:127], v251 offset:53504\n"
"v_cvt_pk_bf16_f32 v32, v40, v41\n"
"v_mfma_f32_16x16x32_bf16 v[200:203], a[80:83], v[36:39], v[200:203]\n"
"v_exp_f32 v44, v44\n"
"v_mfma_f32_16x16x32_bf16 v[204:207], a[84:87], v[36:39], v[204:207]\n"
"v_exp_f32 v45, v45\n"
"v_mfma_f32_16x16x32_bf16 v[208:211], a[88:91], v[36:39], v[208:211]\n"
"v_cvt_pk_bf16_f32 v33, v42, v43\n"
"v_mfma_f32_16x16x32_bf16 v[212:215], a[92:95], v[36:39], v[212:215]\n"
"s_waitcnt vmcnt(19) lgkmcnt(0)\n"
"s_barrier\n"
"v_mfma_f32_16x16x32_bf16 v[48:51], a[32:35], v[56:59], 0\n"
"s_add_u32 s61, s75, 15\n"
"v_exp_f32 v46, v46\n"
"v_mfma_f32_16x16x32_bf16 v[48:51], a[36:39], v[60:63], v[48:51]\n"
"s_and_b32 s61, s61, 15\n"
"v_exp_f32 v47, v47\n"
"v_mfma_f32_16x16x32_bf16 v[48:51], a[40:43], v[64:67], v[48:51]\n"
"s_lshl_b32 s61, s61, 14\n"
"ds_read_b128 a[0:3], v1 offset:0\n"
"v_cvt_pk_bf16_f32 v34, v44, v45\n"
"v_mfma_f32_16x16x32_bf16 v[48:51], a[44:47], v[68:71], v[48:51]\n"
"s_add_u32 s71, s68, s61\n"
"ds_read_b128 a[4:7], v1 offset:256\n"
"v_cvt_pk_bf16_f32 v35, v46, v47\n"
"v_mfma_f32_16x16x32_bf16 v[52:55], a[48:51], v[56:59], 0\n"
"s_add_u32 s76, s71, 0\n"
"ds_read_b128 a[8:11], v2 offset:0\n"
"v_mfma_f32_16x16x32_bf16 v[52:55], a[52:55], v[60:63], v[52:55]\n"
"s_add_u32 m0, s68, 49152\n"
"ds_read_b128 a[12:15], v2 offset:256\n"
"ds_read_b128 a[16:19], v1 offset:4096\n"
"v_mfma_f32_16x16x32_bf16 v[52:55], a[56:59], v[64:67], v[52:55]\n"
"buffer_load_dwordx4 v9, s[28:31], s76 offen nt lds\n"
"ds_read_b128 a[20:23], v1 offset:4352\n"
"v_mfma_f32_16x16x32_bf16 v[52:55], a[60:63], v[68:71], v[52:55]\n"
"v_exp_f32 v48, v48\n"
"v_mfma_f32_16x16x32_bf16 v[232:235], a[80:83], v[32:35], v[232:235]\n"
"v_exp_f32 v49, v49\n"
"v_mfma_f32_16x16x32_bf16 v[236:239], a[84:87], v[32:35], v[236:239]\n"
"v_exp_f32 v50, v50\n"
"v_mfma_f32_16x16x32_bf16 v[240:243], a[88:91], v[32:35], v[240:243]\n"
"v_exp_f32 v51, v51\n"
"v_mfma_f32_16x16x32_bf16 v[244:247], a[92:95], v[32:35], v[244:247]\n"
"v_cvt_pk_bf16_f32 v36, v48, v49\n"
"v_mfma_f32_16x16x32_bf16 v[216:219], a[64:67], v[32:35], v[216:219]\n"
"v_exp_f32 v52, v52\n"
"v_mfma_f32_16x16x32_bf16 v[220:223], a[68:71], v[32:35], v[220:223]\n"
"v_exp_f32 v53, v53\n"
"v_mfma_f32_16x16x32_bf16 v[224:227], a[72:75], v[32:35], v[224:227]\n"
"s_add_u32 s77, s71, 4096\n"
"ds_read_b128 a[24:27], v2 offset:4096\n"
"v_cvt_pk_bf16_f32 v37, v50, v51\n"
"v_mfma_f32_16x16x32_bf16 v[228:231], a[76:79], v[32:35], v[228:231]\n"
"s_add_u32 m0, s68, 53248\n"
"ds_read_b128 a[28:31], v2 offset:4352\n"
"v_mfma_f32_16x16x32_bf16 v[28:31], v[20:23], v[32:35], v[28:31]\n"
"buffer_load_dwordx4 v9, s[28:31], s77 offen nt lds\n"
"ds_read_b64_tr_b16 a[64:65], v5 offset:0\n"
"v_mfma_f32_16x16x32_bf16 v[40:43], a[32:35], v[72:75], 0\n"
"v_exp_f32 v54, v54\n"
"v_mfma_f32_16x16x32_bf16 v[40:43], a[36:39], v[76:79], v[40:43]\n"
"v_exp_f32 v55, v55\n"
"v_mfma_f32_16x16x32_bf16 v[40:43], a[40:43], v[80:83], v[40:43]\n"
"ds_read_b64_tr_b16 a[66:67], v5 offset:4096\n"
"v_cvt_pk_bf16_f32 v38, v52, v53\n"
"v_mfma_f32_16x16x32_bf16 v[40:43], a[44:47], v[84:87], v[40:43]\n"
"ds_read_b64_tr_b16 a[68:69], v6 offset:0\n"
"v_cvt_pk_bf16_f32 v39, v54, v55\n"
"v_mfma_f32_16x16x32_bf16 v[44:47], a[48:51], v[72:75], 0\n"
"ds_read_b64_tr_b16 a[70:71], v6 offset:4096\n"
"ds_read_b64_tr_b16 a[72:73], v5 offset:256\n"
"v_mfma_f32_16x16x32_bf16 v[44:47], a[52:55], v[76:79], v[44:47]\n"
"ds_read_b64_tr_b16 a[74:75], v5 offset:4352\n"
"ds_read_b64_tr_b16 a[76:77], v6 offset:256\n"
"v_mfma_f32_16x16x32_bf16 v[44:47], a[56:59], v[80:83], v[44:47]\n"
"ds_read_b64_tr_b16 a[78:79], v6 offset:4352\n"
"v_mfma_f32_16x16x32_bf16 v[44:47], a[60:63], v[84:87], v[44:47]\n"
"s_add_u32 s76, s71, 8192\n"
"v_exp_f32 v40, v40\n"
"v_mfma_f32_16x16x32_bf16 v[120:123], a[96:99], v[36:39], v[120:123]\n"
"s_add_u32 m0, s68, 57344\n"
"v_exp_f32 v41, v41\n"
"v_mfma_f32_16x16x32_bf16 v[124:127], a[100:103], v[36:39], v[124:127]\n"
"buffer_load_dwordx4 v9, s[28:31], s76 offen nt lds\n"
"v_exp_f32 v42, v42\n"
"v_mfma_f32_16x16x32_bf16 v[128:131], a[104:107], v[36:39], v[128:131]\n"
"v_exp_f32 v43, v43\n"
"v_mfma_f32_16x16x32_bf16 v[132:135], a[108:111], v[36:39], v[132:135]\n"
"v_cvt_pk_bf16_f32 v32, v40, v41\n"
"v_add_f32 v16, v16, v40\n"
"v_mfma_f32_16x16x32_bf16 v[136:139], a[112:115], v[36:39], v[136:139]\n"
"v_exp_f32 v44, v44\n"
"v_mfma_f32_16x16x32_bf16 v[140:143], a[116:119], v[36:39], v[140:143]\n"
"v_exp_f32 v45, v45\n"
"v_mfma_f32_16x16x32_bf16 v[144:147], a[120:123], v[36:39], v[144:147]\n"
"v_cvt_pk_bf16_f32 v33, v42, v43\n"
"v_add_f32 v16, v16, v41\n"
"v_mfma_f32_16x16x32_bf16 v[148:151], a[124:127], v[36:39], v[148:151]\n"
"v_add_f32 v16, v16, v42\n"
"v_add_f32 v16, v16, v43\n"
"v_mfma_f32_16x16x32_bf16 v[24:27], v[20:23], v[36:39], v[24:27]\n"
"ds_read_b64_tr_b16 a[80:81], v248 offset:0\n"
"ds_read_b64_tr_b16 a[82:83], v248 offset:4096\n"
"v_mfma_f32_16x16x32_bf16 v[48:51], a[32:35], v[88:91], 0\n"
"v_exp_f32 v46, v46\n"
"v_mfma_f32_16x16x32_bf16 v[48:51], a[36:39], v[92:95], v[48:51]\n"
"v_exp_f32 v47, v47\n"
"v_mfma_f32_16x16x32_bf16 v[48:51], a[40:43], v[96:99], v[48:51]\n"
"s_add_u32 s77, s71, 12288\n"
"v_cvt_pk_bf16_f32 v34, v44, v45\n"
"v_add_f32 v16, v16, v44\n"
"v_mfma_f32_16x16x32_bf16 v[48:51], a[44:47], v[100:103], v[48:51]\n"
"s_add_u32 m0, s68, 61440\n"
"v_cvt_pk_bf16_f32 v35, v46, v47\n"
"v_add_f32 v16, v16, v45\n"
"v_mfma_f32_16x16x32_bf16 v[52:55], a[48:51], v[88:91], 0\n"
"buffer_load_dwordx4 v9, s[28:31], s77 offen nt lds\n"
"v_add_f32 v16, v16, v46\n"
"v_mfma_f32_16x16x32_bf16 v[52:55], a[52:55], v[92:95], v[52:55]\n"
"ds_read_b64_tr_b16 a[84:85], v249 offset:0\n"
"v_add_f32 v16, v16, v47\n"
"v_mfma_f32_16x16x32_bf16 v[52:55], a[56:59], v[96:99], v[52:55]\n"
"ds_read_b64_tr_b16 a[86:87], v249 offset:4096\n"
"ds_read_b64_tr_b16 a[88:89], v248 offset:256\n"
"v_mfma_f32_16x16x32_bf16 v[52:55], a[60:63], v[100:103], v[52:55]\n"
"v_exp_f32 v48, v48\n"
"v_mfma_f32_16x16x32_bf16 v[152:155], a[96:99], v[32:35], v[152:155]\n"
"v_exp_f32 v49, v49\n"
"v_mfma_f32_16x16x32_bf16 v[156:159], a[100:103], v[32:35], v[156:159]\n"
"v_exp_f32 v50, v50\n"
"v_mfma_f32_16x16x32_bf16 v[160:163], a[104:107], v[32:35], v[160:163]\n"
"s_add_u32 s76, s71, 0\n"
"v_exp_f32 v51, v51\n"
"v_mfma_f32_16x16x32_bf16 v[164:167], a[108:111], v[32:35], v[164:167]\n"
"s_add_u32 m0, s68, 114688\n"
"v_cvt_pk_bf16_f32 v36, v48, v49\n"
"v_add_f32 v17, v17, v48\n"
"v_mfma_f32_16x16x32_bf16 v[168:171], a[112:115], v[32:35], v[168:171]\n"
"buffer_load_dwordx4 v10, s[88:91], s76 offen nt lds\n"
"v_exp_f32 v52, v52\n"
"v_mfma_f32_16x16x32_bf16 v[172:175], a[116:119], v[32:35], v[172:175]\n"
"v_exp_f32 v53, v53\n"
"v_mfma_f32_16x16x32_bf16 v[176:179], a[120:123], v[32:35], v[176:179]\n"
"v_cvt_pk_bf16_f32 v37, v50, v51\n"
"v_add_f32 v17, v17, v49\n"
"v_mfma_f32_16x16x32_bf16 v[180:183], a[124:127], v[32:35], v[180:183]\n"
"s_waitcnt lgkmcnt(13)\n"
"v_add_f32 v17, v17, v50\n"
"v_add_f32 v17, v17, v51\n"
"v_mfma_f32_16x16x32_bf16 v[40:43], a[0:3], v[56:59], 0\n"
"v_exp_f32 v54, v54\n"
"v_mfma_f32_16x16x32_bf16 v[40:43], a[4:7], v[60:63], v[40:43]\n"
"v_exp_f32 v55, v55\n"
"v_mfma_f32_16x16x32_bf16 v[40:43], a[8:11], v[64:67], v[40:43]\n"
"v_cvt_pk_bf16_f32 v38, v52, v53\n"
"v_add_f32 v17, v17, v52\n"
"v_mfma_f32_16x16x32_bf16 v[40:43], a[12:15], v[68:71], v[40:43]\n"
"v_cvt_pk_bf16_f32 v39, v54, v55\n"
"v_add_f32 v17, v17, v53\n"
"v_mfma_f32_16x16x32_bf16 v[44:47], a[16:19], v[56:59], 0\n"
"s_add_u32 s77, s71, 4096\n"
"ds_read_b64_tr_b16 a[90:91], v248 offset:4352\n"
"v_add_f32 v17, v17, v54\n"
"v_mfma_f32_16x16x32_bf16 v[44:47], a[20:23], v[60:63], v[44:47]\n"
"s_add_u32 m0, s68, 118784\n"
"ds_read_b64_tr_b16 a[92:93], v249 offset:256\n"
"v_add_f32 v17, v17, v55\n"
"v_mfma_f32_16x16x32_bf16 v[44:47], a[24:27], v[64:67], v[44:47]\n"
"buffer_load_dwordx4 v10, s[88:91], s77 offen nt lds\n"
"ds_read_b64_tr_b16 a[94:95], v249 offset:4352\n"
"v_mfma_f32_16x16x32_bf16 v[44:47], a[28:31], v[68:71], v[44:47]\n"
"ds_read_b128 a[32:35], v3 offset:0\n"
"v_exp_f32 v40, v40\n"
"v_mfma_f32_16x16x32_bf16 v[184:187], a[96:99], v[36:39], v[184:187]\n"
"v_exp_f32 v41, v41\n"
"v_mfma_f32_16x16x32_bf16 v[188:191], a[100:103], v[36:39], v[188:191]\n"
"ds_read_b128 a[36:39], v3 offset:256\n"
"v_exp_f32 v42, v42\n"
"v_mfma_f32_16x16x32_bf16 v[192:195], a[104:107], v[36:39], v[192:195]\n"
"v_exp_f32 v43, v43\n"
"v_mfma_f32_16x16x32_bf16 v[196:199], a[108:111], v[36:39], v[196:199]\n"
"ds_read_b128 a[40:43], v4 offset:0\n"
"v_cvt_pk_bf16_f32 v32, v40, v41\n"
"v_mfma_f32_16x16x32_bf16 v[200:203], a[112:115], v[36:39], v[200:203]\n"
"ds_read_b128 a[44:47], v4 offset:256\n"
"v_exp_f32 v44, v44\n"
"v_mfma_f32_16x16x32_bf16 v[204:207], a[116:119], v[36:39], v[204:207]\n"
"v_exp_f32 v45, v45\n"
"v_mfma_f32_16x16x32_bf16 v[208:211], a[120:123], v[36:39], v[208:211]\n"
"ds_read_b128 a[48:51], v3 offset:4096\n"
"v_cvt_pk_bf16_f32 v33, v42, v43\n"
"v_mfma_f32_16x16x32_bf16 v[212:215], a[124:127], v[36:39], v[212:215]\n"
"ds_read_b128 a[52:55], v3 offset:4352\n"
"ds_read_b128 a[56:59], v4 offset:4096\n"
"v_mfma_f32_16x16x32_bf16 v[48:51], a[0:3], v[72:75], 0\n"
"s_add_u32 s76, s71, 8192\n"
"ds_read_b128 a[60:63], v4 offset:4352\n"
"v_exp_f32 v46, v46\n"
"v_mfma_f32_16x16x32_bf16 v[48:51], a[4:7], v[76:79], v[48:51]\n"
"s_add_u32 m0, s68, 122880\n"
"ds_read_b64_tr_b16 a[96:97], v7 offset:0\n"
"v_exp_f32 v47, v47\n"
"v_mfma_f32_16x16x32_bf16 v[48:51], a[8:11], v[80:83], v[48:51]\n"
"buffer_load_dwordx4 v10, s[88:91], s76 offen nt lds\n"
"v_cvt_pk_bf16_f32 v34, v44, v45\n"
"v_mfma_f32_16x16x32_bf16 v[48:51], a[12:15], v[84:87], v[48:51]\n"
"ds_read_b64_tr_b16 a[98:99], v7 offset:4096\n"
"v_cvt_pk_bf16_f32 v35, v46, v47\n"
"v_mfma_f32_16x16x32_bf16 v[52:55], a[16:19], v[72:75], 0\n"
"ds_read_b64_tr_b16 a[100:101], v8 offset:0\n"
"ds_read_b64_tr_b16 a[102:103], v8 offset:4096\n"
"v_mfma_f32_16x16x32_bf16 v[52:55], a[20:23], v[76:79], v[52:55]\n"
"ds_read_b64_tr_b16 a[104:105], v7 offset:256\n"
"ds_read_b64_tr_b16 a[106:107], v7 offset:4352\n"
"v_mfma_f32_16x16x32_bf16 v[52:55], a[24:27], v[80:83], v[52:55]\n"
"ds_read_b64_tr_b16 a[108:109], v8 offset:256\n"
"ds_read_b64_tr_b16 a[110:111], v8 offset:4352\n"
"v_mfma_f32_16x16x32_bf16 v[52:55], a[28:31], v[84:87], v[52:55]\n"
"s_waitcnt lgkmcnt(15)\n"
"v_exp_f32 v48, v48\n"
"v_mfma_f32_16x16x32_bf16 v[120:123], a[64:67], v[32:35], v[120:123]\n"
"v_exp_f32 v49, v49\n"
"v_mfma_f32_16x16x32_bf16 v[124:127], a[68:71], v[32:35], v[124:127]\n"
"v_exp_f32 v50, v50\n"
"v_mfma_f32_16x16x32_bf16 v[128:131], a[72:75], v[32:35], v[128:131]\n"
"v_exp_f32 v51, v51\n"
"v_mfma_f32_16x16x32_bf16 v[132:135], a[76:79], v[32:35], v[132:135]\n"
"v_cvt_pk_bf16_f32 v36, v48, v49\n"
"v_add_f32 v16, v16, v48\n"
"v_mfma_f32_16x16x32_bf16 v[136:139], a[80:83], v[32:35], v[136:139]\n"
"v_exp_f32 v52, v52\n"
"v_mfma_f32_16x16x32_bf16 v[140:143], a[84:87], v[32:35], v[140:143]\n"
"v_exp_f32 v53, v53\n"
"v_mfma_f32_16x16x32_bf16 v[144:147], a[88:91], v[32:35], v[144:147]\n"
"s_add_u32 s77, s71, 12288\n"
"v_cvt_pk_bf16_f32 v37, v50, v51\n"
"v_add_f32 v16, v16, v49\n"
"v_mfma_f32_16x16x32_bf16 v[148:151], a[92:95], v[32:35], v[148:151]\n"
"s_add_u32 m0, s68, 126976\n"
"v_add_f32 v16, v16, v50\n"
"v_add_f32 v16, v16, v51\n"
"v_mfma_f32_16x16x32_bf16 v[24:27], v[20:23], v[32:35], v[24:27]\n"
"s_add_u32 s62, s35, 0\n"
"buffer_load_dwordx4 v10, s[88:91], s77 offen nt lds\n"
"ds_read_b64_tr_b16 a[112:113], v250 offset:0\n"
"v_mfma_f32_16x16x32_bf16 v[40:43], a[0:3], v[88:91], 0\n"
"s_and_b32 s62, s62, 7\n"
"s_sub_u32 s60, s83, s82\n"
"ds_read_b64_tr_b16 a[114:115], v250 offset:4096\n"
"v_exp_f32 v54, v54\n"
"v_add_u32 v9, s60, v13\n"
"v_mfma_f32_16x16x32_bf16 v[40:43], a[4:7], v[92:95], v[40:43]\n"
"s_lshl_b32 s62, s62, 17\n"
"v_exp_f32 v55, v55\n"
"v_xor_b32 v10, 32, v9\n"
"v_mfma_f32_16x16x32_bf16 v[40:43], a[8:11], v[96:99], v[40:43]\n"
"s_add_u32 s72, s81, s62\n"
"s_add_u32 s76, s72, 0\n"
"v_cvt_pk_bf16_f32 v38, v52, v53\n"
"v_add_f32 v16, v16, v52\n"
"v_add_u32 v253, 0x10000, v9\n"
"v_mfma_f32_16x16x32_bf16 v[40:43], a[12:15], v[100:103], v[40:43]\n"
"s_add_u32 m0, s82, 0\n"
"v_cvt_pk_bf16_f32 v39, v54, v55\n"
"v_add_f32 v16, v16, v53\n"
"v_xor_b32 v254, 32, v253\n"
"v_mfma_f32_16x16x32_bf16 v[44:47], a[16:19], v[88:91], 0\n"
"buffer_load_dwordx4 v12, s[40:43], s76 offen lds\n"
"v_add_f32 v16, v16, v54\n"
"v_mfma_f32_16x16x32_bf16 v[44:47], a[20:23], v[92:95], v[44:47]\n"
"ds_read_b64_tr_b16 a[116:117], v251 offset:0\n"
"v_add_f32 v16, v16, v55\n"
"v_mfma_f32_16x16x32_bf16 v[44:47], a[24:27], v[96:99], v[44:47]\n"
"ds_read_b64_tr_b16 a[118:119], v251 offset:4096\n"
"ds_read_b64_tr_b16 a[120:121], v250 offset:256\n"
"v_mfma_f32_16x16x32_bf16 v[44:47], a[28:31], v[100:103], v[44:47]\n"
"s_add_u32 s77, s72, 8192\n"
"v_exp_f32 v40, v40\n"
"v_mfma_f32_16x16x32_bf16 v[152:155], a[64:67], v[36:39], v[152:155]\n"
"s_add_u32 m0, s82, 1024\n"
"v_exp_f32 v41, v41\n"
"v_mfma_f32_16x16x32_bf16 v[156:159], a[68:71], v[36:39], v[156:159]\n"
"buffer_load_dwordx4 v12, s[40:43], s77 offen lds\n"
"v_exp_f32 v42, v42\n"
"v_mfma_f32_16x16x32_bf16 v[160:163], a[72:75], v[36:39], v[160:163]\n"
"v_exp_f32 v43, v43\n"
"v_mfma_f32_16x16x32_bf16 v[164:167], a[76:79], v[36:39], v[164:167]\n"
"v_cvt_pk_bf16_f32 v32, v40, v41\n"
"v_add_f32 v17, v17, v40\n"
"v_mfma_f32_16x16x32_bf16 v[168:171], a[80:83], v[36:39], v[168:171]\n"
"v_exp_f32 v44, v44\n"
"v_mfma_f32_16x16x32_bf16 v[172:175], a[84:87], v[36:39], v[172:175]\n"
"v_exp_f32 v45, v45\n"
"v_mfma_f32_16x16x32_bf16 v[176:179], a[88:91], v[36:39], v[176:179]\n"
"v_cvt_pk_bf16_f32 v33, v42, v43\n"
"v_add_f32 v17, v17, v41\n"
"v_mfma_f32_16x16x32_bf16 v[180:183], a[92:95], v[36:39], v[180:183]\n"
"v_add_f32 v17, v17, v42\n"
"v_add_f32 v17, v17, v43\n"
"v_mfma_f32_16x16x32_bf16 v[48:51], a[0:3], v[104:107], 0\n"
"ds_read_b64_tr_b16 a[122:123], v250 offset:4352\n"
"v_exp_f32 v46, v46\n"
"v_mfma_f32_16x16x32_bf16 v[48:51], a[4:7], v[108:111], v[48:51]\n"
"v_exp_f32 v47, v47\n"
"v_mfma_f32_16x16x32_bf16 v[48:51], a[8:11], v[112:115], v[48:51]\n"
"s_add_u32 s76, s72, 16384\n"
"v_cvt_pk_bf16_f32 v34, v44, v45\n"
"v_add_f32 v17, v17, v44\n"
"v_mfma_f32_16x16x32_bf16 v[48:51], a[12:15], v[116:119], v[48:51]\n"
"s_add_u32 m0, s82, 2048\n"
"v_cvt_pk_bf16_f32 v35, v46, v47\n"
"v_add_f32 v17, v17, v45\n"
"v_mfma_f32_16x16x32_bf16 v[52:55], a[16:19], v[104:107], 0\n"
"s_add_u32 s77, s72, 24576\n"
"buffer_load_dwordx4 v12, s[40:43], s76 offen lds\n"
"v_add_f32 v17, v17, v46\n"
"v_mfma_f32_16x16x32_bf16 v[52:55], a[20:23], v[108:111], v[52:55]\n"
"s_add_u32 m0, s82, 3072\n"
"ds_read_b64_tr_b16 a[124:125], v251 offset:256\n"
"v_add_f32 v17, v17, v47\n"
"v_mfma_f32_16x16x32_bf16 v[52:55], a[24:27], v[112:115], v[52:55]\n"
"buffer_load_dwordx4 v12, s[40:43], s77 offen lds\n"
"ds_read_b64_tr_b16 a[126:127], v251 offset:4352\n"
"v_mfma_f32_16x16x32_bf16 v[52:55], a[28:31], v[116:119], v[52:55]\n"
"v_exp_f32 v48, v48\n"
"v_mfma_f32_16x16x32_bf16 v[184:187], a[64:67], v[32:35], v[184:187]\n"
"v_exp_f32 v49, v49\n"
"v_mfma_f32_16x16x32_bf16 v[188:191], a[68:71], v[32:35], v[188:191]\n"
"s_add_u32 s76, s72, 32768\n"
"v_exp_f32 v50, v50\n"
"v_mfma_f32_16x16x32_bf16 v[192:195], a[72:75], v[32:35], v[192:195]\n"
"s_add_u32 m0, s82, 4096\n"
"v_exp_f32 v51, v51\n"
"v_mfma_f32_16x16x32_bf16 v[196:199], a[76:79], v[32:35], v[196:199]\n"
"buffer_load_dwordx4 v12, s[40:43], s76 offen lds\n"
"v_cvt_pk_bf16_f32 v36, v48, v49\n"
"v_mfma_f32_16x16x32_bf16 v[200:203], a[80:83], v[32:35], v[200:203]\n"
"v_exp_f32 v52, v52\n"
"v_mfma_f32_16x16x32_bf16 v[204:207], a[84:87], v[32:35], v[204:207]\n"
"v_exp_f32 v53, v53\n"
"v_mfma_f32_16x16x32_bf16 v[208:211], a[88:91], v[32:35], v[208:211]\n"
"v_cvt_pk_bf16_f32 v37, v50, v51\n"
"v_mfma_f32_16x16x32_bf16 v[212:215], a[92:95], v[32:35], v[212:215]\n"
"s_waitcnt vmcnt(23) lgkmcnt(0)\n"
"s_barrier\n"
"v_mfma_f32_16x16x32_bf16 v[40:43], a[32:35], v[56:59], 0\n"
"s_add_u32 s77, s72, 40960\n"
"v_exp_f32 v54, v54\n"
"v_mfma_f32_16x16x32_bf16 v[40:43], a[36:39], v[60:63], v[40:43]\n"
"s_add_u32 m0, s82, 5120\n"
"v_exp_f32 v55, v55\n"
"v_mfma_f32_16x16x32_bf16 v[40:43], a[40:43], v[64:67], v[40:43]\n"
"buffer_load_dwordx4 v12, s[40:43], s77 offen lds\n"
"v_cvt_pk_bf16_f32 v38, v52, v53\n"
"v_mfma_f32_16x16x32_bf16 v[40:43], a[44:47], v[68:71], v[40:43]\n"
"ds_read_b128 a[0:3], v1 offset:16384\n"
"v_cvt_pk_bf16_f32 v39, v54, v55\n"
"v_mfma_f32_16x16x32_bf16 v[44:47], a[48:51], v[56:59], 0\n"
"s_add_u32 s76, s72, 49152\n"
"ds_read_b128 a[4:7], v1 offset:16640\n"
"ds_read_b128 a[8:11], v2 offset:16384\n"
"v_mfma_f32_16x16x32_bf16 v[44:47], a[52:55], v[60:63], v[44:47]\n"
"s_add_u32 m0, s82, 6144\n"
"ds_read_b128 a[12:15], v2 offset:16640\n"
"ds_read_b128 a[16:19], v1 offset:20480\n"
"v_mfma_f32_16x16x32_bf16 v[44:47], a[56:59], v[64:67], v[44:47]\n"
"buffer_load_dwordx4 v12, s[40:43], s76 offen lds\n"
"ds_read_b128 a[20:23], v1 offset:20736\n"
"v_mfma_f32_16x16x32_bf16 v[44:47], a[60:63], v[68:71], v[44:47]\n"
"v_exp_f32 v40, v40\n"
"v_mfma_f32_16x16x32_bf16 v[232:235], a[80:83], v[36:39], v[232:235]\n"
"v_exp_f32 v41, v41\n"
"v_mfma_f32_16x16x32_bf16 v[236:239], a[84:87], v[36:39], v[236:239]\n"
"v_exp_f32 v42, v42\n"
"v_mfma_f32_16x16x32_bf16 v[240:243], a[88:91], v[36:39], v[240:243]\n"
"v_exp_f32 v43, v43\n"
"v_mfma_f32_16x16x32_bf16 v[244:247], a[92:95], v[36:39], v[244:247]\n"
"ds_read_b128 a[24:27], v2 offset:20480\n"
"v_cvt_pk_bf16_f32 v32, v40, v41\n"
"v_mfma_f32_16x16x32_bf16 v[216:219], a[64:67], v[36:39], v[216:219]\n"
"s_add_u32 s77, s72, 57344\n"
"v_exp_f32 v44, v44\n"
"v_mfma_f32_16x16x32_bf16 v[220:223], a[68:71], v[36:39], v[220:223]\n"
"s_add_u32 m0, s82, 7168\n"
"v_exp_f32 v45, v45\n"
"v_mfma_f32_16x16x32_bf16 v[224:227], a[72:75], v[36:39], v[224:227]\n"
"buffer_load_dwordx4 v12, s[40:43], s77 offen lds\n"
"v_cvt_pk_bf16_f32 v33, v42, v43\n"
"v_mfma_f32_16x16x32_bf16 v[228:231], a[76:79], v[36:39], v[228:231]\n"
"ds_read_b128 a[28:31], v2 offset:20736\n"
"v_mfma_f32_16x16x32_bf16 v[28:31], v[20:23], v[36:39], v[28:31]\n"
"ds_read_b64_tr_b16 a[64:65], v5 offset:16384\n"
"ds_read_b64_tr_b16 a[66:67], v5 offset:20480\n"
"v_mfma_f32_16x16x32_bf16 v[48:51], a[32:35], v[72:75], 0\n"
"s_add_u32 s76, s72, 65536\n"
"ds_read_b64_tr_b16 a[68:69], v6 offset:16384\n"
"v_exp_f32 v46, v46\n"
"v_mfma_f32_16x16x32_bf16 v[48:51], a[36:39], v[76:79], v[48:51]\n"
"s_add_u32 m0, s83, 0\n"
"v_exp_f32 v47, v47\n"
"v_mfma_f32_16x16x32_bf16 v[48:51], a[40:43], v[80:83], v[48:51]\n"
"buffer_load_dwordx4 v12, s[40:43], s76 offen lds\n"
"v_cvt_pk_bf16_f32 v34, v44, v45\n"
"v_mfma_f32_16x16x32_bf16 v[48:51], a[44:47], v[84:87], v[48:51]\n"
"ds_read_b64_tr_b16 a[70:71], v6 offset:20480\n"
"v_cvt_pk_bf16_f32 v35, v46, v47\n"
"v_mfma_f32_16x16x32_bf16 v[52:55], a[48:51], v[72:75], 0\n"
"s_add_u32 s77, s72, 73728\n"
"ds_read_b64_tr_b16 a[72:73], v5 offset:16640\n"
"ds_read_b64_tr_b16 a[74:75], v5 offset:20736\n"
"v_mfma_f32_16x16x32_bf16 v[52:55], a[52:55], v[76:79], v[52:55]\n"
"s_add_u32 m0, s83, 1024\n"
"ds_read_b64_tr_b16 a[76:77], v6 offset:16640\n"
"ds_read_b64_tr_b16 a[78:79], v6 offset:20736\n"
"v_mfma_f32_16x16x32_bf16 v[52:55], a[56:59], v[80:83], v[52:55]\n"
"buffer_load_dwordx4 v12, s[40:43], s77 offen lds\n"
"ds_read_b64_tr_b16 a[80:81], v248 offset:16384\n"
"v_mfma_f32_16x16x32_bf16 v[52:55], a[60:63], v[84:87], v[52:55]\n"
"v_exp_f32 v48, v48\n"
"v_mfma_f32_16x16x32_bf16 v[120:123], a[96:99], v[32:35], v[120:123]\n"
"v_exp_f32 v49, v49\n"
"v_mfma_f32_16x16x32_bf16 v[124:127], a[100:103], v[32:35], v[124:127]\n"
"v_exp_f32 v50, v50\n"
"v_mfma_f32_16x16x32_bf16 v[128:131], a[104:107], v[32:35], v[128:131]\n"
"v_exp_f32 v51, v51\n"
"v_mfma_f32_16x16x32_bf16 v[132:135], a[108:111], v[32:35], v[132:135]\n"
"v_cvt_pk_bf16_f32 v36, v48, v49\n"
"v_add_f32 v16, v16, v48\n"
"v_mfma_f32_16x16x32_bf16 v[136:139], a[112:115], v[32:35], v[136:139]\n"
"v_exp_f32 v52, v52\n"
"v_mfma_f32_16x16x32_bf16 v[140:143], a[116:119], v[32:35], v[140:143]\n"
"s_waitcnt vmcnt(6)\n"
"ds_read_b64_tr_b16 a[128:129], v13 offset:0\n"
"ds_read_b64_tr_b16 a[130:131], v13 offset:2048\n"
"v_exp_f32 v53, v53\n"
"v_mfma_f32_16x16x32_bf16 v[144:147], a[120:123], v[32:35], v[144:147]\n"
"s_add_u32 s76, s72, 81920\n"
"v_cvt_pk_bf16_f32 v37, v50, v51\n"
"v_add_f32 v16, v16, v49\n"
"v_mfma_f32_16x16x32_bf16 v[148:151], a[124:127], v[32:35], v[148:151]\n"
"s_add_u32 m0, s83, 2048\n"
"ds_read_b64_tr_b16 a[132:133], v14 offset:0\n"
"v_add_f32 v16, v16, v50\n"
"v_add_f32 v16, v16, v51\n"
"v_mfma_f32_16x16x32_bf16 v[24:27], v[20:23], v[32:35], v[24:27]\n"
"buffer_load_dwordx4 v12, s[40:43], s76 offen lds\n"
"ds_read_b64_tr_b16 a[82:83], v248 offset:20480\n"
"v_mfma_f32_16x16x32_bf16 v[40:43], a[32:35], v[88:91], 0\n"
"ds_read_b64_tr_b16 a[134:135], v14 offset:2048\n"
"ds_read_b64_tr_b16 a[136:137], v13 offset:512\n"
"v_exp_f32 v54, v54\n"
"v_mfma_f32_16x16x32_bf16 v[40:43], a[36:39], v[92:95], v[40:43]\n"
"ds_read_b64_tr_b16 a[138:139], v13 offset:2560\n"
"v_exp_f32 v55, v55\n"
"v_mfma_f32_16x16x32_bf16 v[40:43], a[40:43], v[96:99], v[40:43]\n"
"s_add_u32 s77, s72, 90112\n"
"ds_read_b64_tr_b16 a[140:141], v14 offset:512\n"
"v_cvt_pk_bf16_f32 v38, v52, v53\n"
"v_add_f32 v16, v16, v52\n"
"v_mfma_f32_16x16x32_bf16 v[40:43], a[44:47], v[100:103], v[40:43]\n"
"s_add_u32 m0, s83, 3072\n"
"v_cvt_pk_bf16_f32 v39, v54, v55\n"
"v_add_f32 v16, v16, v53\n"
"v_mfma_f32_16x16x32_bf16 v[44:47], a[48:51], v[88:91], 0\n"
"s_add_u32 s76, s72, 98304\n"
"buffer_load_dwordx4 v12, s[40:43], s77 offen lds\n"
"ds_read_b64_tr_b16 a[142:143], v14 offset:2560\n"
"v_add_f32 v16, v16, v54\n"
"v_mfma_f32_16x16x32_bf16 v[44:47], a[52:55], v[92:95], v[44:47]\n"
"s_add_u32 m0, s83, 65536\n"
"ds_read_b64_tr_b16 a[84:85], v249 offset:16384\n"
"v_add_f32 v16, v16, v55\n"
"v_mfma_f32_16x16x32_bf16 v[44:47], a[56:59], v[96:99], v[44:47]\n"
"buffer_load_dwordx4 v12, s[40:43], s76 offen lds\n"
"ds_read_b64_tr_b16 a[86:87], v249 offset:20480\n"
"v_mfma_f32_16x16x32_bf16 v[44:47], a[60:63], v[100:103], v[44:47]\n"
"ds_read_b64_tr_b16 a[88:89], v248 offset:16640\n"
"v_exp_f32 v40, v40\n"
"v_mfma_f32_16x16x32_bf16 v[152:155], a[96:99], v[36:39], v[152:155]\n"
"v_exp_f32 v41, v41\n"
"v_mfma_f32_16x16x32_bf16 v[156:159], a[100:103], v[36:39], v[156:159]\n"
"v_exp_f32 v42, v42\n"
"v_mfma_f32_16x16x32_bf16 v[160:163], a[104:107], v[36:39], v[160:163]\n"
"s_add_u32 s77, s72, 106496\n"
"v_exp_f32 v43, v43\n"
"v_mfma_f32_16x16x32_bf16 v[164:167], a[108:111], v[36:39], v[164:167]\n"
"s_add_u32 m0, s83, 66560\n"
"v_cvt_pk_bf16_f32 v32, v40, v41\n"
"v_add_f32 v17, v17, v40\n"
"v_mfma_f32_16x16x32_bf16 v[168:171], a[112:115], v[36:39], v[168:171]\n"
"buffer_load_dwordx4 v12, s[40:43], s77 offen lds\n"
"v_exp_f32 v44, v44\n"
"v_mfma_f32_16x16x32_bf16 v[172:175], a[116:119], v[36:39], v[172:175]\n"
"v_exp_f32 v45, v45\n"
"v_mfma_f32_16x16x32_bf16 v[176:179], a[120:123], v[36:39], v[176:179]\n"
"v_cvt_pk_bf16_f32 v33, v42, v43\n"
"v_add_f32 v17, v17, v41\n"
"v_mfma_f32_16x16x32_bf16 v[180:183], a[124:127], v[36:39], v[180:183]\n"
"s_waitcnt lgkmcnt(15)\n"
"v_add_f32 v17, v17, v42\n"
"v_add_f32 v17, v17, v43\n"
"v_mfma_f32_16x16x32_bf16 v[48:51], a[0:3], v[56:59], 0\n"
"v_exp_f32 v46, v46\n"
"v_mfma_f32_16x16x32_bf16 v[48:51], a[4:7], v[60:63], v[48:51]\n"
"v_exp_f32 v47, v47\n"
"v_mfma_f32_16x16x32_bf16 v[48:51], a[8:11], v[64:67], v[48:51]\n"
"s_add_u32 s76, s72, 114688\n"
"v_cvt_pk_bf16_f32 v34, v44, v45\n"
"v_add_f32 v17, v17, v44\n"
"v_mfma_f32_16x16x32_bf16 v[48:51], a[12:15], v[68:71], v[48:51]\n"
"s_add_u32 m0, s83, 67584\n"
"v_cvt_pk_bf16_f32 v35, v46, v47\n"
"v_add_f32 v17, v17, v45\n"
"v_mfma_f32_16x16x32_bf16 v[52:55], a[16:19], v[56:59], 0\n"
"buffer_load_dwordx4 v12, s[40:43], s76 offen lds\n"
"s_waitcnt vmcnt(7)\n"
"ds_read_b64_tr_b16 a[144:145], v13 offset:4096\n"
"v_add_f32 v17, v17, v46\n"
"v_mfma_f32_16x16x32_bf16 v[52:55], a[20:23], v[60:63], v[52:55]\n"
"ds_read_b64_tr_b16 a[90:91], v248 offset:20736\n"
"ds_read_b64_tr_b16 a[146:147], v13 offset:6144\n"
"v_add_f32 v17, v17, v47\n"
"v_mfma_f32_16x16x32_bf16 v[52:55], a[24:27], v[64:67], v[52:55]\n"
"ds_read_b64_tr_b16 a[92:93], v249 offset:16640\n"
"ds_read_b64_tr_b16 a[94:95], v249 offset:20736\n"
"v_mfma_f32_16x16x32_bf16 v[52:55], a[28:31], v[68:71], v[52:55]\n"
"ds_read_b128 a[32:35], v3 offset:16384\n"
"ds_read_b64_tr_b16 a[148:149], v14 offset:4096\n"
"v_exp_f32 v48, v48\n"
"v_mfma_f32_16x16x32_bf16 v[184:187], a[96:99], v[32:35], v[184:187]\n"
"ds_read_b128 a[36:39], v3 offset:16640\n"
"v_exp_f32 v49, v49\n"
"v_mfma_f32_16x16x32_bf16 v[188:191], a[100:103], v[32:35], v[188:191]\n"
"s_add_u32 s77, s72, 122880\n"
"ds_read_b64_tr_b16 a[150:151], v14 offset:6144\n"
"ds_read_b64_tr_b16 a[152:153], v13 offset:4608\n"
"v_exp_f32 v50, v50\n"
"v_mfma_f32_16x16x32_bf16 v[192:195], a[104:107], v[32:35], v[192:195]\n"
"s_add_u32 m0, s83, 68608\n"
"ds_read_b128 a[40:43], v4 offset:16384\n"
"ds_read_b64_tr_b16 a[154:155], v13 offset:6656\n"
"v_exp_f32 v51, v51\n"
"v_mfma_f32_16x16x32_bf16 v[196:199], a[108:111], v[32:35], v[196:199]\n"
"s_add_u32 s62, s35, 1\n"
"buffer_load_dwordx4 v12, s[40:43], s77 offen lds\n"
"v_cvt_pk_bf16_f32 v36, v48, v49\n"
"v_mfma_f32_16x16x32_bf16 v[200:203], a[112:115], v[32:35], v[200:203]\n"
"s_and_b32 s62, s62, 7\n"
"ds_read_b128 a[44:47], v4 offset:16640\n"
"ds_read_b64_tr_b16 a[156:157], v14 offset:4608\n"
"v_exp_f32 v52, v52\n"
"v_mfma_f32_16x16x32_bf16 v[204:207], a[116:119], v[32:35], v[204:207]\n"
"s_lshl_b32 s62, s62, 17\n"
"ds_read_b64_tr_b16 a[158:159], v14 offset:6656\n"
"v_exp_f32 v53, v53\n"
"v_mfma_f32_16x16x32_bf16 v[208:211], a[120:123], v[32:35], v[208:211]\n"
"s_add_u32 s72, s81, s62\n"
"ds_read_b128 a[48:51], v3 offset:20480\n"
"v_cvt_pk_bf16_f32 v37, v50, v51\n"
"v_mfma_f32_16x16x32_bf16 v[212:215], a[124:127], v[32:35], v[212:215]\n"
"s_waitcnt lgkmcnt(15)\n"
"ds_read_b128 a[52:55], v3 offset:20736\n"
"ds_read_b128 a[56:59], v4 offset:20480\n"
"v_mfma_f32_16x16x32_bf16 v[40:43], a[0:3], v[72:75], 0\n"
"s_add_u32 s76, s72, 0\n"
"ds_read_b128 a[60:63], v4 offset:20736\n"
"v_exp_f32 v54, v54\n"
"v_mfma_f32_16x16x32_bf16 v[40:43], a[4:7], v[76:79], v[40:43]\n"
"s_add_u32 m0, s82, 0\n"
"ds_read_b64_tr_b16 a[96:97], v7 offset:16384\n"
"v_exp_f32 v55, v55\n"
"v_mfma_f32_16x16x32_bf16 v[40:43], a[8:11], v[80:83], v[40:43]\n"
"buffer_load_dwordx4 v12, s[40:43], s76 offen lds\n"
"v_cvt_pk_bf16_f32 v38, v52, v53\n"
"v_mfma_f32_16x16x32_bf16 v[40:43], a[12:15], v[84:87], v[40:43]\n"
"ds_read_b64_tr_b16 a[98:99], v7 offset:20480\n"
"v_cvt_pk_bf16_f32 v39, v54, v55\n"
"v_mfma_f32_16x16x32_bf16 v[44:47], a[16:19], v[72:75], 0\n"
"s_add_u32 s77, s72, 8192\n"
"ds_read_b64_tr_b16 a[100:101], v8 offset:16384\n"
"ds_read_b64_tr_b16 a[102:103], v8 offset:20480\n"
"v_mfma_f32_16x16x32_bf16 v[44:47], a[20:23], v[76:79], v[44:47]\n"
"s_add_u32 m0, s82, 1024\n"
"ds_read_b64_tr_b16 a[104:105], v7 offset:16640\n"
"ds_read_b64_tr_b16 a[106:107], v7 offset:20736\n"
"v_mfma_f32_16x16x32_bf16 v[44:47], a[24:27], v[80:83], v[44:47]\n"
"buffer_load_dwordx4 v12, s[40:43], s77 offen lds\n"
"ds_read_b64_tr_b16 a[108:109], v8 offset:16640\n"
"v_mfma_f32_16x16x32_bf16 v[44:47], a[28:31], v[84:87], v[44:47]\n"
"s_waitcnt lgkmcnt(15)\n"
"v_exp_f32 v40, v40\n"
"v_mfma_f32_16x16x32_bf16 v[120:123], a[64:67], v[36:39], v[120:123]\n"
"v_exp_f32 v41, v41\n"
"v_mfma_f32_16x16x32_bf16 v[124:127], a[68:71], v[36:39], v[124:127]\n"
"v_exp_f32 v42, v42\n"
"v_mfma_f32_16x16x32_bf16 v[128:131], a[72:75], v[36:39], v[128:131]\n"
"v_exp_f32 v43, v43\n"
"v_mfma_f32_16x16x32_bf16 v[132:135], a[76:79], v[36:39], v[132:135]\n"
"v_cvt_pk_bf16_f32 v32, v40, v41\n"
"v_add_f32 v16, v16, v40\n"
"v_mfma_f32_16x16x32_bf16 v[136:139], a[80:83], v[36:39], v[136:139]\n"
"ds_read_b64_tr_b16 a[110:111], v8 offset:20736\n"
"s_waitcnt vmcnt(6)\n"
"ds_read_b64_tr_b16 a[160:161], v9 offset:0\n"
"v_exp_f32 v44, v44\n"
"v_mfma_f32_16x16x32_bf16 v[140:143], a[84:87], v[36:39], v[140:143]\n"
"ds_read_b64_tr_b16 a[162:163], v9 offset:2048\n"
"v_exp_f32 v45, v45\n"
"v_mfma_f32_16x16x32_bf16 v[144:147], a[88:91], v[36:39], v[144:147]\n"
"s_add_u32 s76, s72, 16384\n"
"ds_read_b64_tr_b16 a[164:165], v10 offset:0\n"
"v_cvt_pk_bf16_f32 v33, v42, v43\n"
"v_add_f32 v16, v16, v41\n"
"v_mfma_f32_16x16x32_bf16 v[148:151], a[92:95], v[36:39], v[148:151]\n"
"s_add_u32 m0, s82, 2048\n"
"v_add_f32 v16, v16, v42\n"
"v_add_f32 v16, v16, v43\n"
"v_mfma_f32_16x16x32_bf16 v[24:27], v[20:23], v[36:39], v[24:27]\n"
"buffer_load_dwordx4 v12, s[40:43], s76 offen lds\n"
"ds_read_b64_tr_b16 a[112:113], v250 offset:16384\n"
"ds_read_b64_tr_b16 a[166:167], v10 offset:2048\n"
"v_mfma_f32_16x16x32_bf16 v[48:51], a[0:3], v[88:91], 0\n"
"ds_read_b64_tr_b16 a[168:169], v9 offset:512\n"
"v_exp_f32 v46, v46\n"
"v_mfma_f32_16x16x32_bf16 v[48:51], a[4:7], v[92:95], v[48:51]\n"
"ds_read_b64_tr_b16 a[170:171], v9 offset:2560\n"
"ds_read_b64_tr_b16 a[172:173], v10 offset:512\n"
"v_exp_f32 v47, v47\n"
"v_mfma_f32_16x16x32_bf16 v[48:51], a[8:11], v[96:99], v[48:51]\n"
"s_add_u32 s77, s72, 24576\n"
"v_cvt_pk_bf16_f32 v34, v44, v45\n"
"v_add_f32 v16, v16, v44\n"
"v_mfma_f32_16x16x32_bf16 v[48:51], a[12:15], v[100:103], v[48:51]\n"
"s_add_u32 m0, s82, 3072\n"
"ds_read_b64_tr_b16 a[174:175], v10 offset:2560\n"
"v_cvt_pk_bf16_f32 v35, v46, v47\n"
"v_add_f32 v16, v16, v45\n"
"v_mfma_f32_16x16x32_bf16 v[52:55], a[16:19], v[88:91], 0\n"
"s_waitcnt lgkmcnt(15)\n"
"s_add_u32 s76, s72, 32768\n"
"buffer_load_dwordx4 v12, s[40:43], s77 offen lds\n"
"v_add_f32 v16, v16, v46\n"
"v_mfma_f32_16x16x32_bf16 v[52:55], a[20:23], v[92:95], v[52:55]\n"
"s_add_u32 m0, s82, 4096\n"
"ds_read_b64_tr_b16 a[114:115], v250 offset:20480\n"
"v_add_f32 v16, v16, v47\n"
"v_mfma_f32_16x16x32_bf16 v[52:55], a[24:27], v[96:99], v[52:55]\n"
"buffer_load_dwordx4 v12, s[40:43], s76 offen lds\n"
"ds_read_b64_tr_b16 a[116:117], v251 offset:16384\n"
"v_mfma_f32_16x16x32_bf16 v[52:55], a[28:31], v[100:103], v[52:55]\n"
"v_exp_f32 v48, v48\n"
"v_mfma_f32_16x16x32_bf16 v[152:155], a[64:67], v[32:35], v[152:155]\n"
"ds_read_b64_tr_b16 a[118:119], v251 offset:20480\n"
"v_exp_f32 v49, v49\n"
"v_mfma_f32_16x16x32_bf16 v[156:159], a[68:71], v[32:35], v[156:159]\n"
"v_exp_f32 v50, v50\n"
"v_mfma_f32_16x16x32_bf16 v[160:163], a[72:75], v[32:35], v[160:163]\n"
"s_add_u32 s77, s72, 40960\n"
"v_exp_f32 v51, v51\n"
"v_mfma_f32_16x16x32_bf16 v[164:167], a[76:79], v[32:35], v[164:167]\n"
"s_add_u32 m0, s82, 5120\n"
"v_cvt_pk_bf16_f32 v36, v48, v49\n"
"v_add_f32 v17, v17, v48\n"
"v_mfma_f32_16x16x32_bf16 v[168:171], a[80:83], v[32:35], v[168:171]\n"
"buffer_load_dwordx4 v12, s[40:43], s77 offen lds\n"
"v_exp_f32 v52, v52\n"
"v_mfma_f32_16x16x32_bf16 v[172:175], a[84:87], v[32:35], v[172:175]\n"
"v_exp_f32 v53, v53\n"
"v_mfma_f32_16x16x32_bf16 v[176:179], a[88:91], v[32:35], v[176:179]\n"
"v_cvt_pk_bf16_f32 v37, v50, v51\n"
"v_add_f32 v17, v17, v49\n"
"v_mfma_f32_16x16x32_bf16 v[180:183], a[92:95], v[32:35], v[180:183]\n"
"v_add_f32 v17, v17, v50\n"
"v_add_f32 v17, v17, v51\n"
"v_mfma_f32_16x16x32_bf16 v[40:43], a[0:3], v[104:107], 0\n"
"v_exp_f32 v54, v54\n"
"v_mfma_f32_16x16x32_bf16 v[40:43], a[4:7], v[108:111], v[40:43]\n"
"ds_read_b64_tr_b16 a[120:121], v250 offset:16640\n"
"v_exp_f32 v55, v55\n"
"v_mfma_f32_16x16x32_bf16 v[40:43], a[8:11], v[112:115], v[40:43]\n"
"s_add_u32 s76, s72, 49152\n"
"s_waitcnt vmcnt(6)\n"
"ds_read_b64_tr_b16 a[176:177], v253 offset:0\n"
"v_cvt_pk_bf16_f32 v38, v52, v53\n"
"v_add_f32 v17, v17, v52\n"
"v_mfma_f32_16x16x32_bf16 v[40:43], a[12:15], v[116:119], v[40:43]\n"
"s_add_u32 m0, s82, 6144\n"
"v_cvt_pk_bf16_f32 v39, v54, v55\n"
"v_add_f32 v17, v17, v53\n"
"v_mfma_f32_16x16x32_bf16 v[44:47], a[16:19], v[104:107], 0\n"
"buffer_load_dwordx4 v12, s[40:43], s76 offen lds\n"
"ds_read_b64_tr_b16 a[178:179], v253 offset:2048\n"
"v_add_f32 v17, v17, v54\n"
"v_mfma_f32_16x16x32_bf16 v[44:47], a[20:23], v[108:111], v[44:47]\n"
"ds_read_b64_tr_b16 a[122:123], v250 offset:20736\n"
"v_add_f32 v17, v17, v55\n"
"v_mfma_f32_16x16x32_bf16 v[44:47], a[24:27], v[112:115], v[44:47]\n"
"ds_read_b64_tr_b16 a[124:125], v251 offset:16640\n"
"ds_read_b64_tr_b16 a[126:127], v251 offset:20736\n"
"v_mfma_f32_16x16x32_bf16 v[44:47], a[28:31], v[116:119], v[44:47]\n"
"ds_read_b64_tr_b16 a[180:181], v254 offset:0\n"
"ds_read_b64_tr_b16 a[182:183], v254 offset:2048\n"
"v_exp_f32 v40, v40\n"
"v_mfma_f32_16x16x32_bf16 v[184:187], a[64:67], v[36:39], v[184:187]\n"
"ds_read_b64_tr_b16 a[184:185], v253 offset:512\n"
"v_exp_f32 v41, v41\n"
"v_mfma_f32_16x16x32_bf16 v[188:191], a[68:71], v[36:39], v[188:191]\n"
"s_add_u32 s77, s72, 57344\n"
"ds_read_b64_tr_b16 a[186:187], v253 offset:2560\n"
"ds_read_b64_tr_b16 a[188:189], v254 offset:512\n"
"v_exp_f32 v42, v42\n"
"v_mfma_f32_16x16x32_bf16 v[192:195], a[72:75], v[36:39], v[192:195]\n"
"s_add_u32 m0, s82, 7168\n"
"ds_read_b64_tr_b16 a[190:191], v254 offset:2560\n"
"v_exp_f32 v43, v43\n"
"v_mfma_f32_16x16x32_bf16 v[196:199], a[76:79], v[36:39], v[196:199]\n"
"buffer_load_dwordx4 v12, s[40:43], s77 offen lds\n"
"v_cvt_pk_bf16_f32 v32, v40, v41\n"
"v_mfma_f32_16x16x32_bf16 v[200:203], a[80:83], v[36:39], v[200:203]\n"
"v_exp_f32 v44, v44\n"
"v_mfma_f32_16x16x32_bf16 v[204:207], a[84:87], v[36:39], v[204:207]\n"
"v_exp_f32 v45, v45\n"
"v_mfma_f32_16x16x32_bf16 v[208:211], a[88:91], v[36:39], v[208:211]\n"
"v_cvt_pk_bf16_f32 v33, v42, v43\n"
"v_mfma_f32_16x16x32_bf16 v[212:215], a[92:95], v[36:39], v[212:215]\n"
"s_waitcnt lgkmcnt(0)\n"
"s_barrier\n"
"v_mfma_f32_16x16x32_bf16 v[48:51], a[32:35], v[56:59], 0\n"
"s_add_u32 s76, s72, 65536\n"
"v_exp_f32 v46, v46\n"
"v_mfma_f32_16x16x32_bf16 v[48:51], a[36:39], v[60:63], v[48:51]\n"
"s_add_u32 m0, s83, 16384\n"
"v_exp_f32 v47, v47\n"
"v_mfma_f32_16x16x32_bf16 v[48:51], a[40:43], v[64:67], v[48:51]\n"
"buffer_load_dwordx4 v12, s[40:43], s76 offen lds\n"
"v_cvt_pk_bf16_f32 v34, v44, v45\n"
"v_mfma_f32_16x16x32_bf16 v[48:51], a[44:47], v[68:71], v[48:51]\n"
"ds_read_b128 a[0:3], v1 offset:32768\n"
"v_cvt_pk_bf16_f32 v35, v46, v47\n"
"v_mfma_f32_16x16x32_bf16 v[52:55], a[48:51], v[56:59], 0\n"
"ds_read_b128 a[4:7], v1 offset:33024\n"
"ds_read_b128 a[8:11], v2 offset:32768\n"
"v_mfma_f32_16x16x32_bf16 v[52:55], a[52:55], v[60:63], v[52:55]\n"
"ds_read_b128 a[12:15], v2 offset:33024\n"
"ds_read_b128 a[16:19], v1 offset:36864\n"
"v_mfma_f32_16x16x32_bf16 v[52:55], a[56:59], v[64:67], v[52:55]\n"
"ds_read_b128 a[20:23], v1 offset:37120\n"
"ds_read_b128 a[24:27], v2 offset:36864\n"
"v_mfma_f32_16x16x32_bf16 v[52:55], a[60:63], v[68:71], v[52:55]\n"
"v_exp_f32 v48, v48\n"
"v_mfma_f32_16x16x32_bf16 v[232:235], a[80:83], v[32:35], v[232:235]\n"
"v_exp_f32 v49, v49\n"
"v_mfma_f32_16x16x32_bf16 v[236:239], a[84:87], v[32:35], v[236:239]\n"
"s_add_u32 s77, s72, 73728\n"
"v_exp_f32 v50, v50\n"
"v_mfma_f32_16x16x32_bf16 v[240:243], a[88:91], v[32:35], v[240:243]\n"
"s_add_u32 m0, s83, 17408\n"
"v_exp_f32 v51, v51\n"
"v_mfma_f32_16x16x32_bf16 v[244:247], a[92:95], v[32:35], v[244:247]\n"
"buffer_load_dwordx4 v12, s[40:43], s77 offen lds\n"
"v_cvt_pk_bf16_f32 v36, v48, v49\n"
"v_mfma_f32_16x16x32_bf16 v[216:219], a[64:67], v[32:35], v[216:219]\n"
"s_add_u32 s76, s72, 81920\n"
"s_waitcnt vmcnt(6)\n"
"ds_read_b64_tr_b16 a[192:193], v13 offset:0\n"
"ds_read_b64_tr_b16 a[194:195], v13 offset:2048\n"
"v_exp_f32 v52, v52\n"
"v_mfma_f32_16x16x32_bf16 v[220:223], a[68:71], v[32:35], v[220:223]\n"
"s_add_u32 m0, s83, 18432\n"
"ds_read_b64_tr_b16 a[196:197], v14 offset:0\n"
"v_exp_f32 v53, v53\n"
"v_mfma_f32_16x16x32_bf16 v[224:227], a[72:75], v[32:35], v[224:227]\n"
"buffer_load_dwordx4 v12, s[40:43], s76 offen lds\n"
"ds_read_b64_tr_b16 a[198:199], v14 offset:2048\n"
"v_cvt_pk_bf16_f32 v37, v50, v51\n"
"v_mfma_f32_16x16x32_bf16 v[228:231], a[76:79], v[32:35], v[228:231]\n"
"ds_read_b128 a[28:31], v2 offset:37120\n"
"ds_read_b64_tr_b16 a[200:201], v13 offset:512\n"
"v_mfma_f32_16x16x32_bf16 v[28:31], v[20:23], v[32:35], v[28:31]\n"
"ds_read_b64_tr_b16 a[64:65], v5 offset:32768\n"
"ds_read_b64_tr_b16 a[66:67], v5 offset:36864\n"
"v_mfma_f32_16x16x32_bf16 v[40:43], a[32:35], v[72:75], 0\n"
"s_add_u32 s77, s72, 90112\n"
"ds_read_b64_tr_b16 a[68:69], v6 offset:32768\n"
"ds_read_b64_tr_b16 a[202:203], v13 offset:2560\n"
"v_exp_f32 v54, v54\n"
"v_mfma_f32_16x16x32_bf16 v[40:43], a[36:39], v[76:79], v[40:43]\n"
"s_add_u32 m0, s83, 19456\n"
"ds_read_b64_tr_b16 a[204:205], v14 offset:512\n"
"v_exp_f32 v55, v55\n"
"v_mfma_f32_16x16x32_bf16 v[40:43], a[40:43], v[80:83], v[40:43]\n"
"buffer_load_dwordx4 v12, s[40:43], s77 offen lds\n"
"ds_read_b64_tr_b16 a[206:207], v14 offset:2560\n"
"v_cvt_pk_bf16_f32 v38, v52, v53\n"
"v_mfma_f32_16x16x32_bf16 v[40:43], a[44:47], v[84:87], v[40:43]\n"
"ds_read_b64_tr_b16 a[70:71], v6 offset:36864\n"
"v_cvt_pk_bf16_f32 v39, v54, v55\n"
"v_mfma_f32_16x16x32_bf16 v[44:47], a[48:51], v[72:75], 0\n"
"ds_read_b64_tr_b16 a[72:73], v5 offset:33024\n"
"ds_read_b64_tr_b16 a[74:75], v5 offset:37120\n"
"v_mfma_f32_16x16x32_bf16 v[44:47], a[52:55], v[76:79], v[44:47]\n"
"s_add_u32 s76, s72, 98304\n"
"ds_read_b64_tr_b16 a[76:77], v6 offset:33024\n"
"ds_read_b64_tr_b16 a[78:79], v6 offset:37120\n"
"v_mfma_f32_16x16x32_bf16 v[44:47], a[56:59], v[80:83], v[44:47]\n"
"s_add_u32 m0, s83, 81920\n"
"ds_read_b64_tr_b16 a[80:81], v248 offset:32768\n"
"ds_read_b64_tr_b16 a[82:83], v248 offset:36864\n"
"v_mfma_f32_16x16x32_bf16 v[44:47], a[60:63], v[84:87], v[44:47]\n"
"buffer_load_dwordx4 v12, s[40:43], s76 offen lds\n"
"v_exp_f32 v40, v40\n"
"v_mfma_f32_16x16x32_bf16 v[120:123], a[96:99], v[36:39], v[120:123]\n"
"v_exp_f32 v41, v41\n"
"v_mfma_f32_16x16x32_bf16 v[124:127], a[100:103], v[36:39], v[124:127]\n"
"v_exp_f32 v42, v42\n"
"v_mfma_f32_16x16x32_bf16 v[128:131], a[104:107], v[36:39], v[128:131]\n"
"v_exp_f32 v43, v43\n"
"v_mfma_f32_16x16x32_bf16 v[132:135], a[108:111], v[36:39], v[132:135]\n"
"v_cvt_pk_bf16_f32 v32, v40, v41\n"
"v_add_f32 v16, v16, v40\n"
"v_mfma_f32_16x16x32_bf16 v[136:139], a[112:115], v[36:39], v[136:139]\n"
"v_exp_f32 v44, v44\n"
"v_mfma_f32_16x16x32_bf16 v[140:143], a[116:119], v[36:39], v[140:143]\n"
"v_exp_f32 v45, v45\n"
"v_mfma_f32_16x16x32_bf16 v[144:147], a[120:123], v[36:39], v[144:147]\n"
"s_add_u32 s77, s72, 106496\n"
"v_cvt_pk_bf16_f32 v33, v42, v43\n"
"v_add_f32 v16, v16, v41\n"
"v_mfma_f32_16x16x32_bf16 v[148:151], a[124:127], v[36:39], v[148:151]\n"
"s_add_u32 m0, s83, 82944\n"
"v_add_f32 v16, v16, v42\n"
"v_add_f32 v16, v16, v43\n"
"v_mfma_f32_16x16x32_bf16 v[24:27], v[20:23], v[36:39], v[24:27]\n"
"buffer_load_dwordx4 v12, s[40:43], s77 offen lds\n"
"ds_read_b64_tr_b16 a[84:85], v249 offset:32768\n"
"v_mfma_f32_16x16x32_bf16 v[48:51], a[32:35], v[88:91], 0\n"
"v_exp_f32 v46, v46\n"
"v_mfma_f32_16x16x32_bf16 v[48:51], a[36:39], v[92:95], v[48:51]\n"
"s_waitcnt vmcnt(6)\n"
"ds_read_b64_tr_b16 a[208:209], v13 offset:4096\n"
"ds_read_b64_tr_b16 a[210:211], v13 offset:6144\n"
"v_exp_f32 v47, v47\n"
"v_mfma_f32_16x16x32_bf16 v[48:51], a[40:43], v[96:99], v[48:51]\n"
"s_add_u32 s76, s72, 114688\n"
"v_cvt_pk_bf16_f32 v34, v44, v45\n"
"v_add_f32 v16, v16, v44\n"
"v_mfma_f32_16x16x32_bf16 v[48:51], a[44:47], v[100:103], v[48:51]\n"
"s_add_u32 m0, s83, 83968\n"
"v_cvt_pk_bf16_f32 v35, v46, v47\n"
"v_add_f32 v16, v16, v45\n"
"v_mfma_f32_16x16x32_bf16 v[52:55], a[48:51], v[88:91], 0\n"
"s_add_u32 s77, s72, 122880\n"
"buffer_load_dwordx4 v12, s[40:43], s76 offen lds\n"
"ds_read_b64_tr_b16 a[212:213], v14 offset:4096\n"
"v_add_f32 v16, v16, v46\n"
"v_mfma_f32_16x16x32_bf16 v[52:55], a[52:55], v[92:95], v[52:55]\n"
"s_add_u32 m0, s83, 84992\n"
"ds_read_b64_tr_b16 a[86:87], v249 offset:36864\n"
"v_add_f32 v16, v16, v47\n"
"v_mfma_f32_16x16x32_bf16 v[52:55], a[56:59], v[96:99], v[52:55]\n"
"s_add_u32 s62, s35, 2\n"
"buffer_load_dwordx4 v12, s[40:43], s77 offen lds\n"
"ds_read_b64_tr_b16 a[88:89], v248 offset:33024\n"
"v_mfma_f32_16x16x32_bf16 v[52:55], a[60:63], v[100:103], v[52:55]\n"
"s_and_b32 s62, s62, 7\n"
"ds_read_b64_tr_b16 a[214:215], v14 offset:6144\n"
"ds_read_b64_tr_b16 a[216:217], v13 offset:4608\n"
"v_exp_f32 v48, v48\n"
"v_mfma_f32_16x16x32_bf16 v[152:155], a[96:99], v[32:35], v[152:155]\n"
"s_lshl_b32 s62, s62, 17\n"
"ds_read_b64_tr_b16 a[218:219], v13 offset:6656\n"
"v_exp_f32 v49, v49\n"
"v_mfma_f32_16x16x32_bf16 v[156:159], a[100:103], v[32:35], v[156:159]\n"
"s_add_u32 s72, s81, s62\n"
"ds_read_b64_tr_b16 a[220:221], v14 offset:4608\n"
"v_exp_f32 v50, v50\n"
"v_mfma_f32_16x16x32_bf16 v[160:163], a[104:107], v[32:35], v[160:163]\n"
"s_add_u32 s76, s72, 0\n"
"ds_read_b64_tr_b16 a[222:223], v14 offset:6656\n"
"v_exp_f32 v51, v51\n"
"v_mfma_f32_16x16x32_bf16 v[164:167], a[108:111], v[32:35], v[164:167]\n"
"s_add_u32 m0, s83, 0\n"
"v_cvt_pk_bf16_f32 v36, v48, v49\n"
"v_add_f32 v17, v17, v48\n"
"v_mfma_f32_16x16x32_bf16 v[168:171], a[112:115], v[32:35], v[168:171]\n"
"buffer_load_dwordx4 v12, s[40:43], s76 offen lds\n"
"v_exp_f32 v52, v52\n"
"v_mfma_f32_16x16x32_bf16 v[172:175], a[116:119], v[32:35], v[172:175]\n"
"v_exp_f32 v53, v53\n"
"v_mfma_f32_16x16x32_bf16 v[176:179], a[120:123], v[32:35], v[176:179]\n"
"v_cvt_pk_bf16_f32 v37, v50, v51\n"
"v_add_f32 v17, v17, v49\n"
"v_mfma_f32_16x16x32_bf16 v[180:183], a[124:127], v[32:35], v[180:183]\n"
"s_waitcnt lgkmcnt(15)\n"
"v_add_f32 v17, v17, v50\n"
"v_add_f32 v17, v17, v51\n"
"v_mfma_f32_16x16x32_bf16 v[40:43], a[0:3], v[56:59], 0\n"
"v_exp_f32 v54, v54\n"
"v_mfma_f32_16x16x32_bf16 v[40:43], a[4:7], v[60:63], v[40:43]\n"
"v_exp_f32 v55, v55\n"
"v_mfma_f32_16x16x32_bf16 v[40:43], a[8:11], v[64:67], v[40:43]\n"
"s_add_u32 s77, s72, 8192\n"
"v_cvt_pk_bf16_f32 v38, v52, v53\n"
"v_add_f32 v17, v17, v52\n"
"v_mfma_f32_16x16x32_bf16 v[40:43], a[12:15], v[68:71], v[40:43]\n"
"s_add_u32 m0, s83, 1024\n"
"v_cvt_pk_bf16_f32 v39, v54, v55\n"
"v_add_f32 v17, v17, v53\n"
"v_mfma_f32_16x16x32_bf16 v[44:47], a[16:19], v[56:59], 0\n"
"buffer_load_dwordx4 v12, s[40:43], s77 offen lds\n"
"v_add_f32 v17, v17, v54\n"
"v_mfma_f32_16x16x32_bf16 v[44:47], a[20:23], v[60:63], v[44:47]\n"
"ds_read_b64_tr_b16 a[90:91], v248 offset:37120\n"
"v_add_f32 v17, v17, v55\n"
"v_mfma_f32_16x16x32_bf16 v[44:47], a[24:27], v[64:67], v[44:47]\n"
"ds_read_b64_tr_b16 a[92:93], v249 offset:33024\n"
"ds_read_b64_tr_b16 a[94:95], v249 offset:37120\n"
"v_mfma_f32_16x16x32_bf16 v[44:47], a[28:31], v[68:71], v[44:47]\n"
"ds_read_b128 a[32:35], v3 offset:32768\n"
"v_exp_f32 v40, v40\n"
"v_mfma_f32_16x16x32_bf16 v[184:187], a[96:99], v[36:39], v[184:187]\n"
"ds_read_b128 a[36:39], v3 offset:33024\n"
"v_exp_f32 v41, v41\n"
"v_mfma_f32_16x16x32_bf16 v[188:191], a[100:103], v[36:39], v[188:191]\n"
"s_add_u32 s76, s72, 16384\n"
"ds_read_b128 a[40:43], v4 offset:32768\n"
"s_waitcnt vmcnt(6)\n"
"ds_read_b64_tr_b16 a[224:225], v9 offset:16384\n"
"v_exp_f32 v42, v42\n"
"v_mfma_f32_16x16x32_bf16 v[192:195], a[104:107], v[36:39], v[192:195]\n"
"s_add_u32 m0, s83, 2048\n"
"ds_read_b64_tr_b16 a[226:227], v9 offset:18432\n"
"ds_read_b64_tr_b16 a[228:229], v10 offset:16384\n"
"v_exp_f32 v43, v43\n"
"v_mfma_f32_16x16x32_bf16 v[196:199], a[108:111], v[36:39], v[196:199]\n"
"buffer_load_dwordx4 v12, s[40:43], s76 offen lds\n"
"ds_read_b64_tr_b16 a[230:231], v10 offset:18432\n"
"v_cvt_pk_bf16_f32 v32, v40, v41\n"
"v_mfma_f32_16x16x32_bf16 v[200:203], a[112:115], v[36:39], v[200:203]\n"
"s_add_u32 s77, s72, 24576\n"
"ds_read_b128 a[44:47], v4 offset:33024\n"
"v_exp_f32 v44, v44\n"
"v_mfma_f32_16x16x32_bf16 v[204:207], a[116:119], v[36:39], v[204:207]\n"
"s_add_u32 m0, s83, 3072\n"
"ds_read_b128 a[48:51], v3 offset:36864\n"
"ds_read_b64_tr_b16 a[232:233], v9 offset:16896\n"
"v_exp_f32 v45, v45\n"
"v_mfma_f32_16x16x32_bf16 v[208:211], a[120:123], v[36:39], v[208:211]\n"
"buffer_load_dwordx4 v12, s[40:43], s77 offen lds\n"
"ds_read_b64_tr_b16 a[234:235], v9 offset:18944\n"
"v_cvt_pk_bf16_f32 v33, v42, v43\n"
"v_mfma_f32_16x16x32_bf16 v[212:215], a[124:127], v[36:39], v[212:215]\n"
"ds_read_b128 a[52:55], v3 offset:37120\n"
"ds_read_b128 a[56:59], v4 offset:36864\n"
"v_mfma_f32_16x16x32_bf16 v[48:51], a[0:3], v[72:75], 0\n"
"s_add_u32 s76, s72, 32768\n"
"ds_read_b128 a[60:63], v4 offset:37120\n"
"ds_read_b64_tr_b16 a[236:237], v10 offset:16896\n"
"v_exp_f32 v46, v46\n"
"v_mfma_f32_16x16x32_bf16 v[48:51], a[4:7], v[76:79], v[48:51]\n"
"s_add_u32 m0, s83, 65536\n"
"ds_read_b64_tr_b16 a[96:97], v7 offset:32768\n"
"ds_read_b64_tr_b16 a[238:239], v10 offset:18944\n"
"v_exp_f32 v47, v47\n"
"v_mfma_f32_16x16x32_bf16 v[48:51], a[8:11], v[80:83], v[48:51]\n"
"buffer_load_dwordx4 v12, s[40:43], s76 offen lds\n"
"v_cvt_pk_bf16_f32 v34, v44, v45\n"
"v_mfma_f32_16x16x32_bf16 v[48:51], a[12:15], v[84:87], v[48:51]\n"
"ds_read_b64_tr_b16 a[98:99], v7 offset:36864\n"
"v_cvt_pk_bf16_f32 v35, v46, v47\n"
"v_mfma_f32_16x16x32_bf16 v[52:55], a[16:19], v[72:75], 0\n"
"ds_read_b64_tr_b16 a[100:101], v8 offset:32768\n"
"ds_read_b64_tr_b16 a[102:103], v8 offset:36864\n"
"v_mfma_f32_16x16x32_bf16 v[52:55], a[20:23], v[76:79], v[52:55]\n"
"ds_read_b64_tr_b16 a[104:105], v7 offset:33024\n"
"ds_read_b64_tr_b16 a[106:107], v7 offset:37120\n"
"v_mfma_f32_16x16x32_bf16 v[52:55], a[24:27], v[80:83], v[52:55]\n"
"s_add_u32 s77, s72, 40960\n"
"ds_read_b64_tr_b16 a[108:109], v8 offset:33024\n"
"ds_read_b64_tr_b16 a[110:111], v8 offset:37120\n"
"v_mfma_f32_16x16x32_bf16 v[52:55], a[28:31], v[84:87], v[52:55]\n"
"s_add_u32 m0, s83, 66560\n"
"s_waitcnt lgkmcnt(15)\n"
"v_exp_f32 v48, v48\n"
"v_mfma_f32_16x16x32_bf16 v[120:123], a[64:67], v[32:35], v[120:123]\n"
"buffer_load_dwordx4 v12, s[40:43], s77 offen lds\n"
"v_exp_f32 v49, v49\n"
"v_mfma_f32_16x16x32_bf16 v[124:127], a[68:71], v[32:35], v[124:127]\n"
"v_exp_f32 v50, v50\n"
"v_mfma_f32_16x16x32_bf16 v[128:131], a[72:75], v[32:35], v[128:131]\n"
"v_exp_f32 v51, v51\n"
"v_mfma_f32_16x16x32_bf16 v[132:135], a[76:79], v[32:35], v[132:135]\n"
"v_cvt_pk_bf16_f32 v36, v48, v49\n"
"v_add_f32 v16, v16, v48\n"
"v_mfma_f32_16x16x32_bf16 v[136:139], a[80:83], v[32:35], v[136:139]\n"
"v_exp_f32 v52, v52\n"
"v_mfma_f32_16x16x32_bf16 v[140:143], a[84:87], v[32:35], v[140:143]\n"
"v_exp_f32 v53, v53\n"
"v_mfma_f32_16x16x32_bf16 v[144:147], a[88:91], v[32:35], v[144:147]\n"
"s_add_u32 s76, s72, 49152\n"
"s_waitcnt vmcnt(6)\n"
"ds_read_b64_tr_b16 a[240:241], v253 offset:16384\n"
"v_cvt_pk_bf16_f32 v37, v50, v51\n"
"v_add_f32 v16, v16, v49\n"
"v_mfma_f32_16x16x32_bf16 v[148:151], a[92:95], v[32:35], v[148:151]\n"
"s_add_u32 m0, s83, 67584\n"
"ds_read_b64_tr_b16 a[242:243], v253 offset:18432\n"
"v_add_f32 v16, v16, v50\n"
"v_add_f32 v16, v16, v51\n"
"v_mfma_f32_16x16x32_bf16 v[24:27], v[20:23], v[32:35], v[24:27]\n"
"buffer_load_dwordx4 v12, s[40:43], s76 offen lds\n"
"ds_read_b64_tr_b16 a[112:113], v250 offset:32768\n"
"v_mfma_f32_16x16x32_bf16 v[40:43], a[0:3], v[88:91], 0\n"
"ds_read_b64_tr_b16 a[244:245], v254 offset:16384\n"
"ds_read_b64_tr_b16 a[246:247], v254 offset:18432\n"
"v_exp_f32 v54, v54\n"
"v_mfma_f32_16x16x32_bf16 v[40:43], a[4:7], v[92:95], v[40:43]\n"
"ds_read_b64_tr_b16 a[248:249], v253 offset:16896\n"
"v_exp_f32 v55, v55\n"
"v_mfma_f32_16x16x32_bf16 v[40:43], a[8:11], v[96:99], v[40:43]\n"
"s_add_u32 s77, s72, 57344\n"
"ds_read_b64_tr_b16 a[250:251], v253 offset:18944\n"
"v_cvt_pk_bf16_f32 v38, v52, v53\n"
"v_add_f32 v16, v16, v52\n"
"v_mfma_f32_16x16x32_bf16 v[40:43], a[12:15], v[100:103], v[40:43]\n"
"s_add_u32 m0, s83, 68608\n"
"ds_read_b64_tr_b16 a[252:253], v254 offset:16896\n"
"v_cvt_pk_bf16_f32 v39, v54, v55\n"
"v_add_f32 v16, v16, v53\n"
"v_mfma_f32_16x16x32_bf16 v[44:47], a[16:19], v[88:91], 0\n"
"buffer_load_dwordx4 v12, s[40:43], s77 offen lds\n"
"v_add_f32 v16, v16, v54\n"
"v_mfma_f32_16x16x32_bf16 v[44:47], a[20:23], v[92:95], v[44:47]\n"
"s_add_u32 s76, s72, 65536\n"
"ds_read_b64_tr_b16 a[114:115], v250 offset:36864\n"
"ds_read_b64_tr_b16 a[254:255], v254 offset:18944\n"
"v_add_f32 v16, v16, v55\n"
"v_mfma_f32_16x16x32_bf16 v[44:47], a[24:27], v[96:99], v[44:47]\n"
"s_add_u32 m0, s82, 0\n"
"ds_read_b64_tr_b16 a[116:117], v251 offset:32768\n"
"ds_read_b64_tr_b16 a[118:119], v251 offset:36864\n"
"v_mfma_f32_16x16x32_bf16 v[44:47], a[28:31], v[100:103], v[44:47]\n"
"buffer_load_dwordx4 v12, s[40:43], s76 offen lds\n"
"v_exp_f32 v40, v40\n"
"v_mfma_f32_16x16x32_bf16 v[152:155], a[64:67], v[36:39], v[152:155]\n"
"v_exp_f32 v41, v41\n"
"v_mfma_f32_16x16x32_bf16 v[156:159], a[68:71], v[36:39], v[156:159]\n"
"ds_read_b64_tr_b16 a[120:121], v250 offset:33024\n"
"v_exp_f32 v42, v42\n"
"v_mfma_f32_16x16x32_bf16 v[160:163], a[72:75], v[36:39], v[160:163]\n"
"v_exp_f32 v43, v43\n"
"v_mfma_f32_16x16x32_bf16 v[164:167], a[76:79], v[36:39], v[164:167]\n"
"s_add_u32 s77, s72, 73728\n"
"v_cvt_pk_bf16_f32 v32, v40, v41\n"
"v_add_f32 v17, v17, v40\n"
"v_mfma_f32_16x16x32_bf16 v[168:171], a[80:83], v[36:39], v[168:171]\n"
"s_add_u32 m0, s82, 1024\n"
"v_exp_f32 v44, v44\n"
"v_mfma_f32_16x16x32_bf16 v[172:175], a[84:87], v[36:39], v[172:175]\n"
"buffer_load_dwordx4 v12, s[40:43], s77 offen lds\n"
"v_exp_f32 v45, v45\n"
"v_mfma_f32_16x16x32_bf16 v[176:179], a[88:91], v[36:39], v[176:179]\n"
"v_cvt_pk_bf16_f32 v33, v42, v43\n"
"v_add_f32 v17, v17, v41\n"
"v_mfma_f32_16x16x32_bf16 v[180:183], a[92:95], v[36:39], v[180:183]\n"
"v_add_f32 v17, v17, v42\n"
"v_add_f32 v17, v17, v43\n"
"v_mfma_f32_16x16x32_bf16 v[48:51], a[0:3], v[104:107], 0\n"
"v_exp_f32 v46, v46\n"
"v_mfma_f32_16x16x32_bf16 v[48:51], a[4:7], v[108:111], v[48:51]\n"
"v_exp_f32 v47, v47\n"
"v_mfma_f32_16x16x32_bf16 v[48:51], a[8:11], v[112:115], v[48:51]\n"
"s_add_u32 s76, s72, 81920\n"
"v_cvt_pk_bf16_f32 v34, v44, v45\n"
"v_add_f32 v17, v17, v44\n"
"v_mfma_f32_16x16x32_bf16 v[48:51], a[12:15], v[116:119], v[48:51]\n"
"s_add_u32 m0, s82, 2048\n"
"v_cvt_pk_bf16_f32 v35, v46, v47\n"
"v_add_f32 v17, v17, v45\n"
"v_mfma_f32_16x16x32_bf16 v[52:55], a[16:19], v[104:107], 0\n"
"buffer_load_dwordx4 v12, s[40:43], s76 offen lds\n"
"v_add_f32 v17, v17, v46\n"
"v_mfma_f32_16x16x32_bf16 v[52:55], a[20:23], v[108:111], v[52:55]\n"
"ds_read_b64_tr_b16 a[122:123], v250 offset:37120\n"
"v_add_f32 v17, v17, v47\n"
"v_mfma_f32_16x16x32_bf16 v[52:55], a[24:27], v[112:115], v[52:55]\n"
"ds_read_b64_tr_b16 a[124:125], v251 offset:33024\n"
"ds_read_b64_tr_b16 a[126:127], v251 offset:37120\n"
"v_mfma_f32_16x16x32_bf16 v[52:55], a[28:31], v[116:119], v[52:55]\n"
"v_exp_f32 v48, v48\n"
"v_mfma_f32_16x16x32_bf16 v[184:187], a[64:67], v[32:35], v[184:187]\n"
"v_exp_f32 v49, v49\n"
"v_mfma_f32_16x16x32_bf16 v[188:191], a[68:71], v[32:35], v[188:191]\n"
"s_add_u32 s77, s72, 90112\n"
"v_exp_f32 v50, v50\n"
"v_mfma_f32_16x16x32_bf16 v[192:195], a[72:75], v[32:35], v[192:195]\n"
"s_add_u32 m0, s82, 3072\n"
"v_exp_f32 v51, v51\n"
"v_mfma_f32_16x16x32_bf16 v[196:199], a[76:79], v[32:35], v[196:199]\n"
"buffer_load_dwordx4 v12, s[40:43], s77 offen lds\n"
"v_cvt_pk_bf16_f32 v36, v48, v49\n"
"v_mfma_f32_16x16x32_bf16 v[200:203], a[80:83], v[32:35], v[200:203]\n"
"s_add_u32 s76, s72, 98304\n"
"v_exp_f32 v52, v52\n"
"v_mfma_f32_16x16x32_bf16 v[204:207], a[84:87], v[32:35], v[204:207]\n"
"s_add_u32 m0, s82, 4096\n"
"v_exp_f32 v53, v53\n"
"v_mfma_f32_16x16x32_bf16 v[208:211], a[88:91], v[32:35], v[208:211]\n"
"buffer_load_dwordx4 v12, s[40:43], s76 offen lds\n"
"v_cvt_pk_bf16_f32 v37, v50, v51\n"
"v_mfma_f32_16x16x32_bf16 v[212:215], a[92:95], v[32:35], v[212:215]\n"
"s_waitcnt lgkmcnt(0)\n"
"s_barrier\n"
"v_mfma_f32_16x16x32_bf16 v[40:43], a[32:35], v[56:59], 0\n"
"v_exp_f32 v54, v54\n"
"v_mfma_f32_16x16x32_bf16 v[40:43], a[36:39], v[60:63], v[40:43]\n"
"s_add_u32 s77, s72, 106496\n"
"v_exp_f32 v55, v55\n"
"v_mfma_f32_16x16x32_bf16 v[40:43], a[40:43], v[64:67], v[40:43]\n"
"s_add_u32 m0, s82, 5120\n"
"ds_read_b128 a[0:3], v1 offset:49152\n"
"v_cvt_pk_bf16_f32 v38, v52, v53\n"
"v_mfma_f32_16x16x32_bf16 v[40:43], a[44:47], v[68:71], v[40:43]\n"
"buffer_load_dwordx4 v12, s[40:43], s77 offen lds\n"
"v_cvt_pk_bf16_f32 v39, v54, v55\n"
"v_mfma_f32_16x16x32_bf16 v[44:47], a[48:51], v[56:59], 0\n"
"ds_read_b128 a[4:7], v1 offset:49408\n"
"ds_read_b128 a[8:11], v2 offset:49152\n"
"v_mfma_f32_16x16x32_bf16 v[44:47], a[52:55], v[60:63], v[44:47]\n"
"ds_read_b128 a[12:15], v2 offset:49408\n"
"ds_read_b128 a[16:19], v1 offset:53248\n"
"v_mfma_f32_16x16x32_bf16 v[44:47], a[56:59], v[64:67], v[44:47]\n"
"ds_read_b128 a[20:23], v1 offset:53504\n"
"ds_read_b128 a[24:27], v2 offset:53248\n"
"v_mfma_f32_16x16x32_bf16 v[44:47], a[60:63], v[68:71], v[44:47]\n"
"v_exp_f32 v40, v40\n"
"v_mfma_f32_16x16x32_bf16 v[232:235], a[80:83], v[36:39], v[232:235]\n"
"v_exp_f32 v41, v41\n"
"v_mfma_f32_16x16x32_bf16 v[236:239], a[84:87], v[36:39], v[236:239]\n"
"s_add_u32 s76, s72, 114688\n"
"v_exp_f32 v42, v42\n"
"v_mfma_f32_16x16x32_bf16 v[240:243], a[88:91], v[36:39], v[240:243]\n"
"s_add_u32 m0, s82, 6144\n"
"v_exp_f32 v43, v43\n"
"v_mfma_f32_16x16x32_bf16 v[244:247], a[92:95], v[36:39], v[244:247]\n"
"buffer_load_dwordx4 v12, s[40:43], s76 offen lds\n"
"v_cvt_pk_bf16_f32 v32, v40, v41\n"
"v_mfma_f32_16x16x32_bf16 v[216:219], a[64:67], v[36:39], v[216:219]\n"
"s_add_u32 s77, s72, 122880\n"
"v_exp_f32 v44, v44\n"
"v_mfma_f32_16x16x32_bf16 v[220:223], a[68:71], v[36:39], v[220:223]\n"
"s_add_u32 m0, s82, 7168\n"
"v_exp_f32 v45, v45\n"
"v_mfma_f32_16x16x32_bf16 v[224:227], a[72:75], v[36:39], v[224:227]\n"
"s_add_u32 s62, s35, 3\n"
"buffer_load_dwordx4 v12, s[40:43], s77 offen lds\n"
"v_cvt_pk_bf16_f32 v33, v42, v43\n"
"v_mfma_f32_16x16x32_bf16 v[228:231], a[76:79], v[36:39], v[228:231]\n"
"s_and_b32 s62, s62, 7\n"
"ds_read_b128 a[28:31], v2 offset:53504\n"
"v_mfma_f32_16x16x32_bf16 v[28:31], v[20:23], v[36:39], v[28:31]\n"
"s_lshl_b32 s62, s62, 17\n"
"ds_read_b64_tr_b16 a[64:65], v5 offset:49152\n"
"ds_read_b64_tr_b16 a[66:67], v5 offset:53248\n"
"v_mfma_f32_16x16x32_bf16 v[48:51], a[32:35], v[72:75], 0\n"
"s_add_u32 s72, s81, s62\n"
"s_add_u32 s76, s72, 0\n"
"v_exp_f32 v46, v46\n"
"v_mfma_f32_16x16x32_bf16 v[48:51], a[36:39], v[76:79], v[48:51]\n"
"s_add_u32 m0, s83, 32768\n"
"v_exp_f32 v47, v47\n"
"v_mfma_f32_16x16x32_bf16 v[48:51], a[40:43], v[80:83], v[48:51]\n"
"buffer_load_dwordx4 v12, s[40:43], s76 offen lds\n"
"v_cvt_pk_bf16_f32 v34, v44, v45\n"
"v_mfma_f32_16x16x32_bf16 v[48:51], a[44:47], v[84:87], v[48:51]\n"
"ds_read_b64_tr_b16 a[68:69], v6 offset:49152\n"
"v_cvt_pk_bf16_f32 v35, v46, v47\n"
"v_mfma_f32_16x16x32_bf16 v[52:55], a[48:51], v[72:75], 0\n"
"ds_read_b64_tr_b16 a[70:71], v6 offset:53248\n"
"ds_read_b64_tr_b16 a[72:73], v5 offset:49408\n"
"v_mfma_f32_16x16x32_bf16 v[52:55], a[52:55], v[76:79], v[52:55]\n"
"ds_read_b64_tr_b16 a[74:75], v5 offset:53504\n"
"ds_read_b64_tr_b16 a[76:77], v6 offset:49408\n"
"v_mfma_f32_16x16x32_bf16 v[52:55], a[56:59], v[80:83], v[52:55]\n"
"s_add_u32 s77, s72, 8192\n"
"ds_read_b64_tr_b16 a[78:79], v6 offset:53504\n"
"ds_read_b64_tr_b16 a[80:81], v248 offset:49152\n"
"v_mfma_f32_16x16x32_bf16 v[52:55], a[60:63], v[84:87], v[52:55]\n"
"s_add_u32 m0, s83, 33792\n"
"v_exp_f32 v48, v48\n"
"v_mfma_f32_16x16x32_bf16 v[120:123], a[96:99], v[32:35], v[120:123]\n"
"buffer_load_dwordx4 v12, s[40:43], s77 offen lds\n"
"v_exp_f32 v49, v49\n"
"v_mfma_f32_16x16x32_bf16 v[124:127], a[100:103], v[32:35], v[124:127]\n"
"v_exp_f32 v50, v50\n"
"v_mfma_f32_16x16x32_bf16 v[128:131], a[104:107], v[32:35], v[128:131]\n"
"v_exp_f32 v51, v51\n"
"v_mfma_f32_16x16x32_bf16 v[132:135], a[108:111], v[32:35], v[132:135]\n"
"v_cvt_pk_bf16_f32 v36, v48, v49\n"
"v_add_f32 v16, v16, v48\n"
"v_mfma_f32_16x16x32_bf16 v[136:139], a[112:115], v[32:35], v[136:139]\n"
"v_exp_f32 v52, v52\n"
"v_mfma_f32_16x16x32_bf16 v[140:143], a[116:119], v[32:35], v[140:143]\n"
"v_exp_f32 v53, v53\n"
"v_mfma_f32_16x16x32_bf16 v[144:147], a[120:123], v[32:35], v[144:147]\n"
"s_add_u32 s76, s72, 16384\n"
"v_cvt_pk_bf16_f32 v37, v50, v51\n"
"v_add_f32 v16, v16, v49\n"
"v_mfma_f32_16x16x32_bf16 v[148:151], a[124:127], v[32:35], v[148:151]\n"
"s_add_u32 m0, s83, 34816\n"
"v_add_f32 v16, v16, v50\n"
"v_add_f32 v16, v16, v51\n"
"v_mfma_f32_16x16x32_bf16 v[24:27], v[20:23], v[32:35], v[24:27]\n"
"buffer_load_dwordx4 v12, s[40:43], s76 offen lds\n"
"ds_read_b64_tr_b16 a[82:83], v248 offset:53248\n"
"v_mfma_f32_16x16x32_bf16 v[40:43], a[32:35], v[88:91], 0\n"
"v_exp_f32 v54, v54\n"
"v_mfma_f32_16x16x32_bf16 v[40:43], a[36:39], v[92:95], v[40:43]\n"
"v_exp_f32 v55, v55\n"
"v_mfma_f32_16x16x32_bf16 v[40:43], a[40:43], v[96:99], v[40:43]\n"
"s_add_u32 s77, s72, 24576\n"
"v_cvt_pk_bf16_f32 v38, v52, v53\n"
"v_add_f32 v16, v16, v52\n"
"v_mfma_f32_16x16x32_bf16 v[40:43], a[44:47], v[100:103], v[40:43]\n"
"s_add_u32 m0, s83, 35840\n"
"v_cvt_pk_bf16_f32 v39, v54, v55\n"
"v_add_f32 v16, v16, v53\n"
"v_mfma_f32_16x16x32_bf16 v[44:47], a[48:51], v[88:91], 0\n"
"buffer_load_dwordx4 v12, s[40:43], s77 offen lds\n"
"v_add_f32 v16, v16, v54\n"
"v_mfma_f32_16x16x32_bf16 v[44:47], a[52:55], v[92:95], v[44:47]\n"
"s_add_u32 s76, s72, 32768\n"
"ds_read_b64_tr_b16 a[84:85], v249 offset:49152\n"
"v_add_f32 v16, v16, v55\n"
"v_mfma_f32_16x16x32_bf16 v[44:47], a[56:59], v[96:99], v[44:47]\n"
"s_add_u32 m0, s83, 98304\n"
"ds_read_b64_tr_b16 a[86:87], v249 offset:53248\n"
"ds_read_b64_tr_b16 a[88:89], v248 offset:49408\n"
"v_mfma_f32_16x16x32_bf16 v[44:47], a[60:63], v[100:103], v[44:47]\n"
"buffer_load_dwordx4 v12, s[40:43], s76 offen lds\n"
"v_exp_f32 v40, v40\n"
"v_mfma_f32_16x16x32_bf16 v[152:155], a[96:99], v[36:39], v[152:155]\n"
"v_exp_f32 v41, v41\n"
"v_mfma_f32_16x16x32_bf16 v[156:159], a[100:103], v[36:39], v[156:159]\n"
"v_exp_f32 v42, v42\n"
"v_mfma_f32_16x16x32_bf16 v[160:163], a[104:107], v[36:39], v[160:163]\n"
"v_exp_f32 v43, v43\n"
"v_mfma_f32_16x16x32_bf16 v[164:167], a[108:111], v[36:39], v[164:167]\n"
"s_add_u32 s77, s72, 40960\n"
"v_cvt_pk_bf16_f32 v32, v40, v41\n"
"v_add_f32 v17, v17, v40\n"
"v_mfma_f32_16x16x32_bf16 v[168:171], a[112:115], v[36:39], v[168:171]\n"
"s_add_u32 m0, s83, 99328\n"
"v_exp_f32 v44, v44\n"
"v_mfma_f32_16x16x32_bf16 v[172:175], a[116:119], v[36:39], v[172:175]\n"
"buffer_load_dwordx4 v12, s[40:43], s77 offen lds\n"
"v_exp_f32 v45, v45\n"
"v_mfma_f32_16x16x32_bf16 v[176:179], a[120:123], v[36:39], v[176:179]\n"
"v_cvt_pk_bf16_f32 v33, v42, v43\n"
"v_add_f32 v17, v17, v41\n"
"v_mfma_f32_16x16x32_bf16 v[180:183], a[124:127], v[36:39], v[180:183]\n"
"s_waitcnt lgkmcnt(13)\n"
"v_add_f32 v17, v17, v42\n"
"v_add_f32 v17, v17, v43\n"
"v_mfma_f32_16x16x32_bf16 v[48:51], a[0:3], v[56:59], 0\n"
"v_exp_f32 v46, v46\n"
"v_mfma_f32_16x16x32_bf16 v[48:51], a[4:7], v[60:63], v[48:51]\n"
"v_exp_f32 v47, v47\n"
"v_mfma_f32_16x16x32_bf16 v[48:51], a[8:11], v[64:67], v[48:51]\n"
"s_add_u32 s76, s72, 49152\n"
"v_cvt_pk_bf16_f32 v34, v44, v45\n"
"v_add_f32 v17, v17, v44\n"
"v_mfma_f32_16x16x32_bf16 v[48:51], a[12:15], v[68:71], v[48:51]\n"
"s_add_u32 m0, s83, 100352\n"
"v_cvt_pk_bf16_f32 v35, v46, v47\n"
"v_add_f32 v17, v17, v45\n"
"v_mfma_f32_16x16x32_bf16 v[52:55], a[16:19], v[56:59], 0\n"
"buffer_load_dwordx4 v12, s[40:43], s76 offen lds\n"
"v_add_f32 v17, v17, v46\n"
"v_mfma_f32_16x16x32_bf16 v[52:55], a[20:23], v[60:63], v[52:55]\n"
"ds_read_b64_tr_b16 a[90:91], v248 offset:53504\n"
"v_add_f32 v17, v17, v47\n"
"v_mfma_f32_16x16x32_bf16 v[52:55], a[24:27], v[64:67], v[52:55]\n"
"ds_read_b64_tr_b16 a[92:93], v249 offset:49408\n"
"ds_read_b64_tr_b16 a[94:95], v249 offset:53504\n"
"v_mfma_f32_16x16x32_bf16 v[52:55], a[28:31], v[68:71], v[52:55]\n"
"ds_read_b128 a[32:35], v3 offset:49152\n"
"v_exp_f32 v48, v48\n"
"v_mfma_f32_16x16x32_bf16 v[184:187], a[96:99], v[32:35], v[184:187]\n"
"ds_read_b128 a[36:39], v3 offset:49408\n"
"v_exp_f32 v49, v49\n"
"v_mfma_f32_16x16x32_bf16 v[188:191], a[100:103], v[32:35], v[188:191]\n"
"s_add_u32 s77, s72, 57344\n"
"v_exp_f32 v50, v50\n"
"v_mfma_f32_16x16x32_bf16 v[192:195], a[104:107], v[32:35], v[192:195]\n"
"s_add_u32 m0, s83, 101376\n"
"ds_read_b128 a[40:43], v4 offset:49152\n"
"v_exp_f32 v51, v51\n"
"v_mfma_f32_16x16x32_bf16 v[196:199], a[108:111], v[32:35], v[196:199]\n"
"buffer_load_dwordx4 v12, s[40:43], s77 offen lds\n"
"v_cvt_pk_bf16_f32 v36, v48, v49\n"
"v_mfma_f32_16x16x32_bf16 v[200:203], a[112:115], v[32:35], v[200:203]\n"
"s_add_u32 s76, s72, 65536\n"
"ds_read_b128 a[44:47], v4 offset:49408\n"
"v_exp_f32 v52, v52\n"
"v_mfma_f32_16x16x32_bf16 v[204:207], a[116:119], v[32:35], v[204:207]\n"
"s_add_u32 m0, s83, 16384\n"
"v_exp_f32 v53, v53\n"
"v_mfma_f32_16x16x32_bf16 v[208:211], a[120:123], v[32:35], v[208:211]\n"
"buffer_load_dwordx4 v12, s[40:43], s76 offen lds\n"
"v_cvt_pk_bf16_f32 v37, v50, v51\n"
"v_mfma_f32_16x16x32_bf16 v[212:215], a[124:127], v[32:35], v[212:215]\n"
"ds_read_b128 a[48:51], v3 offset:53248\n"
"ds_read_b128 a[52:55], v3 offset:53504\n"
"v_mfma_f32_16x16x32_bf16 v[40:43], a[0:3], v[72:75], 0\n"
"ds_read_b128 a[56:59], v4 offset:53248\n"
"v_exp_f32 v54, v54\n"
"v_mfma_f32_16x16x32_bf16 v[40:43], a[4:7], v[76:79], v[40:43]\n"
"s_add_u32 s77, s72, 73728\n"
"v_exp_f32 v55, v55\n"
"v_mfma_f32_16x16x32_bf16 v[40:43], a[8:11], v[80:83], v[40:43]\n"
"s_add_u32 m0, s83, 17408\n"
"ds_read_b128 a[60:63], v4 offset:53504\n"
"v_cvt_pk_bf16_f32 v38, v52, v53\n"
"v_mfma_f32_16x16x32_bf16 v[40:43], a[12:15], v[84:87], v[40:43]\n"
"s_sub_u32 s60, s83, s82\n"
"buffer_load_dwordx4 v12, s[40:43], s77 offen lds\n"
"v_cvt_pk_bf16_f32 v39, v54, v55\n"
"v_mfma_f32_16x16x32_bf16 v[44:47], a[16:19], v[72:75], 0\n"
"ds_read_b64_tr_b16 a[96:97], v7 offset:49152\n"
"ds_read_b64_tr_b16 a[98:99], v7 offset:53248\n"
"v_add_u32 v1, s60, v13\n"
"v_mfma_f32_16x16x32_bf16 v[44:47], a[20:23], v[76:79], v[44:47]\n"
"ds_read_b64_tr_b16 a[100:101], v8 offset:49152\n"
"ds_read_b64_tr_b16 a[102:103], v8 offset:53248\n"
"v_xor_b32 v2, 32, v1\n"
"v_mfma_f32_16x16x32_bf16 v[44:47], a[24:27], v[80:83], v[44:47]\n"
"s_add_u32 s76, s72, 81920\n"
"ds_read_b64_tr_b16 a[104:105], v7 offset:49408\n"
"ds_read_b64_tr_b16 a[106:107], v7 offset:53504\n"
"v_add_u32 v5, 0x10000, v1\n"
"v_mfma_f32_16x16x32_bf16 v[44:47], a[28:31], v[84:87], v[44:47]\n"
"s_add_u32 m0, s83, 18432\n"
"s_waitcnt lgkmcnt(14)\n"
"ds_read_b64_tr_b16 a[108:109], v8 offset:49408\n"
"v_exp_f32 v40, v40\n"
"v_mfma_f32_16x16x32_bf16 v[120:123], a[64:67], v[36:39], v[120:123]\n"
"buffer_load_dwordx4 v12, s[40:43], s76 offen lds\n"
"v_exp_f32 v41, v41\n"
"v_mfma_f32_16x16x32_bf16 v[124:127], a[68:71], v[36:39], v[124:127]\n"
"v_exp_f32 v42, v42\n"
"v_mfma_f32_16x16x32_bf16 v[128:131], a[72:75], v[36:39], v[128:131]\n"
"v_exp_f32 v43, v43\n"
"v_mfma_f32_16x16x32_bf16 v[132:135], a[76:79], v[36:39], v[132:135]\n"
"v_cvt_pk_bf16_f32 v32, v40, v41\n"
"v_add_f32 v16, v16, v40\n"
"v_mfma_f32_16x16x32_bf16 v[136:139], a[80:83], v[36:39], v[136:139]\n"
"v_exp_f32 v44, v44\n"
"v_mfma_f32_16x16x32_bf16 v[140:143], a[84:87], v[36:39], v[140:143]\n"
"v_exp_f32 v45, v45\n"
"v_mfma_f32_16x16x32_bf16 v[144:147], a[88:91], v[36:39], v[144:147]\n"
"s_add_u32 s77, s72, 90112\n"
"v_cvt_pk_bf16_f32 v33, v42, v43\n"
"v_add_f32 v16, v16, v41\n"
"v_mfma_f32_16x16x32_bf16 v[148:151], a[92:95], v[36:39], v[148:151]\n"
"s_add_u32 m0, s83, 19456\n"
"v_add_f32 v16, v16, v42\n"
"v_add_f32 v16, v16, v43\n"
"v_mfma_f32_16x16x32_bf16 v[24:27], v[20:23], v[36:39], v[24:27]\n"
"buffer_load_dwordx4 v12, s[40:43], s77 offen lds\n"
"ds_read_b64_tr_b16 a[110:111], v8 offset:53504\n"
"v_xor_b32 v6, 32, v5\n"
"v_mfma_f32_16x16x32_bf16 v[48:51], a[0:3], v[88:91], 0\n"
"v_exp_f32 v46, v46\n"
"v_mfma_f32_16x16x32_bf16 v[48:51], a[4:7], v[92:95], v[48:51]\n"
"v_exp_f32 v47, v47\n"
"v_mfma_f32_16x16x32_bf16 v[48:51], a[8:11], v[96:99], v[48:51]\n"
"s_add_u32 s76, s72, 98304\n"
"v_cvt_pk_bf16_f32 v34, v44, v45\n"
"v_add_f32 v16, v16, v44\n"
"v_mfma_f32_16x16x32_bf16 v[48:51], a[12:15], v[100:103], v[48:51]\n"
"s_add_u32 m0, s83, 81920\n"
"v_cvt_pk_bf16_f32 v35, v46, v47\n"
"v_add_f32 v16, v16, v45\n"
"v_mfma_f32_16x16x32_bf16 v[52:55], a[16:19], v[88:91], 0\n"
"buffer_load_dwordx4 v12, s[40:43], s76 offen lds\n"
"v_add_f32 v16, v16, v46\n"
"v_mfma_f32_16x16x32_bf16 v[52:55], a[20:23], v[92:95], v[52:55]\n"
"ds_read_b64_tr_b16 a[112:113], v250 offset:49152\n"
"v_add_f32 v16, v16, v47\n"
"v_mfma_f32_16x16x32_bf16 v[52:55], a[24:27], v[96:99], v[52:55]\n"
"s_add_u32 s77, s72, 106496\n"
"ds_read_b64_tr_b16 a[114:115], v250 offset:53248\n"
"ds_read_b64_tr_b16 a[116:117], v251 offset:49152\n"
"v_mfma_f32_16x16x32_bf16 v[52:55], a[28:31], v[100:103], v[52:55]\n"
"s_add_u32 m0, s83, 82944\n"
"v_exp_f32 v48, v48\n"
"v_mfma_f32_16x16x32_bf16 v[152:155], a[64:67], v[32:35], v[152:155]\n"
"buffer_load_dwordx4 v12, s[40:43], s77 offen lds\n"
"v_exp_f32 v49, v49\n"
"v_mfma_f32_16x16x32_bf16 v[156:159], a[68:71], v[32:35], v[156:159]\n"
"v_exp_f32 v50, v50\n"
"v_mfma_f32_16x16x32_bf16 v[160:163], a[72:75], v[32:35], v[160:163]\n"
"ds_read_b64_tr_b16 a[118:119], v251 offset:53248\n"
"v_exp_f32 v51, v51\n"
"v_mfma_f32_16x16x32_bf16 v[164:167], a[76:79], v[32:35], v[164:167]\n"
"s_add_u32 s76, s72, 114688\n"
"v_cvt_pk_bf16_f32 v36, v48, v49\n"
"v_add_f32 v17, v17, v48\n"
"v_mfma_f32_16x16x32_bf16 v[168:171], a[80:83], v[32:35], v[168:171]\n"
"s_add_u32 m0, s83, 83968\n"
"v_exp_f32 v52, v52\n"
"v_mfma_f32_16x16x32_bf16 v[172:175], a[84:87], v[32:35], v[172:175]\n"
"buffer_load_dwordx4 v12, s[40:43], s76 offen lds\n"
"v_exp_f32 v53, v53\n"
"v_mfma_f32_16x16x32_bf16 v[176:179], a[88:91], v[32:35], v[176:179]\n"
"v_cvt_pk_bf16_f32 v37, v50, v51\n"
"v_add_f32 v17, v17, v49\n"
"v_mfma_f32_16x16x32_bf16 v[180:183], a[92:95], v[32:35], v[180:183]\n"
"v_add_f32 v17, v17, v50\n"
"v_add_f32 v17, v17, v51\n"
"v_mfma_f32_16x16x32_bf16 v[40:43], a[0:3], v[104:107], 0\n"
"v_exp_f32 v54, v54\n"
"v_mfma_f32_16x16x32_bf16 v[40:43], a[4:7], v[108:111], v[40:43]\n"
"v_exp_f32 v55, v55\n"
"v_mfma_f32_16x16x32_bf16 v[40:43], a[8:11], v[112:115], v[40:43]\n"
"s_add_u32 s77, s72, 122880\n"
"v_cvt_pk_bf16_f32 v38, v52, v53\n"
"v_add_f32 v17, v17, v52\n"
"v_mfma_f32_16x16x32_bf16 v[40:43], a[12:15], v[116:119], v[40:43]\n"
"s_add_u32 m0, s83, 84992\n"
"v_cvt_pk_bf16_f32 v39, v54, v55\n"
"v_add_f32 v17, v17, v53\n"
"v_mfma_f32_16x16x32_bf16 v[44:47], a[16:19], v[104:107], 0\n"
"buffer_load_dwordx4 v12, s[40:43], s77 offen lds\n"
"v_add_f32 v17, v17, v54\n"
"v_mfma_f32_16x16x32_bf16 v[44:47], a[20:23], v[108:111], v[44:47]\n"
"ds_read_b64_tr_b16 a[120:121], v250 offset:49408\n"
"v_add_f32 v17, v17, v55\n"
"v_mfma_f32_16x16x32_bf16 v[44:47], a[24:27], v[112:115], v[44:47]\n"
"ds_read_b64_tr_b16 a[122:123], v250 offset:53504\n"
"ds_read_b64_tr_b16 a[124:125], v251 offset:49408\n"
"v_mfma_f32_16x16x32_bf16 v[44:47], a[28:31], v[116:119], v[44:47]\n"
"v_exp_f32 v40, v40\n"
"v_mfma_f32_16x16x32_bf16 v[184:187], a[64:67], v[36:39], v[184:187]\n"
"v_exp_f32 v41, v41\n"
"v_mfma_f32_16x16x32_bf16 v[188:191], a[68:71], v[36:39], v[188:191]\n"
"v_exp_f32 v42, v42\n"
"v_mfma_f32_16x16x32_bf16 v[192:195], a[72:75], v[36:39], v[192:195]\n"
"v_exp_f32 v43, v43\n"
"v_mfma_f32_16x16x32_bf16 v[196:199], a[76:79], v[36:39], v[196:199]\n"
"ds_read_b64_tr_b16 a[126:127], v251 offset:53504\n"
"v_cvt_pk_bf16_f32 v32, v40, v41\n"
"v_mfma_f32_16x16x32_bf16 v[200:203], a[80:83], v[36:39], v[200:203]\n"
"v_exp_f32 v44, v44\n"
"v_mfma_f32_16x16x32_bf16 v[204:207], a[84:87], v[36:39], v[204:207]\n"
"v_exp_f32 v45, v45\n"
"v_mfma_f32_16x16x32_bf16 v[208:211], a[88:91], v[36:39], v[208:211]\n"
"v_cvt_pk_bf16_f32 v33, v42, v43\n"
"v_mfma_f32_16x16x32_bf16 v[212:215], a[92:95], v[36:39], v[212:215]\n"
"s_waitcnt lgkmcnt(0)\n"
"s_barrier\n"
"v_mfma_f32_16x16x32_bf16 v[48:51], a[32:35], v[56:59], 0\n"
"v_exp_f32 v46, v46\n"
"v_mfma_f32_16x16x32_bf16 v[48:51], a[36:39], v[60:63], v[48:51]\n"
"v_exp_f32 v47, v47\n"
"v_mfma_f32_16x16x32_bf16 v[48:51], a[40:43], v[64:67], v[48:51]\n"
"v_cvt_pk_bf16_f32 v34, v44, v45\n"
"v_mfma_f32_16x16x32_bf16 v[48:51], a[44:47], v[68:71], v[48:51]\n"
"v_cvt_pk_bf16_f32 v35, v46, v47\n"
"v_mfma_f32_16x16x32_bf16 v[52:55], a[48:51], v[56:59], 0\n"
"s_waitcnt vmcnt(24)\n"
"ds_read_b64_tr_b16 a[0:1], v1 offset:0\n"
"ds_read_b64_tr_b16 a[2:3], v1 offset:2048\n"
"v_mfma_f32_16x16x32_bf16 v[52:55], a[52:55], v[60:63], v[52:55]\n"
"ds_read_b64_tr_b16 a[4:5], v2 offset:0\n"
"ds_read_b64_tr_b16 a[6:7], v2 offset:2048\n"
"v_mfma_f32_16x16x32_bf16 v[52:55], a[56:59], v[64:67], v[52:55]\n"
"ds_read_b64_tr_b16 a[8:9], v1 offset:512\n"
"ds_read_b64_tr_b16 a[10:11], v1 offset:2560\n"
"v_mfma_f32_16x16x32_bf16 v[52:55], a[60:63], v[68:71], v[52:55]\n"
"ds_read_b64_tr_b16 a[12:13], v2 offset:512\n"
"v_exp_f32 v48, v48\n"
"v_mfma_f32_16x16x32_bf16 v[232:235], a[80:83], v[32:35], v[232:235]\n"
"ds_read_b64_tr_b16 a[14:15], v2 offset:2560\n"
"v_exp_f32 v49, v49\n"
"v_mfma_f32_16x16x32_bf16 v[236:239], a[84:87], v[32:35], v[236:239]\n"
"v_exp_f32 v50, v50\n"
"v_mfma_f32_16x16x32_bf16 v[240:243], a[88:91], v[32:35], v[240:243]\n"
"ds_read_b64_tr_b16 a[16:17], v5 offset:0\n"
"v_exp_f32 v51, v51\n"
"v_mfma_f32_16x16x32_bf16 v[244:247], a[92:95], v[32:35], v[244:247]\n"
"ds_read_b64_tr_b16 a[18:19], v5 offset:2048\n"
"v_cvt_pk_bf16_f32 v36, v48, v49\n"
"v_mfma_f32_16x16x32_bf16 v[216:219], a[64:67], v[32:35], v[216:219]\n"
"ds_read_b64_tr_b16 a[20:21], v6 offset:0\n"
"v_exp_f32 v52, v52\n"
"v_mfma_f32_16x16x32_bf16 v[220:223], a[68:71], v[32:35], v[220:223]\n"
"v_exp_f32 v53, v53\n"
"v_mfma_f32_16x16x32_bf16 v[224:227], a[72:75], v[32:35], v[224:227]\n"
"ds_read_b64_tr_b16 a[22:23], v6 offset:2048\n"
"v_cvt_pk_bf16_f32 v37, v50, v51\n"
"v_mfma_f32_16x16x32_bf16 v[228:231], a[76:79], v[32:35], v[228:231]\n"
"ds_read_b64_tr_b16 a[24:25], v5 offset:512\n"
"ds_read_b64_tr_b16 a[26:27], v5 offset:2560\n"
"v_mfma_f32_16x16x32_bf16 v[28:31], v[20:23], v[32:35], v[28:31]\n"
"ds_read_b64_tr_b16 a[28:29], v6 offset:512\n"
"ds_read_b64_tr_b16 a[30:31], v6 offset:2560\n"
"v_mfma_f32_16x16x32_bf16 v[40:43], a[32:35], v[72:75], 0\n"
"v_exp_f32 v54, v54\n"
"v_mfma_f32_16x16x32_bf16 v[40:43], a[36:39], v[76:79], v[40:43]\n"
"s_waitcnt lgkmcnt(8)\n"
"s_add_u32 s62, s69, 4\n"
"s_and_b32 s62, s62, 3\n"
"s_add_u32 s62, s62, s35\n"
"s_xor_b32 s62, s62, 4\n"
"s_and_b32 s62, s62, 7\n"
"s_lshl_b32 s62, s62, 17\n"
"s_add_u32 s72, s81, s62\n"
"v_exp_f32 v55, v55\n"
"v_mfma_f32_16x16x32_bf16 v[40:43], a[40:43], v[80:83], v[40:43]\n"
"s_add_u32 s65, s72, 0\n"
"s_add_u32 m0, s83, 0\n"
"v_cvt_pk_bf16_f32 v38, v52, v53\n"
"v_add_u32 v253, 0x10000, v18\n"
"v_mfma_f32_16x16x32_bf16 v[40:43], a[44:47], v[84:87], v[40:43]\n"
"buffer_load_dwordx4 v12, s[40:43], s65 offen lds\n"
"v_cvt_pk_bf16_f32 v39, v54, v55\n"
"v_add_u32 v254, 0x10000, v19\n"
"v_mfma_f32_16x16x32_bf16 v[44:47], a[48:51], v[72:75], 0\n"
"s_add_u32 s67, s72, 8192\n"
"s_add_u32 m0, s83, 1024\n"
"ds_write_b128 v18, v[232:235] offset:0\n"
"v_mfma_f32_16x16x32_bf16 v[44:47], a[52:55], v[76:79], v[44:47]\n"
"buffer_load_dwordx4 v12, s[40:43], s67 offen lds\n"
"ds_write_b32 v253, v28\n"
"v_mfma_f32_16x16x32_bf16 v[44:47], a[56:59], v[80:83], v[44:47]\n"
"s_add_u32 s65, s72, 16384\n"
"s_add_u32 m0, s83, 2048\n"
"ds_write_b128 v18, v[236:239] offset:1024\n"
"v_mfma_f32_16x16x32_bf16 v[44:47], a[60:63], v[84:87], v[44:47]\n"
"buffer_load_dwordx4 v12, s[40:43], s65 offen lds\n"
"s_waitcnt vmcnt(19)\n"
"ds_read_b64_tr_b16 a[64:65], v13 offset:0\n"
"v_exp_f32 v40, v40\n"
"v_mfma_f32_16x16x32_bf16 v[120:123], a[96:99], v[36:39], v[120:123]\n"
"s_add_u32 s67, s72, 24576\n"
"s_add_u32 m0, s83, 3072\n"
"ds_read_b64_tr_b16 a[66:67], v13 offset:2048\n"
"ds_read_b64_tr_b16 a[68:69], v14 offset:0\n"
"v_exp_f32 v41, v41\n"
"v_mfma_f32_16x16x32_bf16 v[124:127], a[100:103], v[36:39], v[124:127]\n"
"s_waitcnt lgkmcnt(6)\n"
"buffer_load_dwordx4 v12, s[40:43], s67 offen lds\n"
"ds_read_b64_tr_b16 a[70:71], v14 offset:2048\n"
"v_exp_f32 v42, v42\n"
"v_mfma_f32_16x16x32_bf16 v[128:131], a[104:107], v[36:39], v[128:131]\n"
"s_add_u32 s65, s72, 32768\n"
"s_add_u32 m0, s83, 65536\n"
"ds_read_b64_tr_b16 a[72:73], v13 offset:512\n"
"ds_read_b64_tr_b16 a[74:75], v13 offset:2560\n"
"v_exp_f32 v43, v43\n"
"v_mfma_f32_16x16x32_bf16 v[132:135], a[108:111], v[36:39], v[132:135]\n"
"buffer_load_dwordx4 v12, s[40:43], s65 offen lds\n"
"ds_read_b64_tr_b16 a[76:77], v14 offset:512\n"
"v_cvt_pk_bf16_f32 v32, v40, v41\n"
"v_add_f32 v16, v16, v40\n"
"v_mfma_f32_16x16x32_bf16 v[136:139], a[112:115], v[36:39], v[136:139]\n"
"s_add_u32 s67, s72, 40960\n"
"s_add_u32 m0, s83, 66560\n"
"ds_read_b64_tr_b16 a[78:79], v14 offset:2560\n"
"ds_read_b64_tr_b16 a[80:81], v13 offset:4096\n"
"v_exp_f32 v44, v44\n"
"v_mfma_f32_16x16x32_bf16 v[140:143], a[116:119], v[36:39], v[140:143]\n"
"buffer_load_dwordx4 v12, s[40:43], s67 offen lds\n"
"v_exp_f32 v45, v45\n"
"v_mfma_f32_16x16x32_bf16 v[144:147], a[120:123], v[36:39], v[144:147]\n"
"s_add_u32 s65, s72, 49152\n"
"s_add_u32 m0, s83, 67584\n"
"ds_read_b64_tr_b16 a[82:83], v13 offset:6144\n"
"v_cvt_pk_bf16_f32 v33, v42, v43\n"
"v_add_f32 v16, v16, v41\n"
"v_mfma_f32_16x16x32_bf16 v[148:151], a[124:127], v[36:39], v[148:151]\n"
"buffer_load_dwordx4 v12, s[40:43], s65 offen lds\n"
"ds_write_b128 v18, v[240:243] offset:2048\n"
"ds_read_b64_tr_b16 a[84:85], v14 offset:4096\n"
"v_add_f32 v16, v16, v42\n"
"v_add_f32 v16, v16, v43\n"
"v_mfma_f32_16x16x32_bf16 v[24:27], v[20:23], v[36:39], v[24:27]\n"
"s_add_u32 s67, s72, 57344\n"
"s_add_u32 m0, s83, 68608\n"
"ds_write_b128 v18, v[244:247] offset:3072\n"
"ds_read_b64_tr_b16 a[86:87], v14 offset:6144\n"
"ds_read_b64_tr_b16 a[88:89], v13 offset:4608\n"
"v_mfma_f32_16x16x32_bf16 v[48:51], a[32:35], v[88:91], 0\n"
"buffer_load_dwordx4 v12, s[40:43], s67 offen lds\n"
"ds_read_b64_tr_b16 a[90:91], v13 offset:6656\n"
"v_exp_f32 v46, v46\n"
"v_mfma_f32_16x16x32_bf16 v[48:51], a[36:39], v[92:95], v[48:51]\n"
"ds_read_b64_tr_b16 a[92:93], v14 offset:4608\n"
"ds_read_b64_tr_b16 a[94:95], v14 offset:6656\n"
"v_exp_f32 v47, v47\n"
"v_mfma_f32_16x16x32_bf16 v[48:51], a[40:43], v[96:99], v[48:51]\n"
"v_cvt_pk_bf16_f32 v34, v44, v45\n"
"v_add_f32 v16, v16, v44\n"
"v_mfma_f32_16x16x32_bf16 v[48:51], a[44:47], v[100:103], v[48:51]\n"
"s_waitcnt lgkmcnt(0)\n"
"v_cvt_pk_bf16_f32 v35, v46, v47\n"
"v_add_f32 v16, v16, v45\n"
"v_mfma_f32_16x16x32_bf16 v[52:55], a[48:51], v[88:91], 0\n"
"s_barrier\n"
"v_add_f32 v16, v16, v46\n"
"v_rcp_f32 v9, v24\n"
"v_mfma_f32_16x16x32_bf16 v[52:55], a[52:55], v[92:95], v[52:55]\n"
"v_add_f32 v16, v16, v47\n"
"v_mfma_f32_16x16x32_bf16 v[52:55], a[56:59], v[96:99], v[52:55]\n"
"s_add_u32 s65, s72, 65536\n"
"s_add_u32 m0, s82, 0\n"
"ds_read_b32 v255, v254\n"
"ds_read_b128 v[232:235], v19 offset:0\n"
"v_mul_f32 v120, v120, v9\n"
"v_mul_f32 v121, v121, v9\n"
"v_mul_f32 v122, v122, v9\n"
"v_mul_f32 v123, v123, v9\n"
"v_mfma_f32_16x16x32_bf16 v[52:55], a[60:63], v[100:103], v[52:55]\n"
"buffer_load_dwordx4 v12, s[40:43], s65 offen lds\n"
"v_exp_f32 v48, v48\n"
"v_mul_f32 v124, v124, v9\n"
"v_mul_f32 v125, v125, v9\n"
"v_mul_f32 v126, v126, v9\n"
"v_mul_f32 v127, v127, v9\n"
"v_mfma_f32_16x16x32_bf16 v[152:155], a[96:99], v[32:35], v[152:155]\n"
"s_add_u32 s67, s72, 73728\n"
"s_add_u32 m0, s82, 1024\n"
"ds_read_b128 v[236:239], v19 offset:1024\n"
"v_exp_f32 v49, v49\n"
"v_cvt_pk_bf16_f32 v120, v120, v121\n"
"v_cvt_pk_bf16_f32 v121, v122, v123\n"
"v_cvt_pk_bf16_f32 v122, v124, v125\n"
"v_cvt_pk_bf16_f32 v123, v126, v127\n"
"v_mfma_f32_16x16x32_bf16 v[156:159], a[100:103], v[32:35], v[156:159]\n"
"s_add_u32 s76, s78, 0\n"
"buffer_load_dwordx4 v12, s[40:43], s67 offen lds\n"
"v_exp_f32 v50, v50\n"
"v_mul_f32 v128, v128, v9\n"
"v_mul_f32 v129, v129, v9\n"
"v_mul_f32 v130, v130, v9\n"
"v_mul_f32 v131, v131, v9\n"
"v_mfma_f32_16x16x32_bf16 v[160:163], a[104:107], v[32:35], v[160:163]\n"
"s_add_u32 s65, s72, 81920\n"
"s_add_u32 m0, s82, 2048\n"
"ds_read_b128 v[240:243], v19 offset:2048\n"
"buffer_store_dwordx4 v[120:123], v11, s[36:39], s76 offen sc1\n"
"v_exp_f32 v51, v51\n"
"v_mul_f32 v132, v132, v9\n"
"v_mul_f32 v133, v133, v9\n"
"v_mul_f32 v134, v134, v9\n"
"v_mul_f32 v135, v135, v9\n"
"v_mfma_f32_16x16x32_bf16 v[164:167], a[108:111], v[32:35], v[164:167]\n"
"buffer_load_dwordx4 v12, s[40:43], s65 offen lds\n"
"v_cvt_pk_bf16_f32 v36, v48, v49\n"
"v_add_f32 v17, v17, v48\n"
"v_cvt_pk_bf16_f32 v128, v128, v129\n"
"v_cvt_pk_bf16_f32 v129, v130, v131\n"
"v_cvt_pk_bf16_f32 v130, v132, v133\n"
"v_cvt_pk_bf16_f32 v131, v134, v135\n"
"v_mfma_f32_16x16x32_bf16 v[168:171], a[112:115], v[32:35], v[168:171]\n"
"s_add_u32 s67, s72, 90112\n"
"s_add_u32 m0, s82, 3072\n"
"ds_read_b128 v[244:247], v19 offset:3072\n"
"v_exp_f32 v52, v52\n"
"v_mul_f32 v136, v136, v9\n"
"v_mul_f32 v137, v137, v9\n"
"v_mul_f32 v138, v138, v9\n"
"v_mul_f32 v139, v139, v9\n"
"v_mfma_f32_16x16x32_bf16 v[172:175], a[116:119], v[32:35], v[172:175]\n"
"s_add_u32 s77, s78, 1024\n"
"buffer_load_dwordx4 v12, s[40:43], s67 offen lds\n"
"v_exp_f32 v53, v53\n"
"v_mul_f32 v140, v140, v9\n"
"v_mul_f32 v141, v141, v9\n"
"v_mul_f32 v142, v142, v9\n"
"v_mul_f32 v143, v143, v9\n"
"v_mfma_f32_16x16x32_bf16 v[176:179], a[120:123], v[32:35], v[176:179]\n"
"s_add_u32 s65, s72, 98304\n"
"s_add_u32 m0, s82, 4096\n"
"buffer_store_dwordx4 v[128:131], v11, s[36:39], s77 offen sc1\n"
"v_cvt_pk_bf16_f32 v37, v50, v51\n"
"v_add_f32 v17, v17, v49\n"
"v_cvt_pk_bf16_f32 v136, v136, v137\n"
"v_cvt_pk_bf16_f32 v137, v138, v139\n"
"v_cvt_pk_bf16_f32 v138, v140, v141\n"
"v_cvt_pk_bf16_f32 v139, v142, v143\n"
"v_mfma_f32_16x16x32_bf16 v[180:183], a[124:127], v[32:35], v[180:183]\n"
"buffer_load_dwordx4 v12, s[40:43], s65 offen lds\n"
"v_add_f32 v17, v17, v50\n"
"v_add_f32 v17, v17, v51\n"
"v_exp_f32 v54, v54\n"
"v_exp_f32 v55, v55\n"
"v_cvt_pk_bf16_f32 v38, v52, v53\n"
"v_add_f32 v17, v17, v52\n"
"v_cvt_pk_bf16_f32 v39, v54, v55\n"
"v_add_f32 v17, v17, v53\n"
"v_add_f32 v17, v17, v54\n"
"v_add_f32 v17, v17, v55\n"
"v_mul_f32 v144, v144, v9\n"
"v_mul_f32 v145, v145, v9\n"
"v_mul_f32 v146, v146, v9\n"
"v_mul_f32 v147, v147, v9\n"
"v_mfma_f32_16x16x32_bf16 v[184:187], a[96:99], v[36:39], v[184:187]\n"
"s_add_u32 s67, s72, 106496\n"
"s_add_u32 m0, s82, 5120\n"
"s_waitcnt vmcnt(23)\n"
"ds_read_b64_tr_b16 a[32:33], v1 offset:32768\n"
"ds_read_b64_tr_b16 a[34:35], v1 offset:34816\n"
"ds_read_b64_tr_b16 a[36:37], v2 offset:32768\n"
"v_mul_f32 v148, v148, v9\n"
"v_mul_f32 v149, v149, v9\n"
"v_mul_f32 v150, v150, v9\n"
"v_mul_f32 v151, v151, v9\n"
"v_mfma_f32_16x16x32_bf16 v[188:191], a[100:103], v[36:39], v[188:191]\n"
"s_add_u32 s76, s79, 0\n"
"buffer_load_dwordx4 v12, s[40:43], s67 offen lds\n"
"ds_read_b64_tr_b16 a[38:39], v2 offset:34816\n"
"v_cvt_pk_bf16_f32 v144, v144, v145\n"
"v_cvt_pk_bf16_f32 v145, v146, v147\n"
"v_cvt_pk_bf16_f32 v146, v148, v149\n"
"v_cvt_pk_bf16_f32 v147, v150, v151\n"
"v_mfma_f32_16x16x32_bf16 v[192:195], a[104:107], v[36:39], v[192:195]\n"
"s_add_u32 s65, s72, 114688\n"
"s_add_u32 m0, s82, 6144\n"
"ds_read_b64_tr_b16 a[40:41], v1 offset:33280\n"
"ds_read_b64_tr_b16 a[42:43], v1 offset:35328\n"
"ds_read_b64_tr_b16 a[44:45], v2 offset:33280\n"
"buffer_store_dwordx4 v[136:139], v11, s[36:39], s76 offen sc1\n"
"v_mfma_f32_16x16x32_bf16 v[196:199], a[108:111], v[36:39], v[196:199]\n"
"buffer_load_dwordx4 v12, s[40:43], s65 offen lds\n"
"ds_read_b64_tr_b16 a[46:47], v2 offset:35328\n"
"v_mfma_f32_16x16x32_bf16 v[200:203], a[112:115], v[36:39], v[200:203]\n"
"s_add_u32 s67, s72, 122880\n"
"s_add_u32 m0, s82, 7168\n"
"ds_read_b64_tr_b16 a[48:49], v5 offset:32768\n"
"ds_read_b64_tr_b16 a[50:51], v5 offset:34816\n"
"v_mfma_f32_16x16x32_bf16 v[204:207], a[116:119], v[36:39], v[204:207]\n"
"s_add_u32 s77, s79, 1024\n"
"buffer_load_dwordx4 v12, s[40:43], s67 offen lds\n"
"ds_read_b64_tr_b16 a[52:53], v6 offset:32768\n"
"ds_read_b64_tr_b16 a[54:55], v6 offset:34816\n"
"v_mfma_f32_16x16x32_bf16 v[208:211], a[120:123], v[36:39], v[208:211]\n"
"ds_read_b64_tr_b16 a[56:57], v5 offset:33280\n"
"ds_read_b64_tr_b16 a[58:59], v5 offset:35328\n"
"buffer_store_dwordx4 v[144:147], v11, s[36:39], s77 offen sc1\n"
"v_mfma_f32_16x16x32_bf16 v[212:215], a[124:127], v[36:39], v[212:215]\n"
"ds_read_b64_tr_b16 a[60:61], v6 offset:33280\n"
"ds_read_b64_tr_b16 a[62:63], v6 offset:35328\n"
"v_mfma_f32_16x16x4_f32 v[40:43], v252, v16, 0\n"
"v_mfma_f32_16x16x4_f32 v[44:47], v252, v17, 0\n"
"s_waitcnt vmcnt(20) lgkmcnt(15)\n"
"v_mbcnt_lo_u32_b32 v0, -1, 0\n"
"v_mbcnt_hi_u32_b32 v0, -1, v0\n"
"v_add_f32 v28, v28, v255\n"
"v_add_f32 v216, v216, v232\n"
"v_add_f32 v217, v217, v233\n"
"v_add_f32 v218, v218, v234\n"
"v_add_f32 v219, v219, v235\n"
"v_add_f32 v220, v220, v236\n"
"v_add_f32 v221, v221, v237\n"
"v_add_f32 v222, v222, v238\n"
"v_add_f32 v223, v223, v239\n"
"v_add_f32 v224, v224, v240\n"
"v_add_f32 v225, v225, v241\n"
"v_add_f32 v226, v226, v242\n"
"v_add_f32 v227, v227, v243\n"
"v_add_f32 v228, v228, v244\n"
"v_add_f32 v229, v229, v245\n"
"v_add_f32 v230, v230, v246\n"
"v_add_f32 v231, v231, v247\n"
"v_max3_f32 v253, v24, v28, v40\n"
"v_min3_f32 v254, v24, v28, v40\n"
"v_max_f32 v253, v253, v44\n"
"v_min_f32 v254, v254, v44\n"
"v_cmp_nge_f32 vcc, 0x5f800000, v253\n"
"s_mov_b32 s62, 0x1f800000\n"
"v_cmp_nle_f32 s[60:61], s62, v254\n"
"v_rcp_f32 v33, v28\n"
"s_or_b64 vcc, vcc, s[60:61]\n"
"s_cbranch_vccnz L_slow_%=\n"
"v_rcp_f32 v34, v40\n"
"v_rcp_f32 v35, v44\n"
"L_norm_%=:\n"
"s_add_u32 s76, s80, 0\n"
"ds_read_b64_tr_b16 a[96:97], v1 offset:16384\n"
"v_pk_mul_f32 v[216:217], v[216:217], v[32:33] op_sel:[0,1] op_sel_hi:[1,1]\n"
"ds_read_b64_tr_b16 a[98:99], v1 offset:18432\n"
"v_pk_mul_f32 v[218:219], v[218:219], v[32:33] op_sel:[0,1] op_sel_hi:[1,1]\n"
"v_pk_mul_f32 v[220:221], v[220:221], v[32:33] op_sel:[0,1] op_sel_hi:[1,1]\n"
"v_pk_mul_f32 v[222:223], v[222:223], v[32:33] op_sel:[0,1] op_sel_hi:[1,1]\n"
"v_cvt_pk_bf16_f32 v216, v216, v217\n"
"v_cvt_pk_bf16_f32 v217, v218, v219\n"
"v_cvt_pk_bf16_f32 v218, v220, v221\n"
"v_cvt_pk_bf16_f32 v219, v222, v223\n"
"buffer_store_dwordx4 v[216:219], v11, s[36:39], s76 offen sc1\n"
"s_add_u32 s77, s80, 1024\n"
"ds_read_b64_tr_b16 a[100:101], v2 offset:16384\n"
"v_pk_mul_f32 v[224:225], v[224:225], v[32:33] op_sel:[0,1] op_sel_hi:[1,1]\n"
"ds_read_b64_tr_b16 a[102:103], v2 offset:18432\n"
"v_pk_mul_f32 v[226:227], v[226:227], v[32:33] op_sel:[0,1] op_sel_hi:[1,1]\n"
"v_pk_mul_f32 v[228:229], v[228:229], v[32:33] op_sel:[0,1] op_sel_hi:[1,1]\n"
"v_pk_mul_f32 v[230:231], v[230:231], v[32:33] op_sel:[0,1] op_sel_hi:[1,1]\n"
"v_cvt_pk_bf16_f32 v224, v224, v225\n"
"v_cvt_pk_bf16_f32 v225, v226, v227\n"
"v_cvt_pk_bf16_f32 v226, v228, v229\n"
"v_cvt_pk_bf16_f32 v227, v230, v231\n"
"buffer_store_dwordx4 v[224:227], v11, s[36:39], s77 offen sc1\n"
"s_add_u32 s76, s78, 131072\n"
"ds_read_b64_tr_b16 a[104:105], v1 offset:16896\n"
"v_pk_mul_f32 v[152:153], v[152:153], v[34:35] op_sel_hi:[1,0]\n"
"ds_read_b64_tr_b16 a[106:107], v1 offset:18944\n"
"v_pk_mul_f32 v[154:155], v[154:155], v[34:35] op_sel_hi:[1,0]\n"
"v_pk_mul_f32 v[156:157], v[156:157], v[34:35] op_sel_hi:[1,0]\n"
"v_pk_mul_f32 v[158:159], v[158:159], v[34:35] op_sel_hi:[1,0]\n"
"v_cvt_pk_bf16_f32 v152, v152, v153\n"
"v_cvt_pk_bf16_f32 v153, v154, v155\n"
"v_cvt_pk_bf16_f32 v154, v156, v157\n"
"v_cvt_pk_bf16_f32 v155, v158, v159\n"
"buffer_store_dwordx4 v[152:155], v11, s[36:39], s76 offen sc1\n"
"s_add_u32 s77, s78, 132096\n"
"ds_read_b64_tr_b16 a[108:109], v2 offset:16896\n"
"v_pk_mul_f32 v[160:161], v[160:161], v[34:35] op_sel_hi:[1,0]\n"
"ds_read_b64_tr_b16 a[110:111], v2 offset:18944\n"
"v_pk_mul_f32 v[162:163], v[162:163], v[34:35] op_sel_hi:[1,0]\n"
"v_pk_mul_f32 v[164:165], v[164:165], v[34:35] op_sel_hi:[1,0]\n"
"v_pk_mul_f32 v[166:167], v[166:167], v[34:35] op_sel_hi:[1,0]\n"
"v_cvt_pk_bf16_f32 v160, v160, v161\n"
"v_cvt_pk_bf16_f32 v161, v162, v163\n"
"v_cvt_pk_bf16_f32 v162, v164, v165\n"
"v_cvt_pk_bf16_f32 v163, v166, v167\n"
"buffer_store_dwordx4 v[160:163], v11, s[36:39], s77 offen sc1\n"
"s_add_u32 s76, s79, 131072\n"
"v_pk_mul_f32 v[168:169], v[168:169], v[34:35] op_sel_hi:[1,0]\n"
"ds_read_b64_tr_b16 a[112:113], v5 offset:16384\n"
"v_pk_mul_f32 v[170:171], v[170:171], v[34:35] op_sel_hi:[1,0]\n"
"v_pk_mul_f32 v[172:173], v[172:173], v[34:35] op_sel_hi:[1,0]\n"
"v_pk_mul_f32 v[174:175], v[174:175], v[34:35] op_sel_hi:[1,0]\n"
"v_cvt_pk_bf16_f32 v168, v168, v169\n"
"v_cvt_pk_bf16_f32 v169, v170, v171\n"
"v_cvt_pk_bf16_f32 v170, v172, v173\n"
"v_cvt_pk_bf16_f32 v171, v174, v175\n"
"buffer_store_dwordx4 v[168:171], v11, s[36:39], s76 offen sc1\n"
"s_add_u32 s77, s79, 132096\n"
"ds_read_b64_tr_b16 a[114:115], v5 offset:18432\n"
"v_pk_mul_f32 v[176:177], v[176:177], v[34:35] op_sel_hi:[1,0]\n"
"ds_read_b64_tr_b16 a[116:117], v6 offset:16384\n"
"v_pk_mul_f32 v[178:179], v[178:179], v[34:35] op_sel_hi:[1,0]\n"
"v_pk_mul_f32 v[180:181], v[180:181], v[34:35] op_sel_hi:[1,0]\n"
"v_pk_mul_f32 v[182:183], v[182:183], v[34:35] op_sel_hi:[1,0]\n"
"v_cvt_pk_bf16_f32 v176, v176, v177\n"
"v_cvt_pk_bf16_f32 v177, v178, v179\n"
"v_cvt_pk_bf16_f32 v178, v180, v181\n"
"v_cvt_pk_bf16_f32 v179, v182, v183\n"
"buffer_store_dwordx4 v[176:179], v11, s[36:39], s77 offen sc1\n"
"s_add_u32 s76, s78, 262144\n"
"ds_read_b64_tr_b16 a[118:119], v6 offset:18432\n"
"v_pk_mul_f32 v[184:185], v[184:185], v[34:35] op_sel:[0,1] op_sel_hi:[1,1]\n"
"ds_read_b64_tr_b16 a[120:121], v5 offset:16896\n"
"v_pk_mul_f32 v[186:187], v[186:187], v[34:35] op_sel:[0,1] op_sel_hi:[1,1]\n"
"v_pk_mul_f32 v[188:189], v[188:189], v[34:35] op_sel:[0,1] op_sel_hi:[1,1]\n"
"v_pk_mul_f32 v[190:191], v[190:191], v[34:35] op_sel:[0,1] op_sel_hi:[1,1]\n"
"v_cvt_pk_bf16_f32 v184, v184, v185\n"
"v_cvt_pk_bf16_f32 v185, v186, v187\n"
"v_cvt_pk_bf16_f32 v186, v188, v189\n"
"v_cvt_pk_bf16_f32 v187, v190, v191\n"
"buffer_store_dwordx4 v[184:187], v11, s[36:39], s76 offen sc1\n"
"s_add_u32 s77, s78, 263168\n"
"ds_read_b64_tr_b16 a[122:123], v5 offset:18944\n"
"v_pk_mul_f32 v[192:193], v[192:193], v[34:35] op_sel:[0,1] op_sel_hi:[1,1]\n"
"ds_read_b64_tr_b16 a[124:125], v6 offset:16896\n"
"v_pk_mul_f32 v[194:195], v[194:195], v[34:35] op_sel:[0,1] op_sel_hi:[1,1]\n"
"v_pk_mul_f32 v[196:197], v[196:197], v[34:35] op_sel:[0,1] op_sel_hi:[1,1]\n"
"v_pk_mul_f32 v[198:199], v[198:199], v[34:35] op_sel:[0,1] op_sel_hi:[1,1]\n"
"v_cvt_pk_bf16_f32 v192, v192, v193\n"
"v_cvt_pk_bf16_f32 v193, v194, v195\n"
"v_cvt_pk_bf16_f32 v194, v196, v197\n"
"v_cvt_pk_bf16_f32 v195, v198, v199\n"
"buffer_store_dwordx4 v[192:195], v11, s[36:39], s77 offen sc1\n"
"s_add_u32 s76, s79, 262144\n"
"ds_read_b64_tr_b16 a[126:127], v6 offset:18944\n"
"v_pk_mul_f32 v[200:201], v[200:201], v[34:35] op_sel:[0,1] op_sel_hi:[1,1]\n"
"v_pk_mul_f32 v[202:203], v[202:203], v[34:35] op_sel:[0,1] op_sel_hi:[1,1]\n"
"v_pk_mul_f32 v[204:205], v[204:205], v[34:35] op_sel:[0,1] op_sel_hi:[1,1]\n"
"v_pk_mul_f32 v[206:207], v[206:207], v[34:35] op_sel:[0,1] op_sel_hi:[1,1]\n"
"v_cvt_pk_bf16_f32 v200, v200, v201\n"
"v_cvt_pk_bf16_f32 v201, v202, v203\n"
"v_cvt_pk_bf16_f32 v202, v204, v205\n"
"v_cvt_pk_bf16_f32 v203, v206, v207\n"
"buffer_store_dwordx4 v[200:203], v11, s[36:39], s76 offen sc1\n"
"s_add_u32 s77, s79, 263168\n"
"v_pk_mul_f32 v[208:209], v[208:209], v[34:35] op_sel:[0,1] op_sel_hi:[1,1]\n"
"v_pk_mul_f32 v[210:211], v[210:211], v[34:35] op_sel:[0,1] op_sel_hi:[1,1]\n"
"v_pk_mul_f32 v[212:213], v[212:213], v[34:35] op_sel:[0,1] op_sel_hi:[1,1]\n"
"v_pk_mul_f32 v[214:215], v[214:215], v[34:35] op_sel:[0,1] op_sel_hi:[1,1]\n"
"v_cvt_pk_bf16_f32 v208, v208, v209\n"
"v_cvt_pk_bf16_f32 v209, v210, v211\n"
"v_cvt_pk_bf16_f32 v210, v212, v213\n"
"v_cvt_pk_bf16_f32 v211, v214, v215\n"
"buffer_store_dwordx4 v[208:211], v11, s[36:39], s77 offen sc1\n"
"s_waitcnt vmcnt(26)\n"
"ds_read_b64_tr_b16 v[80:81], v1 offset:0\n"
"ds_read_b64_tr_b16 v[82:83], v1 offset:2048\n"
"ds_read_b64_tr_b16 v[84:85], v2 offset:0\n"
"ds_read_b64_tr_b16 v[86:87], v2 offset:2048\n"
"ds_read_b64_tr_b16 v[88:89], v1 offset:512\n"
"ds_read_b64_tr_b16 v[90:91], v1 offset:2560\n"
"ds_read_b64_tr_b16 v[92:93], v2 offset:512\n"
"ds_read_b64_tr_b16 v[94:95], v2 offset:2560\n"
"s_waitcnt vmcnt(22)\n"
"ds_read_b64_tr_b16 v[96:97], v5 offset:0\n"
"ds_read_b64_tr_b16 v[98:99], v5 offset:2048\n"
"ds_read_b64_tr_b16 v[100:101], v6 offset:0\n"
"ds_read_b64_tr_b16 v[102:103], v6 offset:2048\n"
"ds_read_b64_tr_b16 v[104:105], v5 offset:512\n"
"ds_read_b64_tr_b16 v[106:107], v5 offset:2560\n"
"ds_read_b64_tr_b16 v[108:109], v6 offset:512\n"
"ds_read_b64_tr_b16 v[110:111], v6 offset:2560\n"
"s_waitcnt vmcnt(17)\n"
"ds_read_b64_tr_b16 v[112:113], v13 offset:0\n"
"ds_read_b64_tr_b16 v[114:115], v13 offset:2048\n"
"ds_read_b64_tr_b16 v[116:117], v14 offset:0\n"
"ds_read_b64_tr_b16 v[118:119], v14 offset:2048\n"
"ds_read_b64_tr_b16 v[120:121], v13 offset:512\n"
"ds_read_b64_tr_b16 v[122:123], v13 offset:2560\n"
"ds_read_b64_tr_b16 v[124:125], v14 offset:512\n"
"ds_read_b64_tr_b16 v[126:127], v14 offset:2560\n"
"s_waitcnt vmcnt(11)\n"
"ds_read_b64_tr_b16 v[128:129], v13 offset:4096\n"
"ds_read_b64_tr_b16 v[130:131], v13 offset:6144\n"
"ds_read_b64_tr_b16 v[132:133], v14 offset:4096\n"
"ds_read_b64_tr_b16 v[134:135], v14 offset:6144\n"
"ds_read_b64_tr_b16 v[136:137], v13 offset:4608\n"
"ds_read_b64_tr_b16 v[138:139], v13 offset:6656\n"
"ds_read_b64_tr_b16 v[140:141], v14 offset:4608\n"
"ds_read_b64_tr_b16 v[142:143], v14 offset:6656\n"
"s_and_b32 s61, s57, 1\n"
"v_bfe_u32 v16, v0, 3, 1\n"
"v_cmp_eq_u32 vcc, s61, v16\n"
"v_mov_b32 v17, 0x7ff00000\n"
"s_lshr_b32 s60, s57, 1\n"
"s_mul_i32 s60, s60, 7\n"
"v_cndmask_b32 v16, v17, v11, vcc\n"
"s_lshl2_add_u32 s62, s61, s60\n"
"s_add_u32 s63, s60, 3\n"
"s_mul_i32 s60, s55, 1835008\n"
"s_lshl_b32 s76, s52, 15\n"
"s_add_u32 s60, s60, s76\n"
"s_add_u32 s76, s52, 0\n"
"s_and_b32 s76, s76, 3\n"
"s_add_u32 s77, s76, s62\n"
"s_cmp_eq_u32 s76, 3\n"
"s_cselect_b32 s77, s63, s77\n"
"s_cselect_b64 s[84:85], -1, 0\n"
"s_lshl_b32 s61, s77, 14\n"
"v_cndmask_b32 v3, v11, v16, s[84:85]\n"
"s_lshl_b32 s77, s77, 17\n"
"s_add_u32 s64, s60, s77\n"
"s_add_u32 s76, s52, 1\n"
"s_and_b32 s76, s76, 3\n"
"s_add_u32 s77, s76, s62\n"
"s_cmp_eq_u32 s76, 3\n"
"s_cselect_b32 s77, s63, s77\n"
"s_cselect_b64 s[84:85], -1, 0\n"
"v_cndmask_b32 v4, v11, v16, s[84:85]\n"
"s_lshl_b32 s77, s77, 17\n"
"s_add_u32 s65, s60, s77\n"
"s_add_u32 s76, s52, 2\n"
"s_and_b32 s76, s76, 3\n"
"s_add_u32 s77, s76, s62\n"
"s_cmp_eq_u32 s76, 3\n"
"s_cselect_b32 s77, s63, s77\n"
"s_cselect_b64 s[84:85], -1, 0\n"
"v_cndmask_b32 v7, v11, v16, s[84:85]\n"
"s_lshl_b32 s77, s77, 17\n"
"s_add_u32 s66, s60, s77\n"
"s_add_u32 s76, s52, 3\n"
"s_and_b32 s76, s76, 3\n"
"s_add_u32 s77, s76, s62\n"
"s_cmp_eq_u32 s76, 3\n"
"s_cselect_b32 s77, s63, s77\n"
"s_cselect_b64 s[84:85], -1, 0\n"
"v_cndmask_b32 v11, v11, v16, s[84:85]\n"
"s_lshl_b32 s77, s77, 17\n"
"s_add_u32 s67, s60, s77\n"
"v_and_b32 v9, 15, v0\n"
"v_lshrrev_b32 v10, 4, v0\n"
"v_lshlrev_b32 v10, 3, v10\n"
"v_cmp_eq_u32 vcc, v17, v3\n"
"v_lshl_add_u32 v10, v9, 10, v10\n"
"v_add_u32 v10, s61, v10\n"
"v_cndmask_b32 v8, v10, v17, vcc\n"
"s_mul_i32 s61, s55, 229376\n"
"s_lshl_b32 s60, s58, 7\n"
"s_add_u32 s61, s61, s60\n"
"s_add_u32 s44, s14, s61\n"
"s_addc_u32 s45, s15, 0\n"
"s_and_b32 s45, s45, 0xffff\n"
"s_mov_b32 s46, 0\n"
"s_mov_b32 s47, 0x20000\n"
"s_sub_u32 s46, 229376, s60\n"
"s_cmp_eq_u32 s52, 0\n"
"s_cselect_b32 s60, 31, 7\n"
"s_cselect_b32 s63, 32, 0\n"
"v_and_b32 v9, s60, v0\n"
"s_lshl_b32 s61, s55, 5\n"
"s_add_u32 s62, s61, s56\n"
"s_lshl_b32 s62, s62, 5\n"
"s_lshl3_add_u32 s62, s52, s62\n"
"v_add_lshl_u32 v9, v9, s62, 2\n"
"v_add_lshl_u32 v10, v0, s61, 5\n"
"v_add_lshl_u32 v10, v10, s56, 2\n"
"v_cmp_gt_u32 vcc, s63, v0\n"
"s_nop 1\n"
"v_cndmask_b32 v10, v17, v10, vcc\n"
"s_waitcnt vmcnt(0)\n"
"s_barrier\n"
"buffer_store_dword v15, v10, s[48:51], 0 offen sc1\n"
"s_add_u32 s62, s69, 5\n"
"s_and_b32 s62, s62, 3\n"
"s_add_u32 s62, s62, s35\n"
"s_xor_b32 s62, s62, 4\n"
"s_and_b32 s62, s62, 7\n"
"s_lshl_b32 s62, s62, 17\n"
"s_add_u32 s72, s81, s62\n"
"s_add_u32 m0, s83, 49152\n"
"s_add_u32 s71, s72, 0\n"
"buffer_load_dwordx4 v12, s[40:43], s71 offen lds\n"
"s_add_u32 m0, s83, 50176\n"
"s_add_u32 s70, s72, 8192\n"
"buffer_load_dwordx4 v12, s[40:43], s70 offen lds\n"
"s_add_u32 m0, s83, 51200\n"
"s_add_u32 s71, s72, 16384\n"
"buffer_load_dwordx4 v12, s[40:43], s71 offen lds\n"
"s_add_u32 m0, s83, 52224\n"
"s_add_u32 s70, s72, 24576\n"
"buffer_load_dwordx4 v12, s[40:43], s70 offen lds\n"
"s_add_u32 m0, s83, 114688\n"
"s_add_u32 s71, s72, 32768\n"
"buffer_load_dwordx4 v12, s[40:43], s71 offen lds\n"
"s_add_u32 m0, s83, 115712\n"
"s_add_u32 s70, s72, 40960\n"
"buffer_load_dwordx4 v12, s[40:43], s70 offen lds\n"
"s_add_u32 m0, s83, 116736\n"
"s_add_u32 s71, s72, 49152\n"
"buffer_load_dwordx4 v12, s[40:43], s71 offen lds\n"
"s_add_u32 m0, s83, 117760\n"
"s_add_u32 s70, s72, 57344\n"
"buffer_load_dwordx4 v12, s[40:43], s70 offen lds\n"
"s_waitcnt lgkmcnt(15)\n"
"s_add_u32 m0, s83, 32768\n"
"s_add_u32 s71, s72, 65536\n"
"buffer_load_dwordx4 v12, s[40:43], s71 offen lds\n"
"s_add_u32 m0, s83, 33792\n"
"s_add_u32 s70, s72, 73728\n"
"buffer_load_dwordx4 v12, s[40:43], s70 offen lds\n"
"s_add_u32 m0, s83, 34816\n"
"s_add_u32 s71, s72, 81920\n"
"buffer_load_dwordx4 v12, s[40:43], s71 offen lds\n"
"s_add_u32 m0, s83, 35840\n"
"s_add_u32 s70, s72, 90112\n"
"buffer_load_dwordx4 v12, s[40:43], s70 offen lds\n"
"s_add_u32 m0, s83, 98304\n"
"s_add_u32 s71, s72, 98304\n"
"buffer_load_dwordx4 v12, s[40:43], s71 offen lds\n"
"s_add_u32 m0, s83, 99328\n"
"s_add_u32 s70, s72, 106496\n"
"buffer_load_dwordx4 v12, s[40:43], s70 offen lds\n"
"s_add_u32 m0, s83, 100352\n"
"s_add_u32 s71, s72, 114688\n"
"buffer_load_dwordx4 v12, s[40:43], s71 offen lds\n"
"s_add_u32 m0, s83, 101376\n"
"s_add_u32 s70, s72, 122880\n"
"buffer_load_dwordx4 v12, s[40:43], s70 offen lds\n"
"s_waitcnt vmcnt(12)\n"
"ds_read_b64_tr_b16 v[144:145], v1 offset:49152\n"
"ds_read_b64_tr_b16 v[146:147], v1 offset:51200\n"
"ds_read_b64_tr_b16 v[148:149], v2 offset:49152\n"
"ds_read_b64_tr_b16 v[150:151], v2 offset:51200\n"
"ds_read_b64_tr_b16 v[152:153], v1 offset:49664\n"
"ds_read_b64_tr_b16 v[154:155], v1 offset:51712\n"
"ds_read_b64_tr_b16 v[156:157], v2 offset:49664\n"
"ds_read_b64_tr_b16 v[158:159], v2 offset:51712\n"
"s_add_u32 s62, s69, 6\n"
"s_and_b32 s62, s62, 3\n"
"s_add_u32 s62, s62, s35\n"
"s_xor_b32 s62, s62, 4\n"
"s_and_b32 s62, s62, 7\n"
"s_lshl_b32 s62, s62, 17\n"
"s_add_u32 s72, s81, s62\n"
"s_add_u32 m0, s83, 16384\n"
"s_add_u32 s71, s72, 0\n"
"buffer_load_dwordx4 v12, s[40:43], s71 offen lds\n"
"s_add_u32 m0, s83, 17408\n"
"s_add_u32 s70, s72, 8192\n"
"buffer_load_dwordx4 v12, s[40:43], s70 offen lds\n"
"s_add_u32 m0, s83, 18432\n"
"s_add_u32 s71, s72, 16384\n"
"buffer_load_dwordx4 v12, s[40:43], s71 offen lds\n"
"s_add_u32 m0, s83, 19456\n"
"s_add_u32 s70, s72, 24576\n"
"buffer_load_dwordx4 v12, s[40:43], s70 offen lds\n"
"s_waitcnt vmcnt(12)\n"
"ds_read_b64_tr_b16 v[160:161], v5 offset:49152\n"
"ds_read_b64_tr_b16 v[162:163], v5 offset:51200\n"
"ds_read_b64_tr_b16 v[164:165], v6 offset:49152\n"
"ds_read_b64_tr_b16 v[166:167], v6 offset:51200\n"
"ds_read_b64_tr_b16 v[168:169], v5 offset:49664\n"
"ds_read_b64_tr_b16 v[170:171], v5 offset:51712\n"
"ds_read_b64_tr_b16 v[172:173], v6 offset:49664\n"
"ds_read_b64_tr_b16 v[174:175], v6 offset:51712\n"
"s_add_u32 m0, s83, 81920\n"
"s_add_u32 s71, s72, 32768\n"
"buffer_load_dwordx4 v12, s[40:43], s71 offen lds\n"
"s_add_u32 m0, s83, 82944\n"
"s_add_u32 s70, s72, 40960\n"
"buffer_load_dwordx4 v12, s[40:43], s70 offen lds\n"
"s_add_u32 m0, s83, 83968\n"
"s_add_u32 s71, s72, 49152\n"
"buffer_load_dwordx4 v12, s[40:43], s71 offen lds\n"
"s_add_u32 m0, s83, 84992\n"
"s_add_u32 s70, s72, 57344\n"
"buffer_load_dwordx4 v12, s[40:43], s70 offen lds\n"
"s_add_u32 m0, s83, 0\n"
"s_add_u32 s71, s72, 65536\n"
"buffer_load_dwordx4 v12, s[40:43], s71 offen lds\n"
"s_add_u32 m0, s83, 1024\n"
"s_add_u32 s70, s72, 73728\n"
"buffer_load_dwordx4 v12, s[40:43], s70 offen lds\n"
"s_add_u32 m0, s83, 2048\n"
"s_add_u32 s71, s72, 81920\n"
"buffer_load_dwordx4 v12, s[40:43], s71 offen lds\n"
"s_add_u32 m0, s83, 3072\n"
"s_add_u32 s70, s72, 90112\n"
"buffer_load_dwordx4 v12, s[40:43], s70 offen lds\n"
"s_add_u32 m0, s83, 65536\n"
"s_add_u32 s71, s72, 98304\n"
"buffer_load_dwordx4 v12, s[40:43], s71 offen lds\n"
"s_add_u32 m0, s83, 66560\n"
"s_add_u32 s70, s72, 106496\n"
"buffer_load_dwordx4 v12, s[40:43], s70 offen lds\n"
"s_add_u32 m0, s83, 67584\n"
"s_add_u32 s71, s72, 114688\n"
"buffer_load_dwordx4 v12, s[40:43], s71 offen lds\n"
"s_add_u32 m0, s83, 68608\n"
"s_add_u32 s70, s72, 122880\n"
"buffer_load_dwordx4 v12, s[40:43], s70 offen lds\n"
"buffer_load_dword v16, v9, s[48:51], 0 offen sc1\n"
"s_mov_b32 s75, 0\n"
"v_lshlrev_b32 v18, 2, v0\n"
"s_add_u32 s62, s35, 0\n"
"s_and_b32 s62, s62, 7\n"
"s_lshl_b32 s62, s62, 12\n"
"s_add_u32 s84, s64, s62\n"
"s_add_u32 s85, s65, s62\n"
"s_add_u32 s86, s66, s62\n"
"s_add_u32 s87, s67, s62\n"
"s_lshl_b32 s71, s55, 7\n"
"s_add_u32 s71, s71, 32768\n"
"s_waitcnt vmcnt(0)\n"
"v_cmp_ne_u32 vcc, s73, v16\n"
"s_nop 1\n"
"s_cbranch_vccnz L_wait_%=\n"
"L_go_%=:\n"
"buffer_load_dwordx4 v[176:179], v3, s[36:39], s84 offen offset:0 sc1\n"
"buffer_load_dwordx4 v[192:195], v3, s[36:39], s84 offen offset:1024 sc1\n"
"buffer_load_dwordx4 v[180:183], v4, s[36:39], s85 offen offset:0 sc1\n"
"buffer_load_dwordx4 v[196:199], v4, s[36:39], s85 offen offset:1024 sc1\n"
"buffer_load_dwordx4 v[184:187], v7, s[36:39], s86 offen offset:0 sc1\n"
"buffer_load_dwordx4 v[200:203], v7, s[36:39], s86 offen offset:1024 sc1\n"
"buffer_load_dwordx4 v[188:191], v11, s[36:39], s87 offen offset:0 sc1\n"
"buffer_load_dwordx4 v[204:207], v11, s[36:39], s87 offen offset:1024 sc1\n"
"buffer_load_dwordx4 v[208:211], v3, s[36:39], s84 offen offset:2048 sc1\n"
"buffer_load_dwordx4 v[224:227], v3, s[36:39], s84 offen offset:3072 sc1\n"
"buffer_load_dwordx4 v[212:215], v4, s[36:39], s85 offen offset:2048 sc1\n"
"buffer_load_dwordx4 v[228:231], v4, s[36:39], s85 offen offset:3072 sc1\n"
"buffer_load_dword v9, v18, s[48:51], s71 offen sc1\n"
"s_waitcnt vmcnt(12)\n"
"v_mfma_f32_16x16x32_bf16 v[16:19], a[128:131], v[176:179], 0\n"
"s_waitcnt vmcnt(11)\n"
"v_mfma_f32_16x16x32_bf16 v[16:19], a[144:147], v[192:195], v[16:19]\n"
"buffer_load_dwordx4 v[216:219], v7, s[36:39], s86 offen offset:2048 sc1\n"
"v_mfma_f32_16x16x32_bf16 v[20:23], a[132:135], v[176:179], 0\n"
"v_mfma_f32_16x16x32_bf16 v[20:23], a[148:151], v[192:195], v[20:23]\n"
"s_waitcnt lgkmcnt(15)\n"
"v_mfma_f32_16x16x32_bf16 v[24:27], a[136:139], v[176:179], 0\n"
"s_add_u32 s63, s69, 7\n"
"s_and_b32 s63, s63, 3\n"
"s_add_u32 s63, s63, s35\n"
"s_xor_b32 s63, s63, 4\n"
"s_and_b32 s63, s63, 7\n"
"s_lshl_b32 s63, s63, 17\n"
"s_add_u32 s72, s81, s63\n"
"v_mfma_f32_16x16x32_bf16 v[24:27], a[152:155], v[192:195], v[24:27]\n"
"s_add_u32 s76, s72, 0\n"
"buffer_load_dwordx4 v[232:235], v7, s[36:39], s86 offen offset:3072 sc1\n"
"v_mfma_f32_16x16x32_bf16 v[28:31], a[140:143], v[176:179], 0\n"
"s_add_u32 m0, s82, 0\n"
"v_mfma_f32_16x16x32_bf16 v[28:31], a[156:159], v[192:195], v[28:31]\n"
"s_waitcnt vmcnt(12)\n"
"buffer_load_dwordx4 v12, s[40:43], s76 offen lds\n"
"v_mfma_f32_16x16x32_bf16 v[32:35], a[128:131], v[180:183], 0\n"
"s_waitcnt vmcnt(12)\n"
"v_mfma_f32_16x16x32_bf16 v[32:35], a[144:147], v[196:199], v[32:35]\n"
"buffer_load_dwordx4 v[220:223], v11, s[36:39], s87 offen offset:2048 sc1\n"
"v_mfma_f32_16x16x32_bf16 v[36:39], a[132:135], v[180:183], 0\n"
"v_mfma_f32_16x16x32_bf16 v[36:39], a[148:151], v[196:199], v[36:39]\n"
"v_mfma_f32_16x16x32_bf16 v[40:43], a[136:139], v[180:183], 0\n"
"v_mfma_f32_16x16x32_bf16 v[40:43], a[152:155], v[196:199], v[40:43]\n"
"buffer_load_dwordx4 v[236:239], v11, s[36:39], s87 offen offset:3072 sc1\n"
"v_mfma_f32_16x16x32_bf16 v[44:47], a[140:143], v[180:183], 0\n"
"s_add_u32 s62, s35, 1\n"
"s_and_b32 s62, s62, 7\n"
"v_mfma_f32_16x16x32_bf16 v[44:47], a[156:159], v[196:199], v[44:47]\n"
"s_waitcnt vmcnt(13)\n"
"s_lshl_b32 s62, s62, 12\n"
"s_add_u32 s84, s64, s62\n"
"v_mfma_f32_16x16x32_bf16 v[48:51], a[128:131], v[184:187], 0\n"
"s_waitcnt vmcnt(12)\n"
"s_add_u32 s85, s65, s62\n"
"s_add_u32 s86, s66, s62\n"
"v_mfma_f32_16x16x32_bf16 v[48:51], a[144:147], v[200:203], v[48:51]\n"
"s_add_u32 s87, s67, s62\n"
"buffer_load_dwordx4 v[240:243], v3, s[36:39], s84 offen offset:0 sc1\n"
"v_mfma_f32_16x16x32_bf16 v[52:55], a[132:135], v[184:187], 0\n"
"v_mfma_f32_16x16x32_bf16 v[52:55], a[148:151], v[200:203], v[52:55]\n"
"v_mfma_f32_16x16x32_bf16 v[56:59], a[136:139], v[184:187], 0\n"
"v_mfma_f32_16x16x32_bf16 v[56:59], a[152:155], v[200:203], v[56:59]\n"
"s_add_u32 s77, s72, 8192\n"
"buffer_load_dwordx4 v[176:179], v3, s[36:39], s84 offen offset:1024 sc1\n"
"v_mfma_f32_16x16x32_bf16 v[60:63], a[140:143], v[184:187], 0\n"
"s_add_u32 m0, s82, 1024\n"
"v_mfma_f32_16x16x32_bf16 v[60:63], a[156:159], v[200:203], v[60:63]\n"
"s_waitcnt vmcnt(13)\n"
"buffer_load_dwordx4 v12, s[40:43], s77 offen lds\n"
"v_mfma_f32_16x16x32_bf16 v[64:67], a[128:131], v[188:191], 0\n"
"s_waitcnt vmcnt(13)\n"
"v_mfma_f32_16x16x32_bf16 v[64:67], a[144:147], v[204:207], v[64:67]\n"
"buffer_load_dwordx4 v[244:247], v4, s[36:39], s85 offen offset:0 sc1\n"
"v_mfma_f32_16x16x32_bf16 v[68:71], a[132:135], v[188:191], 0\n"
"v_mfma_f32_16x16x32_bf16 v[68:71], a[148:151], v[204:207], v[68:71]\n"
"v_mfma_f32_16x16x32_bf16 v[72:75], a[136:139], v[188:191], 0\n"
"v_mfma_f32_16x16x32_bf16 v[72:75], a[152:155], v[204:207], v[72:75]\n"
"buffer_load_dwordx4 v[180:183], v4, s[36:39], s85 offen offset:1024 sc1\n"
"v_mfma_f32_16x16x32_bf16 v[76:79], a[140:143], v[188:191], 0\n"
"v_mfma_f32_16x16x32_bf16 v[76:79], a[156:159], v[204:207], v[76:79]\n"
"s_waitcnt vmcnt(14)\n"
"v_mfma_f32_16x16x32_bf16 v[16:19], a[160:163], v[208:211], v[16:19]\n"
"s_waitcnt vmcnt(13)\n"
"v_mfma_f32_16x16x32_bf16 v[16:19], a[176:179], v[224:227], v[16:19]\n"
"buffer_load_dwordx4 v[248:251], v7, s[36:39], s86 offen offset:0 sc1\n"
"v_mfma_f32_16x16x32_bf16 v[20:23], a[164:167], v[208:211], v[20:23]\n"
"v_mfma_f32_16x16x32_bf16 v[20:23], a[180:183], v[224:227], v[20:23]\n"
"v_mfma_f32_16x16x32_bf16 v[24:27], a[168:171], v[208:211], v[24:27]\n"
"v_mfma_f32_16x16x32_bf16 v[24:27], a[184:187], v[224:227], v[24:27]\n"
"s_add_u32 s76, s72, 16384\n"
"buffer_load_dwordx4 v[184:187], v7, s[36:39], s86 offen offset:1024 sc1\n"
"v_mfma_f32_16x16x32_bf16 v[28:31], a[172:175], v[208:211], v[28:31]\n"
"s_add_u32 m0, s82, 2048\n"
"v_mfma_f32_16x16x32_bf16 v[28:31], a[188:191], v[224:227], v[28:31]\n"
"s_waitcnt vmcnt(14)\n"
"buffer_load_dwordx4 v12, s[40:43], s76 offen lds\n"
"v_mfma_f32_16x16x32_bf16 v[32:35], a[160:163], v[212:215], v[32:35]\n"
"s_waitcnt vmcnt(14)\n"
"v_mfma_f32_16x16x32_bf16 v[32:35], a[176:179], v[228:231], v[32:35]\n"
"buffer_load_dwordx4 v[252:255], v11, s[36:39], s87 offen offset:0 sc1\n"
"v_mfma_f32_16x16x32_bf16 v[36:39], a[164:167], v[212:215], v[36:39]\n"
"v_mfma_f32_16x16x32_bf16 v[36:39], a[180:183], v[228:231], v[36:39]\n"
"v_mfma_f32_16x16x32_bf16 v[40:43], a[168:171], v[212:215], v[40:43]\n"
"v_mfma_f32_16x16x32_bf16 v[40:43], a[184:187], v[228:231], v[40:43]\n"
"buffer_load_dwordx4 v[188:191], v11, s[36:39], s87 offen offset:1024 sc1\n"
"v_mfma_f32_16x16x32_bf16 v[44:47], a[172:175], v[212:215], v[44:47]\n"
"v_mfma_f32_16x16x32_bf16 v[44:47], a[188:191], v[228:231], v[44:47]\n"
"s_waitcnt vmcnt(14)\n"
"v_cmp_ne_u32 vcc, v9, v15\n"
"s_nop 1\n"
"s_cmp_lg_u32 vcc_lo, 0\n"
"s_cbranch_scc1 L_fix_%=\n"
"L_fixed_%=:\n"
"v_mfma_f32_16x16x32_bf16 v[48:51], a[160:163], v[216:219], v[48:51]\n"
"s_waitcnt vmcnt(13)\n"
"v_mfma_f32_16x16x32_bf16 v[48:51], a[176:179], v[232:235], v[48:51]\n"
"buffer_load_dwordx4 v[192:195], v3, s[36:39], s84 offen offset:2048 sc1\n"
"v_mfma_f32_16x16x32_bf16 v[52:55], a[164:167], v[216:219], v[52:55]\n"
"v_mfma_f32_16x16x32_bf16 v[52:55], a[180:183], v[232:235], v[52:55]\n"
"v_mfma_f32_16x16x32_bf16 v[56:59], a[168:171], v[216:219], v[56:59]\n"
"v_mfma_f32_16x16x32_bf16 v[56:59], a[184:187], v[232:235], v[56:59]\n"
"s_add_u32 s77, s72, 24576\n"
"buffer_load_dwordx4 v[208:211], v3, s[36:39], s84 offen offset:3072 sc1\n"
"v_mfma_f32_16x16x32_bf16 v[60:63], a[172:175], v[216:219], v[60:63]\n"
"s_add_u32 m0, s82, 3072\n"
"v_mfma_f32_16x16x32_bf16 v[60:63], a[188:191], v[232:235], v[60:63]\n"
"s_waitcnt vmcnt(13)\n"
"buffer_load_dwordx4 v12, s[40:43], s77 offen lds\n"
"v_mfma_f32_16x16x32_bf16 v[64:67], a[160:163], v[220:223], v[64:67]\n"
"s_waitcnt vmcnt(13)\n"
"v_mfma_f32_16x16x32_bf16 v[64:67], a[176:179], v[236:239], v[64:67]\n"
"buffer_load_dwordx4 v[196:199], v4, s[36:39], s85 offen offset:2048 sc1\n"
"v_mfma_f32_16x16x32_bf16 v[68:71], a[164:167], v[220:223], v[68:71]\n"
"v_mfma_f32_16x16x32_bf16 v[68:71], a[180:183], v[236:239], v[68:71]\n"
"v_mfma_f32_16x16x32_bf16 v[72:75], a[168:171], v[220:223], v[72:75]\n"
"v_mfma_f32_16x16x32_bf16 v[72:75], a[184:187], v[236:239], v[72:75]\n"
"buffer_load_dwordx4 v[212:215], v4, s[36:39], s85 offen offset:3072 sc1\n"
"v_mfma_f32_16x16x32_bf16 v[76:79], a[172:175], v[220:223], v[76:79]\n"
"v_mfma_f32_16x16x32_bf16 v[76:79], a[188:191], v[236:239], v[76:79]\n"
"s_waitcnt vmcnt(14)\n"
"v_mfma_f32_16x16x32_bf16 v[16:19], a[192:195], v[240:243], v[16:19]\n"
"s_waitcnt vmcnt(13)\n"
"v_mfma_f32_16x16x32_bf16 v[16:19], a[208:211], v[176:179], v[16:19]\n"
"buffer_load_dwordx4 v[200:203], v7, s[36:39], s86 offen offset:2048 sc1\n"
"v_mfma_f32_16x16x32_bf16 v[20:23], a[196:199], v[240:243], v[20:23]\n"
"v_mfma_f32_16x16x32_bf16 v[20:23], a[212:215], v[176:179], v[20:23]\n"
"v_mfma_f32_16x16x32_bf16 v[24:27], a[200:203], v[240:243], v[24:27]\n"
"v_mfma_f32_16x16x32_bf16 v[24:27], a[216:219], v[176:179], v[24:27]\n"
"s_add_u32 s76, s72, 32768\n"
"buffer_load_dwordx4 v[216:219], v7, s[36:39], s86 offen offset:3072 sc1\n"
"v_mfma_f32_16x16x32_bf16 v[28:31], a[204:207], v[240:243], v[28:31]\n"
"s_add_u32 m0, s82, 4096\n"
"v_mfma_f32_16x16x32_bf16 v[28:31], a[220:223], v[176:179], v[28:31]\n"
"s_waitcnt vmcnt(13)\n"
"buffer_load_dwordx4 v12, s[40:43], s76 offen lds\n"
"v_mfma_f32_16x16x32_bf16 v[32:35], a[192:195], v[244:247], v[32:35]\n"
"s_waitcnt vmcnt(13)\n"
"v_mfma_f32_16x16x32_bf16 v[32:35], a[208:211], v[180:183], v[32:35]\n"
"buffer_load_dwordx4 v[204:207], v11, s[36:39], s87 offen offset:2048 sc1\n"
"v_mfma_f32_16x16x32_bf16 v[36:39], a[196:199], v[244:247], v[36:39]\n"
"v_mfma_f32_16x16x32_bf16 v[36:39], a[212:215], v[180:183], v[36:39]\n"
"v_mfma_f32_16x16x32_bf16 v[40:43], a[200:203], v[244:247], v[40:43]\n"
"v_mfma_f32_16x16x32_bf16 v[40:43], a[216:219], v[180:183], v[40:43]\n"
"buffer_load_dwordx4 v[220:223], v11, s[36:39], s87 offen offset:3072 sc1\n"
"v_mfma_f32_16x16x32_bf16 v[44:47], a[204:207], v[244:247], v[44:47]\n"
"s_add_u32 s62, s35, 2\n"
"s_and_b32 s62, s62, 7\n"
"v_mfma_f32_16x16x32_bf16 v[44:47], a[220:223], v[180:183], v[44:47]\n"
"s_waitcnt vmcnt(14)\n"
"s_lshl_b32 s62, s62, 12\n"
"s_add_u32 s84, s64, s62\n"
"v_mfma_f32_16x16x32_bf16 v[48:51], a[192:195], v[248:251], v[48:51]\n"
"s_waitcnt vmcnt(13)\n"
"s_add_u32 s85, s65, s62\n"
"s_add_u32 s86, s66, s62\n"
"v_mfma_f32_16x16x32_bf16 v[48:51], a[208:211], v[184:187], v[48:51]\n"
"s_add_u32 s87, s67, s62\n"
"buffer_load_dwordx4 v[224:227], v3, s[36:39], s84 offen offset:0 sc1\n"
"v_mfma_f32_16x16x32_bf16 v[52:55], a[196:199], v[248:251], v[52:55]\n"
"v_mfma_f32_16x16x32_bf16 v[52:55], a[212:215], v[184:187], v[52:55]\n"
"v_mfma_f32_16x16x32_bf16 v[56:59], a[200:203], v[248:251], v[56:59]\n"
"v_mfma_f32_16x16x32_bf16 v[56:59], a[216:219], v[184:187], v[56:59]\n"
"s_add_u32 s77, s72, 40960\n"
"buffer_load_dwordx4 v[240:243], v3, s[36:39], s84 offen offset:1024 sc1\n"
"v_mfma_f32_16x16x32_bf16 v[60:63], a[204:207], v[248:251], v[60:63]\n"
"s_add_u32 m0, s82, 5120\n"
"v_mfma_f32_16x16x32_bf16 v[60:63], a[220:223], v[184:187], v[60:63]\n"
"s_waitcnt vmcnt(13)\n"
"buffer_load_dwordx4 v12, s[40:43], s77 offen lds\n"
"v_mfma_f32_16x16x32_bf16 v[64:67], a[192:195], v[252:255], v[64:67]\n"
"s_waitcnt vmcnt(13)\n"
"v_mfma_f32_16x16x32_bf16 v[64:67], a[208:211], v[188:191], v[64:67]\n"
"buffer_load_dwordx4 v[228:231], v4, s[36:39], s85 offen offset:0 sc1\n"
"v_mfma_f32_16x16x32_bf16 v[68:71], a[196:199], v[252:255], v[68:71]\n"
"v_mfma_f32_16x16x32_bf16 v[68:71], a[212:215], v[188:191], v[68:71]\n"
"v_mfma_f32_16x16x32_bf16 v[72:75], a[200:203], v[252:255], v[72:75]\n"
"v_mfma_f32_16x16x32_bf16 v[72:75], a[216:219], v[188:191], v[72:75]\n"
"buffer_load_dwordx4 v[244:247], v4, s[36:39], s85 offen offset:1024 sc1\n"
"v_mfma_f32_16x16x32_bf16 v[76:79], a[204:207], v[252:255], v[76:79]\n"
"v_mfma_f32_16x16x32_bf16 v[76:79], a[220:223], v[188:191], v[76:79]\n"
"s_waitcnt vmcnt(14)\n"
"v_mfma_f32_16x16x32_bf16 v[16:19], a[224:227], v[192:195], v[16:19]\n"
"s_waitcnt vmcnt(13)\n"
"v_mfma_f32_16x16x32_bf16 v[16:19], a[240:243], v[208:211], v[16:19]\n"
"buffer_load_dwordx4 v[232:235], v7, s[36:39], s86 offen offset:0 sc1\n"
"v_mfma_f32_16x16x32_bf16 v[20:23], a[228:231], v[192:195], v[20:23]\n"
"v_mfma_f32_16x16x32_bf16 v[20:23], a[244:247], v[208:211], v[20:23]\n"
"v_mfma_f32_16x16x32_bf16 v[24:27], a[232:235], v[192:195], v[24:27]\n"
"v_mfma_f32_16x16x32_bf16 v[24:27], a[248:251], v[208:211], v[24:27]\n"
"s_add_u32 s76, s72, 49152\n"
"buffer_load_dwordx4 v[248:251], v7, s[36:39], s86 offen offset:1024 sc1\n"
"v_mfma_f32_16x16x32_bf16 v[28:31], a[236:239], v[192:195], v[28:31]\n"
"s_add_u32 m0, s82, 6144\n"
"v_mfma_f32_16x16x32_bf16 v[28:31], a[252:255], v[208:211], v[28:31]\n"
"s_waitcnt vmcnt(13)\n"
"buffer_load_dwordx4 v12, s[40:43], s76 offen lds\n"
"v_mfma_f32_16x16x32_bf16 v[32:35], a[224:227], v[196:199], v[32:35]\n"
"s_waitcnt vmcnt(13)\n"
"v_mfma_f32_16x16x32_bf16 v[32:35], a[240:243], v[212:215], v[32:35]\n"
"buffer_load_dwordx4 v[236:239], v11, s[36:39], s87 offen offset:0 sc1\n"
"v_mfma_f32_16x16x32_bf16 v[36:39], a[228:231], v[196:199], v[36:39]\n"
"v_mfma_f32_16x16x32_bf16 v[36:39], a[244:247], v[212:215], v[36:39]\n"
"v_mfma_f32_16x16x32_bf16 v[40:43], a[232:235], v[196:199], v[40:43]\n"
"v_mfma_f32_16x16x32_bf16 v[40:43], a[248:251], v[212:215], v[40:43]\n"
"buffer_load_dwordx4 v[252:255], v11, s[36:39], s87 offen offset:1024 sc1\n"
"v_mfma_f32_16x16x32_bf16 v[44:47], a[236:239], v[196:199], v[44:47]\n"
"v_mfma_f32_16x16x32_bf16 v[44:47], a[252:255], v[212:215], v[44:47]\n"
"s_waitcnt vmcnt(14)\n"
"v_mfma_f32_16x16x32_bf16 v[48:51], a[224:227], v[200:203], v[48:51]\n"
"s_waitcnt vmcnt(13)\n"
"v_mfma_f32_16x16x32_bf16 v[48:51], a[240:243], v[216:219], v[48:51]\n"
"buffer_load_dwordx4 v[176:179], v3, s[36:39], s84 offen offset:2048 sc1\n"
"v_mfma_f32_16x16x32_bf16 v[52:55], a[228:231], v[200:203], v[52:55]\n"
"v_mfma_f32_16x16x32_bf16 v[52:55], a[244:247], v[216:219], v[52:55]\n"
"v_mfma_f32_16x16x32_bf16 v[56:59], a[232:235], v[200:203], v[56:59]\n"
"v_mfma_f32_16x16x32_bf16 v[56:59], a[248:251], v[216:219], v[56:59]\n"
"s_add_u32 s77, s72, 57344\n"
"buffer_load_dwordx4 v[192:195], v3, s[36:39], s84 offen offset:3072 sc1\n"
"v_mfma_f32_16x16x32_bf16 v[60:63], a[236:239], v[200:203], v[60:63]\n"
"s_add_u32 m0, s82, 7168\n"
"v_mfma_f32_16x16x32_bf16 v[60:63], a[252:255], v[216:219], v[60:63]\n"
"s_waitcnt vmcnt(13)\n"
"buffer_load_dwordx4 v12, s[40:43], s77 offen lds\n"
"v_mfma_f32_16x16x32_bf16 v[64:67], a[224:227], v[204:207], v[64:67]\n"
"s_waitcnt vmcnt(13)\n"
"v_mfma_f32_16x16x32_bf16 v[64:67], a[240:243], v[220:223], v[64:67]\n"
"buffer_load_dwordx4 v[180:183], v4, s[36:39], s85 offen offset:2048 sc1\n"
"v_mfma_f32_16x16x32_bf16 v[68:71], a[228:231], v[204:207], v[68:71]\n"
"v_mfma_f32_16x16x32_bf16 v[68:71], a[244:247], v[220:223], v[68:71]\n"
"v_mfma_f32_16x16x32_bf16 v[72:75], a[232:235], v[204:207], v[72:75]\n"
"v_mfma_f32_16x16x32_bf16 v[72:75], a[248:251], v[220:223], v[72:75]\n"
"buffer_load_dwordx4 v[196:199], v4, s[36:39], s85 offen offset:3072 sc1\n"
"v_mfma_f32_16x16x32_bf16 v[76:79], a[236:239], v[204:207], v[76:79]\n"
"v_mfma_f32_16x16x32_bf16 v[76:79], a[252:255], v[220:223], v[76:79]\n"
"s_waitcnt vmcnt(14)\n"
"v_mfma_f32_16x16x32_bf16 v[16:19], a[0:3], v[224:227], v[16:19]\n"
"s_waitcnt vmcnt(13)\n"
"v_mfma_f32_16x16x32_bf16 v[16:19], a[16:19], v[240:243], v[16:19]\n"
"buffer_load_dwordx4 v[184:187], v7, s[36:39], s86 offen offset:2048 sc1\n"
"v_mfma_f32_16x16x32_bf16 v[20:23], a[4:7], v[224:227], v[20:23]\n"
"v_mfma_f32_16x16x32_bf16 v[20:23], a[20:23], v[240:243], v[20:23]\n"
"s_waitcnt lgkmcnt(8)\n"
"v_mfma_f32_16x16x32_bf16 v[24:27], a[8:11], v[224:227], v[24:27]\n"
"v_mfma_f32_16x16x32_bf16 v[24:27], a[24:27], v[240:243], v[24:27]\n"
"s_add_u32 s76, s72, 65536\n"
"buffer_load_dwordx4 v[200:203], v7, s[36:39], s86 offen offset:3072 sc1\n"
"v_mfma_f32_16x16x32_bf16 v[28:31], a[12:15], v[224:227], v[28:31]\n"
"s_add_u32 m0, s83, 49152\n"
"v_mfma_f32_16x16x32_bf16 v[28:31], a[28:31], v[240:243], v[28:31]\n"
"s_waitcnt vmcnt(13)\n"
"buffer_load_dwordx4 v12, s[40:43], s76 offen lds\n"
"v_mfma_f32_16x16x32_bf16 v[32:35], a[0:3], v[228:231], v[32:35]\n"
"s_waitcnt vmcnt(13)\n"
"v_mfma_f32_16x16x32_bf16 v[32:35], a[16:19], v[244:247], v[32:35]\n"
"buffer_load_dwordx4 v[188:191], v11, s[36:39], s87 offen offset:2048 sc1\n"
"v_mfma_f32_16x16x32_bf16 v[36:39], a[4:7], v[228:231], v[36:39]\n"
"v_mfma_f32_16x16x32_bf16 v[36:39], a[20:23], v[244:247], v[36:39]\n"
"v_mfma_f32_16x16x32_bf16 v[40:43], a[8:11], v[228:231], v[40:43]\n"
"v_mfma_f32_16x16x32_bf16 v[40:43], a[24:27], v[244:247], v[40:43]\n"
"buffer_load_dwordx4 v[204:207], v11, s[36:39], s87 offen offset:3072 sc1\n"
"v_mfma_f32_16x16x32_bf16 v[44:47], a[12:15], v[228:231], v[44:47]\n"
"s_add_u32 s62, s35, 3\n"
"s_and_b32 s62, s62, 7\n"
"v_mfma_f32_16x16x32_bf16 v[44:47], a[28:31], v[244:247], v[44:47]\n"
"s_waitcnt vmcnt(14)\n"
"s_lshl_b32 s62, s62, 12\n"
"s_add_u32 s84, s64, s62\n"
"v_mfma_f32_16x16x32_bf16 v[48:51], a[0:3], v[232:235], v[48:51]\n"
"s_waitcnt vmcnt(13)\n"
"s_add_u32 s85, s65, s62\n"
"s_add_u32 s86, s66, s62\n"
"v_mfma_f32_16x16x32_bf16 v[48:51], a[16:19], v[248:251], v[48:51]\n"
"s_add_u32 s87, s67, s62\n"
"buffer_load_dwordx4 v[208:211], v3, s[36:39], s84 offen offset:0 sc1\n"
"v_mfma_f32_16x16x32_bf16 v[52:55], a[4:7], v[232:235], v[52:55]\n"
"v_mfma_f32_16x16x32_bf16 v[52:55], a[20:23], v[248:251], v[52:55]\n"
"v_mfma_f32_16x16x32_bf16 v[56:59], a[8:11], v[232:235], v[56:59]\n"
"v_mfma_f32_16x16x32_bf16 v[56:59], a[24:27], v[248:251], v[56:59]\n"
"s_add_u32 s77, s72, 73728\n"
"buffer_load_dwordx4 v[224:227], v3, s[36:39], s84 offen offset:1024 sc1\n"
"v_mfma_f32_16x16x32_bf16 v[60:63], a[12:15], v[232:235], v[60:63]\n"
"s_add_u32 m0, s83, 50176\n"
"v_mfma_f32_16x16x32_bf16 v[60:63], a[28:31], v[248:251], v[60:63]\n"
"s_waitcnt vmcnt(13)\n"
"buffer_load_dwordx4 v12, s[40:43], s77 offen lds\n"
"v_mfma_f32_16x16x32_bf16 v[64:67], a[0:3], v[236:239], v[64:67]\n"
"s_waitcnt vmcnt(13)\n"
"v_mfma_f32_16x16x32_bf16 v[64:67], a[16:19], v[252:255], v[64:67]\n"
"buffer_load_dwordx4 v[212:215], v4, s[36:39], s85 offen offset:0 sc1\n"
"v_mfma_f32_16x16x32_bf16 v[68:71], a[4:7], v[236:239], v[68:71]\n"
"v_mfma_f32_16x16x32_bf16 v[68:71], a[20:23], v[252:255], v[68:71]\n"
"v_mfma_f32_16x16x32_bf16 v[72:75], a[8:11], v[236:239], v[72:75]\n"
"v_mfma_f32_16x16x32_bf16 v[72:75], a[24:27], v[252:255], v[72:75]\n"
"buffer_load_dwordx4 v[228:231], v4, s[36:39], s85 offen offset:1024 sc1\n"
"v_mfma_f32_16x16x32_bf16 v[76:79], a[12:15], v[236:239], v[76:79]\n"
"v_mfma_f32_16x16x32_bf16 v[76:79], a[28:31], v[252:255], v[76:79]\n"
"s_waitcnt vmcnt(14)\n"
"v_mfma_f32_16x16x32_bf16 v[16:19], a[64:67], v[176:179], v[16:19]\n"
"s_waitcnt vmcnt(13)\n"
"v_mfma_f32_16x16x32_bf16 v[16:19], a[80:83], v[192:195], v[16:19]\n"
"buffer_load_dwordx4 v[216:219], v7, s[36:39], s86 offen offset:0 sc1\n"
"v_mfma_f32_16x16x32_bf16 v[20:23], a[68:71], v[176:179], v[20:23]\n"
"v_mfma_f32_16x16x32_bf16 v[20:23], a[84:87], v[192:195], v[20:23]\n"
"v_mfma_f32_16x16x32_bf16 v[24:27], a[72:75], v[176:179], v[24:27]\n"
"v_mfma_f32_16x16x32_bf16 v[24:27], a[88:91], v[192:195], v[24:27]\n"
"s_add_u32 s76, s72, 81920\n"
"buffer_load_dwordx4 v[232:235], v7, s[36:39], s86 offen offset:1024 sc1\n"
"v_mfma_f32_16x16x32_bf16 v[28:31], a[76:79], v[176:179], v[28:31]\n"
"s_add_u32 m0, s83, 51200\n"
"v_mfma_f32_16x16x32_bf16 v[28:31], a[92:95], v[192:195], v[28:31]\n"
"s_waitcnt vmcnt(13)\n"
"buffer_load_dwordx4 v12, s[40:43], s76 offen lds\n"
"v_mfma_f32_16x16x32_bf16 v[32:35], a[64:67], v[180:183], v[32:35]\n"
"s_waitcnt vmcnt(13)\n"
"v_mfma_f32_16x16x32_bf16 v[32:35], a[80:83], v[196:199], v[32:35]\n"
"buffer_load_dwordx4 v[220:223], v11, s[36:39], s87 offen offset:0 sc1\n"
"v_mfma_f32_16x16x32_bf16 v[36:39], a[68:71], v[180:183], v[36:39]\n"
"v_mfma_f32_16x16x32_bf16 v[36:39], a[84:87], v[196:199], v[36:39]\n"
"v_mfma_f32_16x16x32_bf16 v[40:43], a[72:75], v[180:183], v[40:43]\n"
"v_mfma_f32_16x16x32_bf16 v[40:43], a[88:91], v[196:199], v[40:43]\n"
"buffer_load_dwordx4 v[236:239], v11, s[36:39], s87 offen offset:1024 sc1\n"
"v_mfma_f32_16x16x32_bf16 v[44:47], a[76:79], v[180:183], v[44:47]\n"
"v_mfma_f32_16x16x32_bf16 v[44:47], a[92:95], v[196:199], v[44:47]\n"
"s_waitcnt vmcnt(14)\n"
"v_mfma_f32_16x16x32_bf16 v[48:51], a[64:67], v[184:187], v[48:51]\n"
"s_waitcnt vmcnt(13)\n"
"v_mfma_f32_16x16x32_bf16 v[48:51], a[80:83], v[200:203], v[48:51]\n"
"buffer_load_dwordx4 v[240:243], v3, s[36:39], s84 offen offset:2048 sc1\n"
"v_mfma_f32_16x16x32_bf16 v[52:55], a[68:71], v[184:187], v[52:55]\n"
"v_mfma_f32_16x16x32_bf16 v[52:55], a[84:87], v[200:203], v[52:55]\n"
"v_mfma_f32_16x16x32_bf16 v[56:59], a[72:75], v[184:187], v[56:59]\n"
"v_mfma_f32_16x16x32_bf16 v[56:59], a[88:91], v[200:203], v[56:59]\n"
"s_add_u32 s77, s72, 90112\n"
"buffer_load_dwordx4 v[176:179], v3, s[36:39], s84 offen offset:3072 sc1\n"
"v_mfma_f32_16x16x32_bf16 v[60:63], a[76:79], v[184:187], v[60:63]\n"
"s_add_u32 m0, s83, 52224\n"
"v_mfma_f32_16x16x32_bf16 v[60:63], a[92:95], v[200:203], v[60:63]\n"
"s_waitcnt vmcnt(13)\n"
"buffer_load_dwordx4 v12, s[40:43], s77 offen lds\n"
"v_mfma_f32_16x16x32_bf16 v[64:67], a[64:67], v[188:191], v[64:67]\n"
"s_waitcnt vmcnt(13)\n"
"v_mfma_f32_16x16x32_bf16 v[64:67], a[80:83], v[204:207], v[64:67]\n"
"buffer_load_dwordx4 v[244:247], v4, s[36:39], s85 offen offset:2048 sc1\n"
"v_mfma_f32_16x16x32_bf16 v[68:71], a[68:71], v[188:191], v[68:71]\n"
"v_mfma_f32_16x16x32_bf16 v[68:71], a[84:87], v[204:207], v[68:71]\n"
"v_mfma_f32_16x16x32_bf16 v[72:75], a[72:75], v[188:191], v[72:75]\n"
"v_mfma_f32_16x16x32_bf16 v[72:75], a[88:91], v[204:207], v[72:75]\n"
"buffer_load_dwordx4 v[180:183], v4, s[36:39], s85 offen offset:3072 sc1\n"
"v_mfma_f32_16x16x32_bf16 v[76:79], a[76:79], v[188:191], v[76:79]\n"
"v_mfma_f32_16x16x32_bf16 v[76:79], a[92:95], v[204:207], v[76:79]\n"
"s_waitcnt vmcnt(14)\n"
"v_mfma_f32_16x16x32_bf16 v[16:19], a[32:35], v[208:211], v[16:19]\n"
"s_waitcnt vmcnt(13)\n"
"v_mfma_f32_16x16x32_bf16 v[16:19], a[48:51], v[224:227], v[16:19]\n"
"buffer_load_dwordx4 v[248:251], v7, s[36:39], s86 offen offset:2048 sc1\n"
"v_mfma_f32_16x16x32_bf16 v[20:23], a[36:39], v[208:211], v[20:23]\n"
"v_mfma_f32_16x16x32_bf16 v[20:23], a[52:55], v[224:227], v[20:23]\n"
"s_waitcnt lgkmcnt(0)\n"
"v_mfma_f32_16x16x32_bf16 v[24:27], a[40:43], v[208:211], v[24:27]\n"
"v_mfma_f32_16x16x32_bf16 v[24:27], a[56:59], v[224:227], v[24:27]\n"
"s_add_u32 s76, s72, 98304\n"
"buffer_load_dwordx4 v[184:187], v7, s[36:39], s86 offen offset:3072 sc1\n"
"v_mfma_f32_16x16x32_bf16 v[28:31], a[44:47], v[208:211], v[28:31]\n"
"s_add_u32 m0, s83, 114688\n"
"v_mfma_f32_16x16x32_bf16 v[28:31], a[60:63], v[224:227], v[28:31]\n"
"s_waitcnt vmcnt(13)\n"
"buffer_load_dwordx4 v12, s[40:43], s76 offen lds\n"
"v_mfma_f32_16x16x32_bf16 v[32:35], a[32:35], v[212:215], v[32:35]\n"
"s_waitcnt vmcnt(13)\n"
"v_mfma_f32_16x16x32_bf16 v[32:35], a[48:51], v[228:231], v[32:35]\n"
"buffer_load_dwordx4 v[252:255], v11, s[36:39], s87 offen offset:2048 sc1\n"
"v_mfma_f32_16x16x32_bf16 v[36:39], a[36:39], v[212:215], v[36:39]\n"
"v_mfma_f32_16x16x32_bf16 v[36:39], a[52:55], v[228:231], v[36:39]\n"
"v_mfma_f32_16x16x32_bf16 v[40:43], a[40:43], v[212:215], v[40:43]\n"
"v_mfma_f32_16x16x32_bf16 v[40:43], a[56:59], v[228:231], v[40:43]\n"
"buffer_load_dwordx4 v[188:191], v11, s[36:39], s87 offen offset:3072 sc1\n"
"v_mfma_f32_16x16x32_bf16 v[44:47], a[44:47], v[212:215], v[44:47]\n"
"s_add_u32 s62, s69, 4\n"
"s_and_b32 s62, s62, 3\n"
"s_add_u32 s62, s62, s35\n"
"v_mfma_f32_16x16x32_bf16 v[44:47], a[60:63], v[228:231], v[44:47]\n"
"s_waitcnt vmcnt(14)\n"
"s_xor_b32 s62, s62, 4\n"
"s_and_b32 s62, s62, 7\n"
"v_mfma_f32_16x16x32_bf16 v[48:51], a[32:35], v[216:219], v[48:51]\n"
"s_waitcnt vmcnt(13)\n"
"s_lshl_b32 s62, s62, 12\n"
"s_add_u32 s84, s64, s62\n"
"s_add_u32 s85, s65, s62\n"
"v_mfma_f32_16x16x32_bf16 v[48:51], a[48:51], v[232:235], v[48:51]\n"
"s_add_u32 s86, s66, s62\n"
"s_add_u32 s87, s67, s62\n"
"buffer_load_dwordx4 v[192:195], v3, s[36:39], s84 offen offset:0 sc1\n"
"v_mfma_f32_16x16x32_bf16 v[52:55], a[36:39], v[216:219], v[52:55]\n"
"v_mfma_f32_16x16x32_bf16 v[52:55], a[52:55], v[232:235], v[52:55]\n"
"v_mfma_f32_16x16x32_bf16 v[56:59], a[40:43], v[216:219], v[56:59]\n"
"v_mfma_f32_16x16x32_bf16 v[56:59], a[56:59], v[232:235], v[56:59]\n"
"s_add_u32 s77, s72, 106496\n"
"buffer_load_dwordx4 v[208:211], v3, s[36:39], s84 offen offset:1024 sc1\n"
"v_mfma_f32_16x16x32_bf16 v[60:63], a[44:47], v[216:219], v[60:63]\n"
"s_add_u32 m0, s83, 115712\n"
"v_mfma_f32_16x16x32_bf16 v[60:63], a[60:63], v[232:235], v[60:63]\n"
"s_waitcnt vmcnt(13)\n"
"buffer_load_dwordx4 v12, s[40:43], s77 offen lds\n"
"v_mfma_f32_16x16x32_bf16 v[64:67], a[32:35], v[220:223], v[64:67]\n"
"s_waitcnt vmcnt(13)\n"
"v_mfma_f32_16x16x32_bf16 v[64:67], a[48:51], v[236:239], v[64:67]\n"
"buffer_load_dwordx4 v[196:199], v4, s[36:39], s85 offen offset:0 sc1\n"
"v_mfma_f32_16x16x32_bf16 v[68:71], a[36:39], v[220:223], v[68:71]\n"
"v_mfma_f32_16x16x32_bf16 v[68:71], a[52:55], v[236:239], v[68:71]\n"
"v_mfma_f32_16x16x32_bf16 v[72:75], a[40:43], v[220:223], v[72:75]\n"
"v_mfma_f32_16x16x32_bf16 v[72:75], a[56:59], v[236:239], v[72:75]\n"
"buffer_load_dwordx4 v[212:215], v4, s[36:39], s85 offen offset:1024 sc1\n"
"v_mfma_f32_16x16x32_bf16 v[76:79], a[44:47], v[220:223], v[76:79]\n"
"v_mfma_f32_16x16x32_bf16 v[76:79], a[60:63], v[236:239], v[76:79]\n"
"s_waitcnt vmcnt(14)\n"
"v_mfma_f32_16x16x32_bf16 v[16:19], a[96:99], v[240:243], v[16:19]\n"
"s_waitcnt vmcnt(13)\n"
"v_mfma_f32_16x16x32_bf16 v[16:19], a[112:115], v[176:179], v[16:19]\n"
"buffer_load_dwordx4 v[200:203], v7, s[36:39], s86 offen offset:0 sc1\n"
"v_mfma_f32_16x16x32_bf16 v[20:23], a[100:103], v[240:243], v[20:23]\n"
"v_mfma_f32_16x16x32_bf16 v[20:23], a[116:119], v[176:179], v[20:23]\n"
"v_mfma_f32_16x16x32_bf16 v[24:27], a[104:107], v[240:243], v[24:27]\n"
"v_mfma_f32_16x16x32_bf16 v[24:27], a[120:123], v[176:179], v[24:27]\n"
"s_add_u32 s76, s72, 114688\n"
"buffer_load_dwordx4 v[216:219], v7, s[36:39], s86 offen offset:1024 sc1\n"
"v_mfma_f32_16x16x32_bf16 v[28:31], a[108:111], v[240:243], v[28:31]\n"
"s_add_u32 m0, s83, 116736\n"
"v_mfma_f32_16x16x32_bf16 v[28:31], a[124:127], v[176:179], v[28:31]\n"
"s_waitcnt vmcnt(13)\n"
"buffer_load_dwordx4 v12, s[40:43], s76 offen lds\n"
"v_mfma_f32_16x16x32_bf16 v[32:35], a[96:99], v[244:247], v[32:35]\n"
"s_waitcnt vmcnt(13)\n"
"v_mfma_f32_16x16x32_bf16 v[32:35], a[112:115], v[180:183], v[32:35]\n"
"buffer_load_dwordx4 v[204:207], v11, s[36:39], s87 offen offset:0 sc1\n"
"v_mfma_f32_16x16x32_bf16 v[36:39], a[100:103], v[244:247], v[36:39]\n"
"v_mfma_f32_16x16x32_bf16 v[36:39], a[116:119], v[180:183], v[36:39]\n"
"v_mfma_f32_16x16x32_bf16 v[40:43], a[104:107], v[244:247], v[40:43]\n"
"v_mfma_f32_16x16x32_bf16 v[40:43], a[120:123], v[180:183], v[40:43]\n"
"buffer_load_dwordx4 v[220:223], v11, s[36:39], s87 offen offset:1024 sc1\n"
"v_mfma_f32_16x16x32_bf16 v[44:47], a[108:111], v[244:247], v[44:47]\n"
"v_mfma_f32_16x16x32_bf16 v[44:47], a[124:127], v[180:183], v[44:47]\n"
"s_waitcnt vmcnt(14)\n"
"v_mfma_f32_16x16x32_bf16 v[48:51], a[96:99], v[248:251], v[48:51]\n"
"s_waitcnt vmcnt(13)\n"
"v_mfma_f32_16x16x32_bf16 v[48:51], a[112:115], v[184:187], v[48:51]\n"
"buffer_load_dwordx4 v[224:227], v3, s[36:39], s84 offen offset:2048 sc1\n"
"v_mfma_f32_16x16x32_bf16 v[52:55], a[100:103], v[248:251], v[52:55]\n"
"v_mfma_f32_16x16x32_bf16 v[52:55], a[116:119], v[184:187], v[52:55]\n"
"v_mfma_f32_16x16x32_bf16 v[56:59], a[104:107], v[248:251], v[56:59]\n"
"v_mfma_f32_16x16x32_bf16 v[56:59], a[120:123], v[184:187], v[56:59]\n"
"s_add_u32 s77, s72, 122880\n"
"buffer_load_dwordx4 v[240:243], v3, s[36:39], s84 offen offset:3072 sc1\n"
"v_mfma_f32_16x16x32_bf16 v[60:63], a[108:111], v[248:251], v[60:63]\n"
"s_add_u32 m0, s83, 117760\n"
"v_mfma_f32_16x16x32_bf16 v[60:63], a[124:127], v[184:187], v[60:63]\n"
"s_waitcnt vmcnt(13)\n"
"buffer_load_dwordx4 v12, s[40:43], s77 offen lds\n"
"v_mfma_f32_16x16x32_bf16 v[64:67], a[96:99], v[252:255], v[64:67]\n"
"s_waitcnt vmcnt(13)\n"
"v_mfma_f32_16x16x32_bf16 v[64:67], a[112:115], v[188:191], v[64:67]\n"
"buffer_load_dwordx4 v[228:231], v4, s[36:39], s85 offen offset:2048 sc1\n"
"v_mfma_f32_16x16x32_bf16 v[68:71], a[100:103], v[252:255], v[68:71]\n"
"v_mfma_f32_16x16x32_bf16 v[68:71], a[116:119], v[188:191], v[68:71]\n"
"v_mfma_f32_16x16x32_bf16 v[72:75], a[104:107], v[252:255], v[72:75]\n"
"v_mfma_f32_16x16x32_bf16 v[72:75], a[120:123], v[188:191], v[72:75]\n"
"buffer_load_dwordx4 v[244:247], v4, s[36:39], s85 offen offset:3072 sc1\n"
"v_mfma_f32_16x16x32_bf16 v[76:79], a[108:111], v[252:255], v[76:79]\n"
"v_mfma_f32_16x16x32_bf16 v[76:79], a[124:127], v[188:191], v[76:79]\n"
"s_waitcnt vmcnt(14)\n"
"v_mfma_f32_16x16x32_bf16 v[16:19], v[80:83], v[192:195], v[16:19]\n"
"s_waitcnt vmcnt(13)\n"
"v_mfma_f32_16x16x32_bf16 v[16:19], v[96:99], v[208:211], v[16:19]\n"
"buffer_load_dwordx4 v[232:235], v7, s[36:39], s86 offen offset:2048 sc1\n"
"v_mfma_f32_16x16x32_bf16 v[20:23], v[84:87], v[192:195], v[20:23]\n"
"v_mfma_f32_16x16x32_bf16 v[20:23], v[100:103], v[208:211], v[20:23]\n"
"v_mfma_f32_16x16x32_bf16 v[24:27], v[88:91], v[192:195], v[24:27]\n"
"v_mfma_f32_16x16x32_bf16 v[24:27], v[104:107], v[208:211], v[24:27]\n"
"buffer_load_dwordx4 v[248:251], v7, s[36:39], s86 offen offset:3072 sc1\n"
"v_mfma_f32_16x16x32_bf16 v[28:31], v[92:95], v[192:195], v[28:31]\n"
"v_mfma_f32_16x16x32_bf16 v[28:31], v[108:111], v[208:211], v[28:31]\n"
"s_waitcnt vmcnt(13)\n"
"v_mfma_f32_16x16x32_bf16 v[32:35], v[80:83], v[196:199], v[32:35]\n"
"s_waitcnt vmcnt(12)\n"
"v_mfma_f32_16x16x32_bf16 v[32:35], v[96:99], v[212:215], v[32:35]\n"
"buffer_load_dwordx4 v[236:239], v11, s[36:39], s87 offen offset:2048 sc1\n"
"v_mfma_f32_16x16x32_bf16 v[36:39], v[84:87], v[196:199], v[36:39]\n"
"v_mfma_f32_16x16x32_bf16 v[36:39], v[100:103], v[212:215], v[36:39]\n"
"v_mfma_f32_16x16x32_bf16 v[40:43], v[88:91], v[196:199], v[40:43]\n"
"v_mfma_f32_16x16x32_bf16 v[40:43], v[104:107], v[212:215], v[40:43]\n"
"buffer_load_dwordx4 v[252:255], v11, s[36:39], s87 offen offset:3072 sc1\n"
"v_mfma_f32_16x16x32_bf16 v[44:47], v[92:95], v[196:199], v[44:47]\n"
"s_add_u32 s62, s69, 5\n"
"s_and_b32 s62, s62, 3\n"
"s_add_u32 s62, s62, s35\n"
"v_mfma_f32_16x16x32_bf16 v[44:47], v[108:111], v[212:215], v[44:47]\n"
"s_waitcnt vmcnt(13)\n"
"s_xor_b32 s62, s62, 4\n"
"s_and_b32 s62, s62, 7\n"
"v_mfma_f32_16x16x32_bf16 v[48:51], v[80:83], v[200:203], v[48:51]\n"
"s_waitcnt vmcnt(12)\n"
"s_lshl_b32 s62, s62, 12\n"
"s_add_u32 s84, s64, s62\n"
"s_add_u32 s85, s65, s62\n"
"v_mfma_f32_16x16x32_bf16 v[48:51], v[96:99], v[216:219], v[48:51]\n"
"s_add_u32 s86, s66, s62\n"
"s_add_u32 s87, s67, s62\n"
"buffer_load_dwordx4 v[176:179], v3, s[36:39], s84 offen offset:0 sc1\n"
"v_mfma_f32_16x16x32_bf16 v[52:55], v[84:87], v[200:203], v[52:55]\n"
"v_mfma_f32_16x16x32_bf16 v[52:55], v[100:103], v[216:219], v[52:55]\n"
"v_mfma_f32_16x16x32_bf16 v[56:59], v[88:91], v[200:203], v[56:59]\n"
"v_mfma_f32_16x16x32_bf16 v[56:59], v[104:107], v[216:219], v[56:59]\n"
"buffer_load_dwordx4 v[192:195], v3, s[36:39], s84 offen offset:1024 sc1\n"
"v_mfma_f32_16x16x32_bf16 v[60:63], v[92:95], v[200:203], v[60:63]\n"
"v_mfma_f32_16x16x32_bf16 v[60:63], v[108:111], v[216:219], v[60:63]\n"
"s_waitcnt vmcnt(12)\n"
"v_mfma_f32_16x16x32_bf16 v[64:67], v[80:83], v[204:207], v[64:67]\n"
"s_waitcnt vmcnt(11)\n"
"v_mfma_f32_16x16x32_bf16 v[64:67], v[96:99], v[220:223], v[64:67]\n"
"buffer_load_dwordx4 v[180:183], v4, s[36:39], s85 offen offset:0 sc1\n"
"v_mfma_f32_16x16x32_bf16 v[68:71], v[84:87], v[204:207], v[68:71]\n"
"v_mfma_f32_16x16x32_bf16 v[68:71], v[100:103], v[220:223], v[68:71]\n"
"v_mfma_f32_16x16x32_bf16 v[72:75], v[88:91], v[204:207], v[72:75]\n"
"v_mfma_f32_16x16x32_bf16 v[72:75], v[104:107], v[220:223], v[72:75]\n"
"buffer_load_dwordx4 v[196:199], v4, s[36:39], s85 offen offset:1024 sc1\n"
"v_mfma_f32_16x16x32_bf16 v[76:79], v[92:95], v[204:207], v[76:79]\n"
"v_mfma_f32_16x16x32_bf16 v[76:79], v[108:111], v[220:223], v[76:79]\n"
"s_waitcnt vmcnt(12)\n"
"v_mfma_f32_16x16x32_bf16 v[16:19], v[112:115], v[224:227], v[16:19]\n"
"s_waitcnt vmcnt(11)\n"
"v_mfma_f32_16x16x32_bf16 v[16:19], v[128:131], v[240:243], v[16:19]\n"
"buffer_load_dwordx4 v[184:187], v7, s[36:39], s86 offen offset:0 sc1\n"
"v_mfma_f32_16x16x32_bf16 v[20:23], v[116:119], v[224:227], v[20:23]\n"
"v_mfma_f32_16x16x32_bf16 v[20:23], v[132:135], v[240:243], v[20:23]\n"
"v_mfma_f32_16x16x32_bf16 v[24:27], v[120:123], v[224:227], v[24:27]\n"
"v_mfma_f32_16x16x32_bf16 v[24:27], v[136:139], v[240:243], v[24:27]\n"
"buffer_load_dwordx4 v[200:203], v7, s[36:39], s86 offen offset:1024 sc1\n"
"v_mfma_f32_16x16x32_bf16 v[28:31], v[124:127], v[224:227], v[28:31]\n"
"v_mfma_f32_16x16x32_bf16 v[28:31], v[140:143], v[240:243], v[28:31]\n"
"s_waitcnt vmcnt(11)\n"
"v_mfma_f32_16x16x32_bf16 v[32:35], v[112:115], v[228:231], v[32:35]\n"
"s_waitcnt vmcnt(10)\n"
"v_mfma_f32_16x16x32_bf16 v[32:35], v[128:131], v[244:247], v[32:35]\n"
"buffer_load_dwordx4 v[188:191], v11, s[36:39], s87 offen offset:0 sc1\n"
"v_mfma_f32_16x16x32_bf16 v[36:39], v[116:119], v[228:231], v[36:39]\n"
"v_mfma_f32_16x16x32_bf16 v[36:39], v[132:135], v[244:247], v[36:39]\n"
"v_mfma_f32_16x16x32_bf16 v[40:43], v[120:123], v[228:231], v[40:43]\n"
"v_mfma_f32_16x16x32_bf16 v[40:43], v[136:139], v[244:247], v[40:43]\n"
"buffer_load_dwordx4 v[204:207], v11, s[36:39], s87 offen offset:1024 sc1\n"
"v_mfma_f32_16x16x32_bf16 v[44:47], v[124:127], v[228:231], v[44:47]\n"
"v_mfma_f32_16x16x32_bf16 v[44:47], v[140:143], v[244:247], v[44:47]\n"
"s_waitcnt vmcnt(11)\n"
"v_mfma_f32_16x16x32_bf16 v[48:51], v[112:115], v[232:235], v[48:51]\n"
"s_waitcnt vmcnt(10)\n"
"ds_read_b64_tr_b16 a[128:129], v1 offset:32768\n"
"v_mfma_f32_16x16x32_bf16 v[48:51], v[128:131], v[248:251], v[48:51]\n"
"buffer_load_dwordx4 v[208:211], v3, s[36:39], s84 offen offset:2048 sc1\n"
"ds_read_b64_tr_b16 a[130:131], v1 offset:34816\n"
"v_mfma_f32_16x16x32_bf16 v[52:55], v[116:119], v[232:235], v[52:55]\n"
"ds_read_b64_tr_b16 a[132:133], v2 offset:32768\n"
"v_mfma_f32_16x16x32_bf16 v[52:55], v[132:135], v[248:251], v[52:55]\n"
"v_mfma_f32_16x16x32_bf16 v[56:59], v[120:123], v[232:235], v[56:59]\n"
"ds_read_b64_tr_b16 a[134:135], v2 offset:34816\n"
"v_mfma_f32_16x16x32_bf16 v[56:59], v[136:139], v[248:251], v[56:59]\n"
"buffer_load_dwordx4 v[224:227], v3, s[36:39], s84 offen offset:3072 sc1\n"
"ds_read_b64_tr_b16 a[136:137], v1 offset:33280\n"
"v_mfma_f32_16x16x32_bf16 v[60:63], v[124:127], v[232:235], v[60:63]\n"
"ds_read_b64_tr_b16 a[138:139], v1 offset:35328\n"
"v_mfma_f32_16x16x32_bf16 v[60:63], v[140:143], v[248:251], v[60:63]\n"
"s_waitcnt vmcnt(11)\n"
"v_mfma_f32_16x16x32_bf16 v[64:67], v[112:115], v[236:239], v[64:67]\n"
"s_waitcnt vmcnt(10)\n"
"ds_read_b64_tr_b16 a[140:141], v2 offset:33280\n"
"v_mfma_f32_16x16x32_bf16 v[64:67], v[128:131], v[252:255], v[64:67]\n"
"buffer_load_dwordx4 v[212:215], v4, s[36:39], s85 offen offset:2048 sc1\n"
"ds_read_b64_tr_b16 a[142:143], v2 offset:35328\n"
"v_mfma_f32_16x16x32_bf16 v[68:71], v[116:119], v[236:239], v[68:71]\n"
"v_mfma_f32_16x16x32_bf16 v[68:71], v[132:135], v[252:255], v[68:71]\n"
"v_mfma_f32_16x16x32_bf16 v[72:75], v[120:123], v[236:239], v[72:75]\n"
"v_mfma_f32_16x16x32_bf16 v[72:75], v[136:139], v[252:255], v[72:75]\n"
"buffer_load_dwordx4 v[228:231], v4, s[36:39], s85 offen offset:3072 sc1\n"
"v_mfma_f32_16x16x32_bf16 v[76:79], v[124:127], v[236:239], v[76:79]\n"
"v_mfma_f32_16x16x32_bf16 v[76:79], v[140:143], v[252:255], v[76:79]\n"
"s_waitcnt vmcnt(11)\n"
"v_mfma_f32_16x16x32_bf16 v[16:19], v[144:147], v[176:179], v[16:19]\n"
"s_waitcnt vmcnt(10)\n"
"ds_read_b64_tr_b16 a[144:145], v5 offset:32768\n"
"v_mfma_f32_16x16x32_bf16 v[16:19], v[160:163], v[192:195], v[16:19]\n"
"buffer_load_dwordx4 v[216:219], v7, s[36:39], s86 offen offset:2048 sc1\n"
"ds_read_b64_tr_b16 a[146:147], v5 offset:34816\n"
"v_mfma_f32_16x16x32_bf16 v[20:23], v[148:151], v[176:179], v[20:23]\n"
"ds_read_b64_tr_b16 a[148:149], v6 offset:32768\n"
"v_mfma_f32_16x16x32_bf16 v[20:23], v[164:167], v[192:195], v[20:23]\n"
"v_mfma_f32_16x16x32_bf16 v[24:27], v[152:155], v[176:179], v[24:27]\n"
"ds_read_b64_tr_b16 a[150:151], v6 offset:34816\n"
"v_mfma_f32_16x16x32_bf16 v[24:27], v[168:171], v[192:195], v[24:27]\n"
"buffer_load_dwordx4 v[232:235], v7, s[36:39], s86 offen offset:3072 sc1\n"
"ds_read_b64_tr_b16 a[152:153], v5 offset:33280\n"
"v_mfma_f32_16x16x32_bf16 v[28:31], v[156:159], v[176:179], v[28:31]\n"
"ds_read_b64_tr_b16 a[154:155], v5 offset:35328\n"
"v_mfma_f32_16x16x32_bf16 v[28:31], v[172:175], v[192:195], v[28:31]\n"
"s_waitcnt vmcnt(11)\n"
"v_mfma_f32_16x16x32_bf16 v[32:35], v[144:147], v[180:183], v[32:35]\n"
"s_waitcnt vmcnt(10)\n"
"ds_read_b64_tr_b16 a[156:157], v6 offset:33280\n"
"v_mfma_f32_16x16x32_bf16 v[32:35], v[160:163], v[196:199], v[32:35]\n"
"buffer_load_dwordx4 v[220:223], v11, s[36:39], s87 offen offset:2048 sc1\n"
"ds_read_b64_tr_b16 a[158:159], v6 offset:35328\n"
"v_mfma_f32_16x16x32_bf16 v[36:39], v[148:151], v[180:183], v[36:39]\n"
"v_mfma_f32_16x16x32_bf16 v[36:39], v[164:167], v[196:199], v[36:39]\n"
"v_mfma_f32_16x16x32_bf16 v[40:43], v[152:155], v[180:183], v[40:43]\n"
"v_mfma_f32_16x16x32_bf16 v[40:43], v[168:171], v[196:199], v[40:43]\n"
"buffer_load_dwordx4 v[236:239], v11, s[36:39], s87 offen offset:3072 sc1\n"
"v_mfma_f32_16x16x32_bf16 v[44:47], v[156:159], v[180:183], v[44:47]\n"
"s_add_u32 s62, s69, 6\n"
"s_and_b32 s62, s62, 3\n"
"s_add_u32 s62, s62, s35\n"
"v_mfma_f32_16x16x32_bf16 v[44:47], v[172:175], v[196:199], v[44:47]\n"
"s_waitcnt vmcnt(11)\n"
"s_xor_b32 s62, s62, 4\n"
"s_and_b32 s62, s62, 7\n"
"v_mfma_f32_16x16x32_bf16 v[48:51], v[144:147], v[184:187], v[48:51]\n"
"s_waitcnt vmcnt(10)\n"
"s_lshl_b32 s62, s62, 12\n"
"s_add_u32 s84, s64, s62\n"
"s_add_u32 s85, s65, s62\n"
"ds_read_b64_tr_b16 a[160:161], v1 offset:16384\n"
"v_mfma_f32_16x16x32_bf16 v[48:51], v[160:163], v[200:203], v[48:51]\n"
"s_add_u32 s86, s66, s62\n"
"s_add_u32 s87, s67, s62\n"
"buffer_load_dwordx4 v[240:243], v3, s[36:39], s84 offen offset:0 sc1\n"
"ds_read_b64_tr_b16 a[162:163], v1 offset:18432\n"
"v_mfma_f32_16x16x32_bf16 v[52:55], v[148:151], v[184:187], v[52:55]\n"
"ds_read_b64_tr_b16 a[164:165], v2 offset:16384\n"
"v_mfma_f32_16x16x32_bf16 v[52:55], v[164:167], v[200:203], v[52:55]\n"
"v_mfma_f32_16x16x32_bf16 v[56:59], v[152:155], v[184:187], v[56:59]\n"
"ds_read_b64_tr_b16 a[166:167], v2 offset:18432\n"
"v_mfma_f32_16x16x32_bf16 v[56:59], v[168:171], v[200:203], v[56:59]\n"
"buffer_load_dwordx4 v[176:179], v3, s[36:39], s84 offen offset:1024 sc1\n"
"ds_read_b64_tr_b16 a[168:169], v1 offset:16896\n"
"v_mfma_f32_16x16x32_bf16 v[60:63], v[156:159], v[184:187], v[60:63]\n"
"ds_read_b64_tr_b16 a[170:171], v1 offset:18944\n"
"v_mfma_f32_16x16x32_bf16 v[60:63], v[172:175], v[200:203], v[60:63]\n"
"s_waitcnt vmcnt(11)\n"
"v_mfma_f32_16x16x32_bf16 v[64:67], v[144:147], v[188:191], v[64:67]\n"
"s_waitcnt vmcnt(10)\n"
"ds_read_b64_tr_b16 a[172:173], v2 offset:16896\n"
"v_mfma_f32_16x16x32_bf16 v[64:67], v[160:163], v[204:207], v[64:67]\n"
"buffer_load_dwordx4 v[244:247], v4, s[36:39], s85 offen offset:0 sc1\n"
"ds_read_b64_tr_b16 a[174:175], v2 offset:18944\n"
"v_mfma_f32_16x16x32_bf16 v[68:71], v[148:151], v[188:191], v[68:71]\n"
"v_mfma_f32_16x16x32_bf16 v[68:71], v[164:167], v[204:207], v[68:71]\n"
"v_mfma_f32_16x16x32_bf16 v[72:75], v[152:155], v[188:191], v[72:75]\n"
"v_mfma_f32_16x16x32_bf16 v[72:75], v[168:171], v[204:207], v[72:75]\n"
"buffer_load_dwordx4 v[180:183], v4, s[36:39], s85 offen offset:1024 sc1\n"
"v_mfma_f32_16x16x32_bf16 v[76:79], v[156:159], v[188:191], v[76:79]\n"
"s_waitcnt lgkmcnt(15)\n"
"v_mfma_f32_16x16x32_bf16 v[76:79], v[172:175], v[204:207], v[76:79]\n"
"s_waitcnt vmcnt(11)\n"
"s_waitcnt lgkmcnt(8)\n"
"v_mfma_f32_16x16x32_bf16 v[16:19], a[128:131], v[208:211], v[16:19]\n"
"s_waitcnt vmcnt(10)\n"
"ds_read_b64_tr_b16 a[176:177], v5 offset:16384\n"
"v_mfma_f32_16x16x32_bf16 v[16:19], a[144:147], v[224:227], v[16:19]\n"
"buffer_load_dwordx4 v[248:251], v7, s[36:39], s86 offen offset:0 sc1\n"
"ds_read_b64_tr_b16 a[178:179], v5 offset:18432\n"
"v_mfma_f32_16x16x32_bf16 v[20:23], a[132:135], v[208:211], v[20:23]\n"
"ds_read_b64_tr_b16 a[180:181], v6 offset:16384\n"
"v_mfma_f32_16x16x32_bf16 v[20:23], a[148:151], v[224:227], v[20:23]\n"
"v_mfma_f32_16x16x32_bf16 v[24:27], a[136:139], v[208:211], v[24:27]\n"
"ds_read_b64_tr_b16 a[182:183], v6 offset:18432\n"
"v_mfma_f32_16x16x32_bf16 v[24:27], a[152:155], v[224:227], v[24:27]\n"
"buffer_load_dwordx4 v[184:187], v7, s[36:39], s86 offen offset:1024 sc1\n"
"ds_read_b64_tr_b16 a[184:185], v5 offset:16896\n"
"v_mfma_f32_16x16x32_bf16 v[28:31], a[140:143], v[208:211], v[28:31]\n"
"ds_read_b64_tr_b16 a[186:187], v5 offset:18944\n"
"v_mfma_f32_16x16x32_bf16 v[28:31], a[156:159], v[224:227], v[28:31]\n"
"s_waitcnt vmcnt(11)\n"
"v_mfma_f32_16x16x32_bf16 v[32:35], a[128:131], v[212:215], v[32:35]\n"
"s_waitcnt vmcnt(10)\n"
"ds_read_b64_tr_b16 a[188:189], v6 offset:16896\n"
"v_mfma_f32_16x16x32_bf16 v[32:35], a[144:147], v[228:231], v[32:35]\n"
"buffer_load_dwordx4 v[252:255], v11, s[36:39], s87 offen offset:0 sc1\n"
"ds_read_b64_tr_b16 a[190:191], v6 offset:18944\n"
"v_mfma_f32_16x16x32_bf16 v[36:39], a[132:135], v[212:215], v[36:39]\n"
"v_mfma_f32_16x16x32_bf16 v[36:39], a[148:151], v[228:231], v[36:39]\n"
"v_mfma_f32_16x16x32_bf16 v[40:43], a[136:139], v[212:215], v[40:43]\n"
"v_mfma_f32_16x16x32_bf16 v[40:43], a[152:155], v[228:231], v[40:43]\n"
"buffer_load_dwordx4 v[188:191], v11, s[36:39], s87 offen offset:1024 sc1\n"
"v_mfma_f32_16x16x32_bf16 v[44:47], a[140:143], v[212:215], v[44:47]\n"
"v_mfma_f32_16x16x32_bf16 v[44:47], a[156:159], v[228:231], v[44:47]\n"
"s_waitcnt vmcnt(11)\n"
"v_mfma_f32_16x16x32_bf16 v[48:51], a[128:131], v[216:219], v[48:51]\n"
"s_waitcnt vmcnt(10)\n"
"ds_read_b64_tr_b16 a[192:193], v1 offset:0\n"
"v_mfma_f32_16x16x32_bf16 v[48:51], a[144:147], v[232:235], v[48:51]\n"
"buffer_load_dwordx4 v[192:195], v3, s[36:39], s84 offen offset:2048 sc1\n"
"ds_read_b64_tr_b16 a[194:195], v1 offset:2048\n"
"v_mfma_f32_16x16x32_bf16 v[52:55], a[132:135], v[216:219], v[52:55]\n"
"ds_read_b64_tr_b16 a[196:197], v2 offset:0\n"
"v_mfma_f32_16x16x32_bf16 v[52:55], a[148:151], v[232:235], v[52:55]\n"
"v_mfma_f32_16x16x32_bf16 v[56:59], a[136:139], v[216:219], v[56:59]\n"
"ds_read_b64_tr_b16 a[198:199], v2 offset:2048\n"
"v_mfma_f32_16x16x32_bf16 v[56:59], a[152:155], v[232:235], v[56:59]\n"
"buffer_load_dwordx4 v[208:211], v3, s[36:39], s84 offen offset:3072 sc1\n"
"ds_read_b64_tr_b16 a[200:201], v1 offset:512\n"
"v_mfma_f32_16x16x32_bf16 v[60:63], a[140:143], v[216:219], v[60:63]\n"
"ds_read_b64_tr_b16 a[202:203], v1 offset:2560\n"
"v_mfma_f32_16x16x32_bf16 v[60:63], a[156:159], v[232:235], v[60:63]\n"
"s_waitcnt vmcnt(11)\n"
"v_mfma_f32_16x16x32_bf16 v[64:67], a[128:131], v[220:223], v[64:67]\n"
"s_waitcnt vmcnt(10)\n"
"ds_read_b64_tr_b16 a[204:205], v2 offset:512\n"
"v_mfma_f32_16x16x32_bf16 v[64:67], a[144:147], v[236:239], v[64:67]\n"
"buffer_load_dwordx4 v[196:199], v4, s[36:39], s85 offen offset:2048 sc1\n"
"ds_read_b64_tr_b16 a[206:207], v2 offset:2560\n"
"v_mfma_f32_16x16x32_bf16 v[68:71], a[132:135], v[220:223], v[68:71]\n"
"v_mfma_f32_16x16x32_bf16 v[68:71], a[148:151], v[236:239], v[68:71]\n"
"v_mfma_f32_16x16x32_bf16 v[72:75], a[136:139], v[220:223], v[72:75]\n"
"v_mfma_f32_16x16x32_bf16 v[72:75], a[152:155], v[236:239], v[72:75]\n"
"buffer_load_dwordx4 v[212:215], v4, s[36:39], s85 offen offset:3072 sc1\n"
"v_mfma_f32_16x16x32_bf16 v[76:79], a[140:143], v[220:223], v[76:79]\n"
"s_waitcnt lgkmcnt(15)\n"
"v_mfma_f32_16x16x32_bf16 v[76:79], a[156:159], v[236:239], v[76:79]\n"
"s_waitcnt vmcnt(11)\n"
"s_waitcnt lgkmcnt(8)\n"
"v_mfma_f32_16x16x32_bf16 v[16:19], a[160:163], v[240:243], v[16:19]\n"
"s_waitcnt vmcnt(10)\n"
"ds_read_b64_tr_b16 a[208:209], v5 offset:0\n"
"ds_read_b64_tr_b16 a[224:225], v13 offset:0\n"
"v_mfma_f32_16x16x32_bf16 v[16:19], a[176:179], v[176:179], v[16:19]\n"
"buffer_load_dwordx4 v[200:203], v7, s[36:39], s86 offen offset:2048 sc1\n"
"ds_read_b64_tr_b16 a[210:211], v5 offset:2048\n"
"v_mfma_f32_16x16x32_bf16 v[20:23], a[164:167], v[240:243], v[20:23]\n"
"ds_read_b64_tr_b16 a[212:213], v6 offset:0\n"
"ds_read_b64_tr_b16 a[226:227], v13 offset:2048\n"
"v_mfma_f32_16x16x32_bf16 v[20:23], a[180:183], v[176:179], v[20:23]\n"
"ds_read_b64_tr_b16 a[228:229], v14 offset:0\n"
"v_mfma_f32_16x16x32_bf16 v[24:27], a[168:171], v[240:243], v[24:27]\n"
"ds_read_b64_tr_b16 a[214:215], v6 offset:2048\n"
"ds_read_b64_tr_b16 a[230:231], v14 offset:2048\n"
"v_mfma_f32_16x16x32_bf16 v[24:27], a[184:187], v[176:179], v[24:27]\n"
"buffer_load_dwordx4 v[216:219], v7, s[36:39], s86 offen offset:3072 sc1\n"
"ds_read_b64_tr_b16 a[216:217], v5 offset:512\n"
"v_mfma_f32_16x16x32_bf16 v[28:31], a[172:175], v[240:243], v[28:31]\n"
"ds_read_b64_tr_b16 a[218:219], v5 offset:2560\n"
"ds_read_b64_tr_b16 a[232:233], v13 offset:512\n"
"v_mfma_f32_16x16x32_bf16 v[28:31], a[188:191], v[176:179], v[28:31]\n"
"s_waitcnt vmcnt(11)\n"
"ds_read_b64_tr_b16 a[234:235], v13 offset:2560\n"
"v_mfma_f32_16x16x32_bf16 v[32:35], a[160:163], v[244:247], v[32:35]\n"
"s_waitcnt vmcnt(10)\n"
"ds_read_b64_tr_b16 a[220:221], v6 offset:512\n"
"ds_read_b64_tr_b16 a[236:237], v14 offset:512\n"
"v_mfma_f32_16x16x32_bf16 v[32:35], a[176:179], v[180:183], v[32:35]\n"
"buffer_load_dwordx4 v[204:207], v11, s[36:39], s87 offen offset:2048 sc1\n"
"ds_read_b64_tr_b16 a[222:223], v6 offset:2560\n"
"v_mfma_f32_16x16x32_bf16 v[36:39], a[164:167], v[244:247], v[36:39]\n"
"ds_read_b64_tr_b16 a[238:239], v14 offset:2560\n"
"v_mfma_f32_16x16x32_bf16 v[36:39], a[180:183], v[180:183], v[36:39]\n"
"v_mfma_f32_16x16x32_bf16 v[40:43], a[168:171], v[244:247], v[40:43]\n"
"ds_read_b64_tr_b16 a[240:241], v13 offset:4096\n"
"v_mfma_f32_16x16x32_bf16 v[40:43], a[184:187], v[180:183], v[40:43]\n"
"buffer_load_dwordx4 v[220:223], v11, s[36:39], s87 offen offset:3072 sc1\n"
"ds_read_b64_tr_b16 a[242:243], v13 offset:6144\n"
"v_mfma_f32_16x16x32_bf16 v[44:47], a[172:175], v[244:247], v[44:47]\n"
"s_add_u32 s62, s69, 7\n"
"s_and_b32 s62, s62, 3\n"
"s_add_u32 s62, s62, s35\n"
"ds_read_b64_tr_b16 a[244:245], v14 offset:4096\n"
"v_mfma_f32_16x16x32_bf16 v[44:47], a[188:191], v[180:183], v[44:47]\n"
"s_waitcnt vmcnt(11)\n"
"s_xor_b32 s62, s62, 4\n"
"s_and_b32 s62, s62, 7\n"
"v_mfma_f32_16x16x32_bf16 v[48:51], a[160:163], v[248:251], v[48:51]\n"
"s_waitcnt vmcnt(10)\n"
"s_lshl_b32 s62, s62, 12\n"
"s_add_u32 s84, s64, s62\n"
"s_add_u32 s85, s65, s62\n"
"ds_read_b64_tr_b16 a[246:247], v14 offset:6144\n"
"v_mfma_f32_16x16x32_bf16 v[48:51], a[176:179], v[184:187], v[48:51]\n"
"s_add_u32 s86, s66, s62\n"
"s_add_u32 s87, s67, s62\n"
"buffer_load_dwordx4 v[84:87], v4, s[36:39], s85 offen offset:0 sc1\n"
"ds_read_b64_tr_b16 a[248:249], v13 offset:4608\n"
"v_mfma_f32_16x16x32_bf16 v[52:55], a[164:167], v[248:251], v[52:55]\n"
"ds_read_b64_tr_b16 a[250:251], v13 offset:6656\n"
"v_mfma_f32_16x16x32_bf16 v[52:55], a[180:183], v[184:187], v[52:55]\n"
"v_mfma_f32_16x16x32_bf16 v[56:59], a[168:171], v[248:251], v[56:59]\n"
"ds_read_b64_tr_b16 a[252:253], v14 offset:4608\n"
"v_mfma_f32_16x16x32_bf16 v[56:59], a[184:187], v[184:187], v[56:59]\n"
"buffer_load_dwordx4 v[100:103], v4, s[36:39], s85 offen offset:1024 sc1\n"
"ds_read_b64_tr_b16 a[254:255], v14 offset:6656\n"
"v_mfma_f32_16x16x32_bf16 v[60:63], a[172:175], v[248:251], v[60:63]\n"
"v_mfma_f32_16x16x32_bf16 v[60:63], a[188:191], v[184:187], v[60:63]\n"
"s_waitcnt vmcnt(11)\n"
"v_mfma_f32_16x16x32_bf16 v[64:67], a[160:163], v[252:255], v[64:67]\n"
"s_waitcnt vmcnt(10)\n"
"ds_read_b64_tr_b16 a[0:1], v1 offset:49152\n"
"v_mfma_f32_16x16x32_bf16 v[64:67], a[176:179], v[188:191], v[64:67]\n"
"buffer_load_dwordx4 v[116:119], v4, s[36:39], s85 offen offset:2048 sc1\n"
"ds_read_b64_tr_b16 a[2:3], v1 offset:51200\n"
"v_mfma_f32_16x16x32_bf16 v[68:71], a[164:167], v[252:255], v[68:71]\n"
"ds_read_b64_tr_b16 a[4:5], v2 offset:49152\n"
"v_mfma_f32_16x16x32_bf16 v[68:71], a[180:183], v[188:191], v[68:71]\n"
"v_mfma_f32_16x16x32_bf16 v[72:75], a[168:171], v[252:255], v[72:75]\n"
"ds_read_b64_tr_b16 a[6:7], v2 offset:51200\n"
"v_mfma_f32_16x16x32_bf16 v[72:75], a[184:187], v[188:191], v[72:75]\n"
"buffer_load_dwordx4 v[132:135], v4, s[36:39], s85 offen offset:3072 sc1\n"
"ds_read_b64_tr_b16 a[8:9], v1 offset:49664\n"
"v_mfma_f32_16x16x32_bf16 v[76:79], a[172:175], v[252:255], v[76:79]\n"
"s_waitcnt lgkmcnt(15)\n"
"ds_read_b64_tr_b16 a[10:11], v1 offset:51712\n"
"v_mfma_f32_16x16x32_bf16 v[76:79], a[188:191], v[188:191], v[76:79]\n"
"s_waitcnt vmcnt(11)\n"
"s_waitcnt lgkmcnt(15)\n"
"v_mfma_f32_16x16x32_bf16 v[16:19], a[192:195], v[192:195], v[16:19]\n"
"s_waitcnt vmcnt(10)\n"
"ds_read_b64_tr_b16 a[12:13], v2 offset:49664\n"
"v_mfma_f32_16x16x32_bf16 v[16:19], a[208:211], v[208:211], v[16:19]\n"
"buffer_load_dwordx4 v[88:91], v7, s[36:39], s86 offen offset:0 sc1\n"
"ds_read_b64_tr_b16 a[14:15], v2 offset:51712\n"
"v_mfma_f32_16x16x32_bf16 v[20:23], a[196:199], v[192:195], v[20:23]\n"
"v_mfma_f32_16x16x32_bf16 v[20:23], a[212:215], v[208:211], v[20:23]\n"
"v_mfma_f32_16x16x32_bf16 v[24:27], a[200:203], v[192:195], v[24:27]\n"
"ds_read_b64_tr_b16 a[16:17], v5 offset:49152\n"
"v_mfma_f32_16x16x32_bf16 v[24:27], a[216:219], v[208:211], v[24:27]\n"
"buffer_load_dwordx4 v[104:107], v7, s[36:39], s86 offen offset:1024 sc1\n"
"ds_read_b64_tr_b16 a[18:19], v5 offset:51200\n"
"v_mfma_f32_16x16x32_bf16 v[28:31], a[204:207], v[192:195], v[28:31]\n"
"ds_read_b64_tr_b16 a[20:21], v6 offset:49152\n"
"v_mfma_f32_16x16x32_bf16 v[28:31], a[220:223], v[208:211], v[28:31]\n"
"s_waitcnt vmcnt(11)\n"
"v_mfma_f32_16x16x32_bf16 v[32:35], a[192:195], v[196:199], v[32:35]\n"
"s_waitcnt vmcnt(10)\n"
"ds_read_b64_tr_b16 a[22:23], v6 offset:51200\n"
"v_mfma_f32_16x16x32_bf16 v[32:35], a[208:211], v[212:215], v[32:35]\n"
"buffer_load_dwordx4 v[120:123], v7, s[36:39], s86 offen offset:2048 sc1\n"
"ds_read_b64_tr_b16 a[24:25], v5 offset:49664\n"
"v_mfma_f32_16x16x32_bf16 v[36:39], a[196:199], v[196:199], v[36:39]\n"
"ds_read_b64_tr_b16 a[26:27], v5 offset:51712\n"
"v_mfma_f32_16x16x32_bf16 v[36:39], a[212:215], v[212:215], v[36:39]\n"
"v_mfma_f32_16x16x32_bf16 v[40:43], a[200:203], v[196:199], v[40:43]\n"
"ds_read_b64_tr_b16 a[28:29], v6 offset:49664\n"
"v_mfma_f32_16x16x32_bf16 v[40:43], a[216:219], v[212:215], v[40:43]\n"
"buffer_load_dwordx4 v[136:139], v7, s[36:39], s86 offen offset:3072 sc1\n"
"ds_read_b64_tr_b16 a[30:31], v6 offset:51712\n"
"v_mfma_f32_16x16x32_bf16 v[44:47], a[204:207], v[196:199], v[44:47]\n"
"v_mfma_f32_16x16x32_bf16 v[44:47], a[220:223], v[212:215], v[44:47]\n"
"s_waitcnt vmcnt(11)\n"
"v_mfma_f32_16x16x32_bf16 v[48:51], a[192:195], v[200:203], v[48:51]\n"
"s_waitcnt vmcnt(10)\n"
"v_mfma_f32_16x16x32_bf16 v[48:51], a[208:211], v[216:219], v[48:51]\n"
"buffer_load_dwordx4 v[92:95], v11, s[36:39], s87 offen offset:0 sc1\n"
"v_mfma_f32_16x16x32_bf16 v[52:55], a[196:199], v[200:203], v[52:55]\n"
"v_mfma_f32_16x16x32_bf16 v[52:55], a[212:215], v[216:219], v[52:55]\n"
"v_mfma_f32_16x16x32_bf16 v[56:59], a[200:203], v[200:203], v[56:59]\n"
"v_mfma_f32_16x16x32_bf16 v[56:59], a[216:219], v[216:219], v[56:59]\n"
"buffer_load_dwordx4 v[108:111], v11, s[36:39], s87 offen offset:1024 sc1\n"
"v_mfma_f32_16x16x32_bf16 v[60:63], a[204:207], v[200:203], v[60:63]\n"
"v_mfma_f32_16x16x32_bf16 v[60:63], a[220:223], v[216:219], v[60:63]\n"
"s_waitcnt vmcnt(11)\n"
"v_mfma_f32_16x16x32_bf16 v[64:67], a[192:195], v[204:207], v[64:67]\n"
"s_waitcnt vmcnt(10)\n"
"v_lshlrev_b32 v12, 4, v0\n"
"v_mfma_f32_16x16x32_bf16 v[64:67], a[208:211], v[220:223], v[64:67]\n"
"s_add_u32 s60, s52, 1\n"
"s_and_b32 s60, s60, 3\n"
"buffer_load_dwordx4 v[124:127], v11, s[36:39], s87 offen offset:2048 sc1\n"
"v_mfma_f32_16x16x32_bf16 v[68:71], a[196:199], v[204:207], v[68:71]\n"
"s_lshl2_add_u32 s61, s60, s52\n"
"v_lshl_add_u32 v1, s61, 12, v12\n"
"v_mfma_f32_16x16x32_bf16 v[68:71], a[212:215], v[220:223], v[68:71]\n"
"s_lshl2_add_u32 s63, s52, s60\n"
"v_lshl_add_u32 v6, s63, 12, v12\n"
"v_mfma_f32_16x16x32_bf16 v[72:75], a[200:203], v[204:207], v[72:75]\n"
"v_mfma_f32_16x16x32_bf16 v[72:75], a[216:219], v[220:223], v[72:75]\n"
"s_add_u32 s60, s52, 2\n"
"s_and_b32 s60, s60, 3\n"
"buffer_load_dwordx4 v[140:143], v11, s[36:39], s87 offen offset:3072 sc1\n"
"v_mfma_f32_16x16x32_bf16 v[76:79], a[204:207], v[204:207], v[76:79]\n"
"s_waitcnt lgkmcnt(15)\n"
"s_lshl2_add_u32 s61, s60, s52\n"
"v_lshl_add_u32 v2, s61, 12, v12\n"
"v_mfma_f32_16x16x32_bf16 v[76:79], a[220:223], v[220:223], v[76:79]\n"
"s_waitcnt vmcnt(11)\n"
"s_lshl2_add_u32 s63, s52, s60\n"
"v_lshl_add_u32 v13, s63, 12, v12\n"
"v_mfma_f32_16x16x32_bf16 v[32:35], a[224:227], v[84:87], v[32:35]\n"
"s_waitcnt vmcnt(10)\n"
"v_mfma_f32_16x16x32_bf16 v[32:35], a[240:243], v[100:103], v[32:35]\n"
"s_add_u32 s60, s52, 3\n"
"s_and_b32 s60, s60, 3\n"
"buffer_load_dwordx4 v[80:83], v3, s[36:39], s84 offen offset:0 sc1\n"
"v_mfma_f32_16x16x32_bf16 v[36:39], a[228:231], v[84:87], v[36:39]\n"
"s_lshl2_add_u32 s61, s60, s52\n"
"v_lshl_add_u32 v5, s61, 12, v12\n"
"v_mfma_f32_16x16x32_bf16 v[36:39], a[244:247], v[100:103], v[36:39]\n"
"s_lshl2_add_u32 s63, s52, s60\n"
"v_lshl_add_u32 v14, s63, 12, v12\n"
"v_mfma_f32_16x16x32_bf16 v[40:43], a[232:235], v[84:87], v[40:43]\n"
"v_mfma_f32_16x16x32_bf16 v[40:43], a[248:251], v[100:103], v[40:43]\n"
"buffer_load_dwordx4 v[96:99], v3, s[36:39], s84 offen offset:1024 sc1\n"
"v_mfma_f32_16x16x32_bf16 v[44:47], a[236:239], v[84:87], v[44:47]\n"
"s_waitcnt lgkmcnt(8)\n"
"v_mfma_f32_16x16x32_bf16 v[44:47], a[252:255], v[100:103], v[44:47]\n"
"s_waitcnt vmcnt(11)\n"
"s_waitcnt lgkmcnt(0)\n"
"v_mfma_f32_16x16x32_bf16 v[32:35], a[0:3], v[116:119], v[32:35]\n"
"s_waitcnt vmcnt(10)\n"
"v_mfma_f32_16x16x32_bf16 v[32:35], a[16:19], v[132:135], v[32:35]\n"
"buffer_load_dwordx4 v[112:115], v3, s[36:39], s84 offen offset:2048 sc1\n"
"v_mfma_f32_16x16x32_bf16 v[36:39], a[4:7], v[116:119], v[36:39]\n"
"v_mfma_f32_16x16x32_bf16 v[36:39], a[20:23], v[132:135], v[36:39]\n"
"v_mfma_f32_16x16x32_bf16 v[40:43], a[8:11], v[116:119], v[40:43]\n"
"v_mfma_f32_16x16x32_bf16 v[40:43], a[24:27], v[132:135], v[40:43]\n"
"buffer_load_dwordx4 v[128:131], v3, s[36:39], s84 offen offset:3072 sc1\n"
"v_mfma_f32_16x16x32_bf16 v[44:47], a[12:15], v[116:119], v[44:47]\n"
"v_mfma_f32_16x16x32_bf16 v[44:47], a[28:31], v[132:135], v[44:47]\n"
"s_waitcnt vmcnt(11)\n"
"v_mfma_f32_16x16x32_bf16 v[48:51], a[224:227], v[88:91], v[48:51]\n"
"s_waitcnt vmcnt(10)\n"
"v_mfma_f32_16x16x32_bf16 v[48:51], a[240:243], v[104:107], v[48:51]\n"
"v_mfma_f32_16x16x32_bf16 v[52:55], a[228:231], v[88:91], v[52:55]\n"
"v_mfma_f32_16x16x32_bf16 v[52:55], a[244:247], v[104:107], v[52:55]\n"
"v_mfma_f32_16x16x32_bf16 v[56:59], a[232:235], v[88:91], v[56:59]\n"
"ds_write_b128 v1, v[32:35] offset:0\n"
"v_mfma_f32_16x16x32_bf16 v[56:59], a[248:251], v[104:107], v[56:59]\n"
"v_mfma_f32_16x16x32_bf16 v[60:63], a[236:239], v[88:91], v[60:63]\n"
"v_mfma_f32_16x16x32_bf16 v[60:63], a[252:255], v[104:107], v[60:63]\n"
"s_waitcnt vmcnt(9)\n"
"ds_write_b128 v1, v[36:39] offset:1024\n"
"v_mfma_f32_16x16x32_bf16 v[48:51], a[0:3], v[120:123], v[48:51]\n"
"s_waitcnt vmcnt(8)\n"
"v_mfma_f32_16x16x32_bf16 v[48:51], a[16:19], v[136:139], v[48:51]\n"
"v_mfma_f32_16x16x32_bf16 v[52:55], a[4:7], v[120:123], v[52:55]\n"
"ds_write_b128 v1, v[40:43] offset:2048\n"
"v_mfma_f32_16x16x32_bf16 v[52:55], a[20:23], v[136:139], v[52:55]\n"
"v_mfma_f32_16x16x32_bf16 v[56:59], a[8:11], v[120:123], v[56:59]\n"
"v_mfma_f32_16x16x32_bf16 v[56:59], a[24:27], v[136:139], v[56:59]\n"
"ds_write_b128 v1, v[44:47] offset:3072\n"
"v_mfma_f32_16x16x32_bf16 v[60:63], a[12:15], v[120:123], v[60:63]\n"
"v_mfma_f32_16x16x32_bf16 v[60:63], a[28:31], v[136:139], v[60:63]\n"
"s_waitcnt vmcnt(7)\n"
"v_mfma_f32_16x16x32_bf16 v[64:67], a[224:227], v[92:95], v[64:67]\n"
"s_waitcnt vmcnt(6)\n"
"v_mfma_f32_16x16x32_bf16 v[64:67], a[240:243], v[108:111], v[64:67]\n"
"v_mfma_f32_16x16x32_bf16 v[68:71], a[228:231], v[92:95], v[68:71]\n"
"v_mfma_f32_16x16x32_bf16 v[68:71], a[244:247], v[108:111], v[68:71]\n"
"v_mfma_f32_16x16x32_bf16 v[72:75], a[232:235], v[92:95], v[72:75]\n"
"ds_write_b128 v2, v[48:51] offset:0\n"
"v_mfma_f32_16x16x32_bf16 v[72:75], a[248:251], v[108:111], v[72:75]\n"
"v_mfma_f32_16x16x32_bf16 v[76:79], a[236:239], v[92:95], v[76:79]\n"
"v_mfma_f32_16x16x32_bf16 v[76:79], a[252:255], v[108:111], v[76:79]\n"
"s_waitcnt vmcnt(5)\n"
"ds_write_b128 v2, v[52:55] offset:1024\n"
"v_mfma_f32_16x16x32_bf16 v[64:67], a[0:3], v[124:127], v[64:67]\n"
"s_waitcnt vmcnt(4)\n"
"v_mfma_f32_16x16x32_bf16 v[64:67], a[16:19], v[140:143], v[64:67]\n"
"v_mfma_f32_16x16x32_bf16 v[68:71], a[4:7], v[124:127], v[68:71]\n"
"ds_write_b128 v2, v[56:59] offset:2048\n"
"v_mfma_f32_16x16x32_bf16 v[68:71], a[20:23], v[140:143], v[68:71]\n"
"v_mfma_f32_16x16x32_bf16 v[72:75], a[8:11], v[124:127], v[72:75]\n"
"v_mfma_f32_16x16x32_bf16 v[72:75], a[24:27], v[140:143], v[72:75]\n"
"ds_write_b128 v2, v[60:63] offset:3072\n"
"v_mfma_f32_16x16x32_bf16 v[76:79], a[12:15], v[124:127], v[76:79]\n"
"v_mfma_f32_16x16x32_bf16 v[76:79], a[28:31], v[140:143], v[76:79]\n"
"s_waitcnt vmcnt(3)\n"
"v_mfma_f32_16x16x32_bf16 v[16:19], a[224:227], v[80:83], v[16:19]\n"
"s_waitcnt vmcnt(2)\n"
"v_mfma_f32_16x16x32_bf16 v[16:19], a[240:243], v[96:99], v[16:19]\n"
"v_mfma_f32_16x16x32_bf16 v[20:23], a[228:231], v[80:83], v[20:23]\n"
"v_mfma_f32_16x16x32_bf16 v[20:23], a[244:247], v[96:99], v[20:23]\n"
"v_mfma_f32_16x16x32_bf16 v[24:27], a[232:235], v[80:83], v[24:27]\n"
"ds_write_b128 v5, v[64:67] offset:0\n"
"v_mfma_f32_16x16x32_bf16 v[24:27], a[248:251], v[96:99], v[24:27]\n"
"v_mfma_f32_16x16x32_bf16 v[28:31], a[236:239], v[80:83], v[28:31]\n"
"ds_write_b128 v5, v[68:71] offset:1024\n"
"v_mfma_f32_16x16x32_bf16 v[28:31], a[252:255], v[96:99], v[28:31]\n"
"s_waitcnt vmcnt(1)\n"
"v_mfma_f32_16x16x32_bf16 v[16:19], a[0:3], v[112:115], v[16:19]\n"
"s_waitcnt vmcnt(0)\n"
"ds_write_b128 v5, v[72:75] offset:2048\n"
"v_mfma_f32_16x16x32_bf16 v[16:19], a[16:19], v[128:131], v[16:19]\n"
"v_mfma_f32_16x16x32_bf16 v[20:23], a[4:7], v[112:115], v[20:23]\n"
"ds_write_b128 v5, v[76:79] offset:3072\n"
"v_mfma_f32_16x16x32_bf16 v[20:23], a[20:23], v[128:131], v[20:23]\n"
"v_mfma_f32_16x16x32_bf16 v[24:27], a[8:11], v[112:115], v[24:27]\n"
"v_mfma_f32_16x16x32_bf16 v[24:27], a[24:27], v[128:131], v[24:27]\n"
"s_waitcnt lgkmcnt(0)\n"
"s_barrier\n"
"v_mfma_f32_16x16x32_bf16 v[28:31], a[12:15], v[112:115], v[28:31]\n"
"v_mfma_f32_16x16x32_bf16 v[28:31], a[28:31], v[128:131], v[28:31]\n"
"ds_read_b128 v[32:35], v6 offset:0\n"
"ds_read_b128 v[36:39], v6 offset:1024\n"
"ds_read_b128 v[40:43], v6 offset:2048\n"
"ds_read_b128 v[44:47], v6 offset:3072\n"
"ds_read_b128 v[48:51], v13 offset:0\n"
"ds_read_b128 v[52:55], v13 offset:1024\n"
"ds_read_b128 v[56:59], v13 offset:2048\n"
"ds_read_b128 v[60:63], v13 offset:3072\n"
"ds_read_b128 v[64:67], v14 offset:0\n"
"ds_read_b128 v[68:71], v14 offset:1024\n"
"ds_read_b128 v[72:75], v14 offset:2048\n"
"ds_read_b128 v[76:79], v14 offset:3072\n"
"s_waitcnt lgkmcnt(8)\n"
"v_pk_add_f32 v[16:17], v[16:17], v[32:33]\n"
"v_pk_add_f32 v[18:19], v[18:19], v[34:35]\n"
"v_pk_add_f32 v[20:21], v[20:21], v[36:37]\n"
"v_pk_add_f32 v[22:23], v[22:23], v[38:39]\n"
"v_pk_add_f32 v[24:25], v[24:25], v[40:41]\n"
"v_pk_add_f32 v[26:27], v[26:27], v[42:43]\n"
"v_pk_add_f32 v[28:29], v[28:29], v[44:45]\n"
"v_pk_add_f32 v[30:31], v[30:31], v[46:47]\n"
"s_waitcnt lgkmcnt(4)\n"
"v_pk_add_f32 v[16:17], v[16:17], v[48:49]\n"
"v_pk_add_f32 v[18:19], v[18:19], v[50:51]\n"
"v_pk_add_f32 v[20:21], v[20:21], v[52:53]\n"
"v_pk_add_f32 v[22:23], v[22:23], v[54:55]\n"
"v_pk_add_f32 v[24:25], v[24:25], v[56:57]\n"
"v_pk_add_f32 v[26:27], v[26:27], v[58:59]\n"
"v_pk_add_f32 v[28:29], v[28:29], v[60:61]\n"
"v_pk_add_f32 v[30:31], v[30:31], v[62:63]\n"
"s_waitcnt lgkmcnt(0)\n"
"v_pk_add_f32 v[16:17], v[16:17], v[64:65]\n"
"v_pk_add_f32 v[18:19], v[18:19], v[66:67]\n"
"v_pk_add_f32 v[20:21], v[20:21], v[68:69]\n"
"v_pk_add_f32 v[22:23], v[22:23], v[70:71]\n"
"v_pk_add_f32 v[24:25], v[24:25], v[72:73]\n"
"v_pk_add_f32 v[26:27], v[26:27], v[74:75]\n"
"v_pk_add_f32 v[28:29], v[28:29], v[76:77]\n"
"v_pk_add_f32 v[30:31], v[30:31], v[78:79]\n"
"v_cvt_pk_bf16_f32 v160, v16, v17\n"
"v_cvt_pk_bf16_f32 v161, v18, v19\n"
"buffer_store_dwordx2 v[160:161], v8, s[44:47], 0 offen offset:0 \n"
"v_cvt_pk_bf16_f32 v162, v20, v21\n"
"v_cvt_pk_bf16_f32 v163, v22, v23\n"
"buffer_store_dwordx2 v[162:163], v8, s[44:47], 0 offen offset:32 \n"
"v_cvt_pk_bf16_f32 v164, v24, v25\n"
"v_cvt_pk_bf16_f32 v165, v26, v27\n"
"buffer_store_dwordx2 v[164:165], v8, s[44:47], 0 offen offset:64 \n"
"v_cvt_pk_bf16_f32 v166, v28, v29\n"
"v_cvt_pk_bf16_f32 v167, v30, v31\n"
"buffer_store_dwordx2 v[166:167], v8, s[44:47], 0 offen offset:96 \n"
"s_endpgm\n"
"L_wait_%=:\n"
"buffer_load_dword v16, v9, s[48:51], 0 offen sc1\n"
"s_add_u32 s75, s75, 1\n"
"s_and_b32 s60, s75, 15\n"
"s_waitcnt vmcnt(0)\n"
"v_cmp_ne_u32 vcc, s73, v16\n"
"s_cmp_lg_u32 s60, 0\n"
"s_nop 0\n"
"s_cbranch_vccz L_go_%=\n"
"s_cbranch_scc1 L_wait_%=\n"
"buffer_load_dword v15, v18, s[48:51], s71 offen sc1\n"
"s_waitcnt vmcnt(0)\n"
"buffer_store_dword v15, v10, s[48:51], 0 offen sc1\n"
"s_waitcnt vmcnt(0)\n"
"s_sleep 8\n"
"s_cmp_gt_u32 s75, 0x100000\n"
"s_cbranch_scc0 L_wait_%=\n"
"s_branch L_go_%=\n"
"L_fix_%=:\n"
"buffer_store_dword v9, v10, s[48:51], 0 offen sc1\n"
"s_waitcnt vmcnt(0)\n"
"s_branch L_fixed_%=\n"
"L_slow_%=:\n"
"s_waitcnt vmcnt(0) lgkmcnt(0)\n"
"s_nop 7\n"
"s_nop 7\n"
"v_mov_b32 v120, 0\n"
"v_mov_b32 v121, 0\n"
"v_mov_b32 v122, 0\n"
"v_mov_b32 v123, 0\n"
"v_mov_b32 v124, 0\n"
"v_mov_b32 v125, 0\n"
"v_mov_b32 v126, 0\n"
"v_mov_b32 v127, 0\n"
"v_mov_b32 v128, 0\n"
"v_mov_b32 v129, 0\n"
"v_mov_b32 v130, 0\n"
"v_mov_b32 v131, 0\n"
"v_mov_b32 v132, 0\n"
"v_mov_b32 v133, 0\n"
"v_mov_b32 v134, 0\n"
"v_mov_b32 v135, 0\n"
"v_mov_b32 v136, 0\n"
"v_mov_b32 v137, 0\n"
"v_mov_b32 v138, 0\n"
"v_mov_b32 v139, 0\n"
"v_mov_b32 v140, 0\n"
"v_mov_b32 v141, 0\n"
"v_mov_b32 v142, 0\n"
"v_mov_b32 v143, 0\n"
"v_mov_b32 v144, 0\n"
"v_mov_b32 v145, 0\n"
"v_mov_b32 v146, 0\n"
"v_mov_b32 v147, 0\n"
"v_mov_b32 v148, 0\n"
"v_mov_b32 v149, 0\n"
"v_mov_b32 v150, 0\n"
"v_mov_b32 v151, 0\n"
"v_mov_b32 v152, 0\n"
"v_mov_b32 v153, 0\n"
"v_mov_b32 v154, 0\n"
"v_mov_b32 v155, 0\n"
"v_mov_b32 v156, 0\n"
"v_mov_b32 v157, 0\n"
"v_mov_b32 v158, 0\n"
"v_mov_b32 v159, 0\n"
"v_mov_b32 v160, 0\n"
"v_mov_b32 v161, 0\n"
"v_mov_b32 v162, 0\n"
"v_mov_b32 v163, 0\n"
"v_mov_b32 v164, 0\n"
"v_mov_b32 v165, 0\n"
"v_mov_b32 v166, 0\n"
"v_mov_b32 v167, 0\n"
"v_mov_b32 v168, 0\n"
"v_mov_b32 v169, 0\n"
"v_mov_b32 v170, 0\n"
"v_mov_b32 v171, 0\n"
"v_mov_b32 v172, 0\n"
"v_mov_b32 v173, 0\n"
"v_mov_b32 v174, 0\n"
"v_mov_b32 v175, 0\n"
"v_mov_b32 v176, 0\n"
"v_mov_b32 v177, 0\n"
"v_mov_b32 v178, 0\n"
"v_mov_b32 v179, 0\n"
"v_mov_b32 v180, 0\n"
"v_mov_b32 v181, 0\n"
"v_mov_b32 v182, 0\n"
"v_mov_b32 v183, 0\n"
"v_mov_b32 v184, 0\n"
"v_mov_b32 v185, 0\n"
"v_mov_b32 v186, 0\n"
"v_mov_b32 v187, 0\n"
"v_mov_b32 v188, 0\n"
"v_mov_b32 v189, 0\n"
"v_mov_b32 v190, 0\n"
"v_mov_b32 v191, 0\n"
"v_mov_b32 v192, 0\n"
"v_mov_b32 v193, 0\n"
"v_mov_b32 v194, 0\n"
"v_mov_b32 v195, 0\n"
"v_mov_b32 v196, 0\n"
"v_mov_b32 v197, 0\n"
"v_mov_b32 v198, 0\n"
"v_mov_b32 v199, 0\n"
"v_mov_b32 v200, 0\n"
"v_mov_b32 v201, 0\n"
"v_mov_b32 v202, 0\n"
"v_mov_b32 v203, 0\n"
"v_mov_b32 v204, 0\n"
"v_mov_b32 v205, 0\n"
"v_mov_b32 v206, 0\n"
"v_mov_b32 v207, 0\n"
"v_mov_b32 v208, 0\n"
"v_mov_b32 v209, 0\n"
"v_mov_b32 v210, 0\n"
"v_mov_b32 v211, 0\n"
"v_mov_b32 v212, 0\n"
"v_mov_b32 v213, 0\n"
"v_mov_b32 v214, 0\n"
"v_mov_b32 v215, 0\n"
"v_mov_b32 v216, 0\n"
"v_mov_b32 v217, 0\n"
"v_mov_b32 v218, 0\n"
"v_mov_b32 v219, 0\n"
"v_mov_b32 v220, 0\n"
"v_mov_b32 v221, 0\n"
"v_mov_b32 v222, 0\n"
"v_mov_b32 v223, 0\n"
"v_mov_b32 v224, 0\n"
"v_mov_b32 v225, 0\n"
"v_mov_b32 v226, 0\n"
"v_mov_b32 v227, 0\n"
"v_mov_b32 v228, 0\n"
"v_mov_b32 v229, 0\n"
"v_mov_b32 v230, 0\n"
"v_mov_b32 v231, 0\n"
"v_mov_b32 v232, 0\n"
"v_mov_b32 v233, 0\n"
"v_mov_b32 v234, 0\n"
"v_mov_b32 v235, 0\n"
"v_mov_b32 v236, 0\n"
"v_mov_b32 v237, 0\n"
"v_mov_b32 v238, 0\n"
"v_mov_b32 v239, 0\n"
"v_mov_b32 v240, 0\n"
"v_mov_b32 v241, 0\n"
"v_mov_b32 v242, 0\n"
"v_mov_b32 v243, 0\n"
"v_mov_b32 v244, 0\n"
"v_mov_b32 v245, 0\n"
"v_mov_b32 v246, 0\n"
"v_mov_b32 v247, 0\n"
"v_mov_b32 v32, 0xff7fffff\n"
"v_mov_b32 v24, 0\n"
"v_mov_b32 v33, 0xff7fffff\n"
"v_mov_b32 v40, 0\n"
"v_mov_b32 v34, 0xff7fffff\n"
"v_mov_b32 v44, 0\n"
"v_mov_b32 v35, 0xff7fffff\n"
"v_mov_b32 v28, 0\n"
"v_and_b32 v9, 15, v0\n"
"v_lshrrev_b32 v10, 4, v0\n"
"s_lshl_b32 s60, s53, 7\n"
"v_lshlrev_b32 v3, 4, v10\n"
"v_lshl_add_u32 v3, v9, 8, v3\n"
"v_add_u32 v3, s60, v3\n"
"v_xor_b32 v4, 128, v3\n"
"v_lshlrev_b32 v9, 1, v9\n"
"v_lshl_add_u32 v9, v10, 10, v9\n"
"v_add_u32 v9, s60, v9\n"
"v_xor_b32 v10, 128, v9\n"
"s_mov_b32 s62, 0\n"
"L_sl_%=:\n"
"s_add_u32 s63, s62, 4096\n"
"buffer_load_dwordx4 a[0:3], v3, s[28:31], s62 offen offset:0\n"
"buffer_load_dwordx4 a[4:7], v3, s[28:31], s62 offen offset:64\n"
"buffer_load_dwordx4 a[8:11], v4, s[28:31], s62 offen offset:0\n"
"buffer_load_dwordx4 a[12:15], v4, s[28:31], s62 offen offset:64\n"
"buffer_load_dwordx4 a[16:19], v3, s[28:31], s63 offen offset:0\n"
"buffer_load_dwordx4 a[20:23], v3, s[28:31], s63 offen offset:64\n"
"buffer_load_dwordx4 a[24:27], v4, s[28:31], s63 offen offset:0\n"
"buffer_load_dwordx4 a[28:31], v4, s[28:31], s63 offen offset:64\n"
"buffer_load_ushort a64, v9, s[88:91], s62 offen offset:0\n"
"buffer_load_ushort a96, v9, s[88:91], s62 offen offset:256\n"
"buffer_load_ushort a65, v9, s[88:91], s62 offen offset:512\n"
"buffer_load_ushort a97, v9, s[88:91], s62 offen offset:768\n"
"buffer_load_ushort a66, v9, s[88:91], s63 offen offset:0\n"
"buffer_load_ushort a98, v9, s[88:91], s63 offen offset:256\n"
"buffer_load_ushort a67, v9, s[88:91], s63 offen offset:512\n"
"buffer_load_ushort a99, v9, s[88:91], s63 offen offset:768\n"
"buffer_load_ushort a68, v9, s[88:91], s62 offen offset:32\n"
"buffer_load_ushort a100, v9, s[88:91], s62 offen offset:288\n"
"buffer_load_ushort a69, v9, s[88:91], s62 offen offset:544\n"
"buffer_load_ushort a101, v9, s[88:91], s62 offen offset:800\n"
"buffer_load_ushort a70, v9, s[88:91], s63 offen offset:32\n"
"buffer_load_ushort a102, v9, s[88:91], s63 offen offset:288\n"
"buffer_load_ushort a71, v9, s[88:91], s63 offen offset:544\n"
"buffer_load_ushort a103, v9, s[88:91], s63 offen offset:800\n"
"s_waitcnt vmcnt(0)\n"
"buffer_load_ushort a72, v9, s[88:91], s62 offen offset:64\n"
"buffer_load_ushort a104, v9, s[88:91], s62 offen offset:320\n"
"buffer_load_ushort a73, v9, s[88:91], s62 offen offset:576\n"
"buffer_load_ushort a105, v9, s[88:91], s62 offen offset:832\n"
"buffer_load_ushort a74, v9, s[88:91], s63 offen offset:64\n"
"buffer_load_ushort a106, v9, s[88:91], s63 offen offset:320\n"
"buffer_load_ushort a75, v9, s[88:91], s63 offen offset:576\n"
"buffer_load_ushort a107, v9, s[88:91], s63 offen offset:832\n"
"buffer_load_ushort a76, v9, s[88:91], s62 offen offset:96\n"
"buffer_load_ushort a108, v9, s[88:91], s62 offen offset:352\n"
"buffer_load_ushort a77, v9, s[88:91], s62 offen offset:608\n"
"buffer_load_ushort a109, v9, s[88:91], s62 offen offset:864\n"
"buffer_load_ushort a78, v9, s[88:91], s63 offen offset:96\n"
"buffer_load_ushort a110, v9, s[88:91], s63 offen offset:352\n"
"buffer_load_ushort a79, v9, s[88:91], s63 offen offset:608\n"
"buffer_load_ushort a111, v9, s[88:91], s63 offen offset:864\n"
"s_waitcnt vmcnt(0)\n"
"buffer_load_ushort a80, v10, s[88:91], s62 offen offset:0\n"
"buffer_load_ushort a112, v10, s[88:91], s62 offen offset:256\n"
"buffer_load_ushort a81, v10, s[88:91], s62 offen offset:512\n"
"buffer_load_ushort a113, v10, s[88:91], s62 offen offset:768\n"
"buffer_load_ushort a82, v10, s[88:91], s63 offen offset:0\n"
"buffer_load_ushort a114, v10, s[88:91], s63 offen offset:256\n"
"buffer_load_ushort a83, v10, s[88:91], s63 offen offset:512\n"
"buffer_load_ushort a115, v10, s[88:91], s63 offen offset:768\n"
"buffer_load_ushort a84, v10, s[88:91], s62 offen offset:32\n"
"buffer_load_ushort a116, v10, s[88:91], s62 offen offset:288\n"
"buffer_load_ushort a85, v10, s[88:91], s62 offen offset:544\n"
"buffer_load_ushort a117, v10, s[88:91], s62 offen offset:800\n"
"buffer_load_ushort a86, v10, s[88:91], s63 offen offset:32\n"
"buffer_load_ushort a118, v10, s[88:91], s63 offen offset:288\n"
"buffer_load_ushort a87, v10, s[88:91], s63 offen offset:544\n"
"buffer_load_ushort a119, v10, s[88:91], s63 offen offset:800\n"
"s_waitcnt vmcnt(0)\n"
"buffer_load_ushort a88, v10, s[88:91], s62 offen offset:64\n"
"buffer_load_ushort a120, v10, s[88:91], s62 offen offset:320\n"
"buffer_load_ushort a89, v10, s[88:91], s62 offen offset:576\n"
"buffer_load_ushort a121, v10, s[88:91], s62 offen offset:832\n"
"buffer_load_ushort a90, v10, s[88:91], s63 offen offset:64\n"
"buffer_load_ushort a122, v10, s[88:91], s63 offen offset:320\n"
"buffer_load_ushort a91, v10, s[88:91], s63 offen offset:576\n"
"buffer_load_ushort a123, v10, s[88:91], s63 offen offset:832\n"
"buffer_load_ushort a92, v10, s[88:91], s62 offen offset:96\n"
"buffer_load_ushort a124, v10, s[88:91], s62 offen offset:352\n"
"buffer_load_ushort a93, v10, s[88:91], s62 offen offset:608\n"
"buffer_load_ushort a125, v10, s[88:91], s62 offen offset:864\n"
"buffer_load_ushort a94, v10, s[88:91], s63 offen offset:96\n"
"buffer_load_ushort a126, v10, s[88:91], s63 offen offset:352\n"
"buffer_load_ushort a95, v10, s[88:91], s63 offen offset:608\n"
"buffer_load_ushort a127, v10, s[88:91], s63 offen offset:864\n"
"s_waitcnt vmcnt(0)\n"
"v_accvgpr_read_b32 v7, a64\n"
"v_accvgpr_read_b32 v8, a96\n"
"v_lshl_or_b32 v7, v8, 16, v7\n"
"v_accvgpr_write_b32 a64, v7\n"
"v_accvgpr_read_b32 v7, a65\n"
"v_accvgpr_read_b32 v8, a97\n"
"v_lshl_or_b32 v7, v8, 16, v7\n"
"v_accvgpr_write_b32 a65, v7\n"
"v_accvgpr_read_b32 v7, a66\n"
"v_accvgpr_read_b32 v8, a98\n"
"v_lshl_or_b32 v7, v8, 16, v7\n"
"v_accvgpr_write_b32 a66, v7\n"
"v_accvgpr_read_b32 v7, a67\n"
"v_accvgpr_read_b32 v8, a99\n"
"v_lshl_or_b32 v7, v8, 16, v7\n"
"v_accvgpr_write_b32 a67, v7\n"
"v_accvgpr_read_b32 v7, a68\n"
"v_accvgpr_read_b32 v8, a100\n"
"v_lshl_or_b32 v7, v8, 16, v7\n"
"v_accvgpr_write_b32 a68, v7\n"
"v_accvgpr_read_b32 v7, a69\n"
"v_accvgpr_read_b32 v8, a101\n"
"v_lshl_or_b32 v7, v8, 16, v7\n"
"v_accvgpr_write_b32 a69, v7\n"
"v_accvgpr_read_b32 v7, a70\n"
"v_accvgpr_read_b32 v8, a102\n"
"v_lshl_or_b32 v7, v8, 16, v7\n"
"v_accvgpr_write_b32 a70, v7\n"
"v_accvgpr_read_b32 v7, a71\n"
"v_accvgpr_read_b32 v8, a103\n"
"v_lshl_or_b32 v7, v8, 16, v7\n"
"v_accvgpr_write_b32 a71, v7\n"
"v_accvgpr_read_b32 v7, a72\n"
"v_accvgpr_read_b32 v8, a104\n"
"v_lshl_or_b32 v7, v8, 16, v7\n"
"v_accvgpr_write_b32 a72, v7\n"
"v_accvgpr_read_b32 v7, a73\n"
"v_accvgpr_read_b32 v8, a105\n"
"v_lshl_or_b32 v7, v8, 16, v7\n"
"v_accvgpr_write_b32 a73, v7\n"
"v_accvgpr_read_b32 v7, a74\n"
"v_accvgpr_read_b32 v8, a106\n"
"v_lshl_or_b32 v7, v8, 16, v7\n"
"v_accvgpr_write_b32 a74, v7\n"
"v_accvgpr_read_b32 v7, a75\n"
"v_accvgpr_read_b32 v8, a107\n"
"v_lshl_or_b32 v7, v8, 16, v7\n"
"v_accvgpr_write_b32 a75, v7\n"
"v_accvgpr_read_b32 v7, a76\n"
"v_accvgpr_read_b32 v8, a108\n"
"v_lshl_or_b32 v7, v8, 16, v7\n"
"v_accvgpr_write_b32 a76, v7\n"
"v_accvgpr_read_b32 v7, a77\n"
"v_accvgpr_read_b32 v8, a109\n"
"v_lshl_or_b32 v7, v8, 16, v7\n"
"v_accvgpr_write_b32 a77, v7\n"
"v_accvgpr_read_b32 v7, a78\n"
"v_accvgpr_read_b32 v8, a110\n"
"v_lshl_or_b32 v7, v8, 16, v7\n"
"v_accvgpr_write_b32 a78, v7\n"
"v_accvgpr_read_b32 v7, a79\n"
"v_accvgpr_read_b32 v8, a111\n"
"v_lshl_or_b32 v7, v8, 16, v7\n"
"v_accvgpr_write_b32 a79, v7\n"
"v_accvgpr_read_b32 v7, a80\n"
"v_accvgpr_read_b32 v8, a112\n"
"v_lshl_or_b32 v7, v8, 16, v7\n"
"v_accvgpr_write_b32 a80, v7\n"
"v_accvgpr_read_b32 v7, a81\n"
"v_accvgpr_read_b32 v8, a113\n"
"v_lshl_or_b32 v7, v8, 16, v7\n"
"v_accvgpr_write_b32 a81, v7\n"
"v_accvgpr_read_b32 v7, a82\n"
"v_accvgpr_read_b32 v8, a114\n"
"v_lshl_or_b32 v7, v8, 16, v7\n"
"v_accvgpr_write_b32 a82, v7\n"
"v_accvgpr_read_b32 v7, a83\n"
"v_accvgpr_read_b32 v8, a115\n"
"v_lshl_or_b32 v7, v8, 16, v7\n"
"v_accvgpr_write_b32 a83, v7\n"
"v_accvgpr_read_b32 v7, a84\n"
"v_accvgpr_read_b32 v8, a116\n"
"v_lshl_or_b32 v7, v8, 16, v7\n"
"v_accvgpr_write_b32 a84, v7\n"
"v_accvgpr_read_b32 v7, a85\n"
"v_accvgpr_read_b32 v8, a117\n"
"v_lshl_or_b32 v7, v8, 16, v7\n"
"v_accvgpr_write_b32 a85, v7\n"
"v_accvgpr_read_b32 v7, a86\n"
"v_accvgpr_read_b32 v8, a118\n"
"v_lshl_or_b32 v7, v8, 16, v7\n"
"v_accvgpr_write_b32 a86, v7\n"
"v_accvgpr_read_b32 v7, a87\n"
"v_accvgpr_read_b32 v8, a119\n"
"v_lshl_or_b32 v7, v8, 16, v7\n"
"v_accvgpr_write_b32 a87, v7\n"
"v_accvgpr_read_b32 v7, a88\n"
"v_accvgpr_read_b32 v8, a120\n"
"v_lshl_or_b32 v7, v8, 16, v7\n"
"v_accvgpr_write_b32 a88, v7\n"
"v_accvgpr_read_b32 v7, a89\n"
"v_accvgpr_read_b32 v8, a121\n"
"v_lshl_or_b32 v7, v8, 16, v7\n"
"v_accvgpr_write_b32 a89, v7\n"
"v_accvgpr_read_b32 v7, a90\n"
"v_accvgpr_read_b32 v8, a122\n"
"v_lshl_or_b32 v7, v8, 16, v7\n"
"v_accvgpr_write_b32 a90, v7\n"
"v_accvgpr_read_b32 v7, a91\n"
"v_accvgpr_read_b32 v8, a123\n"
"v_lshl_or_b32 v7, v8, 16, v7\n"
"v_accvgpr_write_b32 a91, v7\n"
"v_accvgpr_read_b32 v7, a92\n"
"v_accvgpr_read_b32 v8, a124\n"
"v_lshl_or_b32 v7, v8, 16, v7\n"
"v_accvgpr_write_b32 a92, v7\n"
"v_accvgpr_read_b32 v7, a93\n"
"v_accvgpr_read_b32 v8, a125\n"
"v_lshl_or_b32 v7, v8, 16, v7\n"
"v_accvgpr_write_b32 a93, v7\n"
"v_accvgpr_read_b32 v7, a94\n"
"v_accvgpr_read_b32 v8, a126\n"
"v_lshl_or_b32 v7, v8, 16, v7\n"
"v_accvgpr_write_b32 a94, v7\n"
"v_accvgpr_read_b32 v7, a95\n"
"v_accvgpr_read_b32 v8, a127\n"
"v_lshl_or_b32 v7, v8, 16, v7\n"
"v_accvgpr_write_b32 a95, v7\n"
"s_nop 1\n"
"v_mfma_f32_16x16x32_bf16 v[48:51], a[0:3], v[56:59], 0\n"
"v_mfma_f32_16x16x32_bf16 v[48:51], a[4:7], v[60:63], v[48:51]\n"
"v_mfma_f32_16x16x32_bf16 v[48:51], a[8:11], v[64:67], v[48:51]\n"
"v_mfma_f32_16x16x32_bf16 v[48:51], a[12:15], v[68:71], v[48:51]\n"
"v_mfma_f32_16x16x32_bf16 v[52:55], a[16:19], v[56:59], 0\n"
"v_mfma_f32_16x16x32_bf16 v[52:55], a[20:23], v[60:63], v[52:55]\n"
"v_mfma_f32_16x16x32_bf16 v[52:55], a[24:27], v[64:67], v[52:55]\n"
"v_mfma_f32_16x16x32_bf16 v[52:55], a[28:31], v[68:71], v[52:55]\n"
"s_nop 7\n"
"v_max3_f32 v7, v48, v49, v50\n"
"v_max3_f32 v7, v7, v51, v52\n"
"v_max3_f32 v7, v7, v53, v54\n"
"v_max3_f32 v7, v7, v55, v32\n"
"v_mov_b32 v8, v7\n"
"s_nop 1\n"
"v_permlane16_swap_b32 v7, v8\n"
"v_max_f32 v7, v7, v8\n"
"v_mov_b32 v8, v7\n"
"s_nop 1\n"
"v_permlane32_swap_b32 v7, v8\n"
"v_max_f32 v7, v7, v8\n"
"v_sub_f32 v8, v32, v7\n"
"v_exp_f32 v8, v8\n"
"v_mov_b32 v32, v7\n"
"v_sub_f32 v48, v48, v7\n"
"v_sub_f32 v49, v49, v7\n"
"v_sub_f32 v50, v50, v7\n"
"v_sub_f32 v51, v51, v7\n"
"v_sub_f32 v52, v52, v7\n"
"v_sub_f32 v53, v53, v7\n"
"v_sub_f32 v54, v54, v7\n"
"v_sub_f32 v55, v55, v7\n"
"v_exp_f32 v48, v48\n"
"v_exp_f32 v49, v49\n"
"v_exp_f32 v50, v50\n"
"v_exp_f32 v51, v51\n"
"v_exp_f32 v52, v52\n"
"v_exp_f32 v53, v53\n"
"v_exp_f32 v54, v54\n"
"v_exp_f32 v55, v55\n"
"v_mul_f32 v24, v24, v8\n"
"v_add_f32 v24, v24, v48\n"
"v_add_f32 v24, v24, v49\n"
"v_add_f32 v24, v24, v50\n"
"v_add_f32 v24, v24, v51\n"
"v_add_f32 v24, v24, v52\n"
"v_add_f32 v24, v24, v53\n"
"v_add_f32 v24, v24, v54\n"
"v_add_f32 v24, v24, v55\n"
"v_cvt_pk_bf16_f32 v36, v48, v49\n"
"v_cvt_pk_bf16_f32 v37, v50, v51\n"
"v_cvt_pk_bf16_f32 v38, v52, v53\n"
"v_cvt_pk_bf16_f32 v39, v54, v55\n"
"v_mul_f32 v120, v120, v8\n"
"v_mul_f32 v121, v121, v8\n"
"v_mul_f32 v122, v122, v8\n"
"v_mul_f32 v123, v123, v8\n"
"v_mul_f32 v124, v124, v8\n"
"v_mul_f32 v125, v125, v8\n"
"v_mul_f32 v126, v126, v8\n"
"v_mul_f32 v127, v127, v8\n"
"v_mul_f32 v128, v128, v8\n"
"v_mul_f32 v129, v129, v8\n"
"v_mul_f32 v130, v130, v8\n"
"v_mul_f32 v131, v131, v8\n"
"v_mul_f32 v132, v132, v8\n"
"v_mul_f32 v133, v133, v8\n"
"v_mul_f32 v134, v134, v8\n"
"v_mul_f32 v135, v135, v8\n"
"v_mul_f32 v136, v136, v8\n"
"v_mul_f32 v137, v137, v8\n"
"v_mul_f32 v138, v138, v8\n"
"v_mul_f32 v139, v139, v8\n"
"v_mul_f32 v140, v140, v8\n"
"v_mul_f32 v141, v141, v8\n"
"v_mul_f32 v142, v142, v8\n"
"v_mul_f32 v143, v143, v8\n"
"v_mul_f32 v144, v144, v8\n"
"v_mul_f32 v145, v145, v8\n"
"v_mul_f32 v146, v146, v8\n"
"v_mul_f32 v147, v147, v8\n"
"v_mul_f32 v148, v148, v8\n"
"v_mul_f32 v149, v149, v8\n"
"v_mul_f32 v150, v150, v8\n"
"v_mul_f32 v151, v151, v8\n"
"s_nop 1\n"
"v_mfma_f32_16x16x32_bf16 v[120:123], a[64:67], v[36:39], v[120:123]\n"
"v_mfma_f32_16x16x32_bf16 v[124:127], a[68:71], v[36:39], v[124:127]\n"
"v_mfma_f32_16x16x32_bf16 v[128:131], a[72:75], v[36:39], v[128:131]\n"
"v_mfma_f32_16x16x32_bf16 v[132:135], a[76:79], v[36:39], v[132:135]\n"
"v_mfma_f32_16x16x32_bf16 v[136:139], a[80:83], v[36:39], v[136:139]\n"
"v_mfma_f32_16x16x32_bf16 v[140:143], a[84:87], v[36:39], v[140:143]\n"
"v_mfma_f32_16x16x32_bf16 v[144:147], a[88:91], v[36:39], v[144:147]\n"
"v_mfma_f32_16x16x32_bf16 v[148:151], a[92:95], v[36:39], v[148:151]\n"
"v_mfma_f32_16x16x32_bf16 v[48:51], a[0:3], v[72:75], 0\n"
"v_mfma_f32_16x16x32_bf16 v[48:51], a[4:7], v[76:79], v[48:51]\n"
"v_mfma_f32_16x16x32_bf16 v[48:51], a[8:11], v[80:83], v[48:51]\n"
"v_mfma_f32_16x16x32_bf16 v[48:51], a[12:15], v[84:87], v[48:51]\n"
"v_mfma_f32_16x16x32_bf16 v[52:55], a[16:19], v[72:75], 0\n"
"v_mfma_f32_16x16x32_bf16 v[52:55], a[20:23], v[76:79], v[52:55]\n"
"v_mfma_f32_16x16x32_bf16 v[52:55], a[24:27], v[80:83], v[52:55]\n"
"v_mfma_f32_16x16x32_bf16 v[52:55], a[28:31], v[84:87], v[52:55]\n"
"s_nop 7\n"
"v_max3_f32 v7, v48, v49, v50\n"
"v_max3_f32 v7, v7, v51, v52\n"
"v_max3_f32 v7, v7, v53, v54\n"
"v_max3_f32 v7, v7, v55, v33\n"
"v_mov_b32 v8, v7\n"
"s_nop 1\n"
"v_permlane16_swap_b32 v7, v8\n"
"v_max_f32 v7, v7, v8\n"
"v_mov_b32 v8, v7\n"
"s_nop 1\n"
"v_permlane32_swap_b32 v7, v8\n"
"v_max_f32 v7, v7, v8\n"
"v_sub_f32 v8, v33, v7\n"
"v_exp_f32 v8, v8\n"
"v_mov_b32 v33, v7\n"
"v_sub_f32 v48, v48, v7\n"
"v_sub_f32 v49, v49, v7\n"
"v_sub_f32 v50, v50, v7\n"
"v_sub_f32 v51, v51, v7\n"
"v_sub_f32 v52, v52, v7\n"
"v_sub_f32 v53, v53, v7\n"
"v_sub_f32 v54, v54, v7\n"
"v_sub_f32 v55, v55, v7\n"
"v_exp_f32 v48, v48\n"
"v_exp_f32 v49, v49\n"
"v_exp_f32 v50, v50\n"
"v_exp_f32 v51, v51\n"
"v_exp_f32 v52, v52\n"
"v_exp_f32 v53, v53\n"
"v_exp_f32 v54, v54\n"
"v_exp_f32 v55, v55\n"
"v_mul_f32 v40, v40, v8\n"
"v_add_f32 v40, v40, v48\n"
"v_add_f32 v40, v40, v49\n"
"v_add_f32 v40, v40, v50\n"
"v_add_f32 v40, v40, v51\n"
"v_add_f32 v40, v40, v52\n"
"v_add_f32 v40, v40, v53\n"
"v_add_f32 v40, v40, v54\n"
"v_add_f32 v40, v40, v55\n"
"v_cvt_pk_bf16_f32 v36, v48, v49\n"
"v_cvt_pk_bf16_f32 v37, v50, v51\n"
"v_cvt_pk_bf16_f32 v38, v52, v53\n"
"v_cvt_pk_bf16_f32 v39, v54, v55\n"
"v_mul_f32 v152, v152, v8\n"
"v_mul_f32 v153, v153, v8\n"
"v_mul_f32 v154, v154, v8\n"
"v_mul_f32 v155, v155, v8\n"
"v_mul_f32 v156, v156, v8\n"
"v_mul_f32 v157, v157, v8\n"
"v_mul_f32 v158, v158, v8\n"
"v_mul_f32 v159, v159, v8\n"
"v_mul_f32 v160, v160, v8\n"
"v_mul_f32 v161, v161, v8\n"
"v_mul_f32 v162, v162, v8\n"
"v_mul_f32 v163, v163, v8\n"
"v_mul_f32 v164, v164, v8\n"
"v_mul_f32 v165, v165, v8\n"
"v_mul_f32 v166, v166, v8\n"
"v_mul_f32 v167, v167, v8\n"
"v_mul_f32 v168, v168, v8\n"
"v_mul_f32 v169, v169, v8\n"
"v_mul_f32 v170, v170, v8\n"
"v_mul_f32 v171, v171, v8\n"
"v_mul_f32 v172, v172, v8\n"
"v_mul_f32 v173, v173, v8\n"
"v_mul_f32 v174, v174, v8\n"
"v_mul_f32 v175, v175, v8\n"
"v_mul_f32 v176, v176, v8\n"
"v_mul_f32 v177, v177, v8\n"
"v_mul_f32 v178, v178, v8\n"
"v_mul_f32 v179, v179, v8\n"
"v_mul_f32 v180, v180, v8\n"
"v_mul_f32 v181, v181, v8\n"
"v_mul_f32 v182, v182, v8\n"
"v_mul_f32 v183, v183, v8\n"
"s_nop 1\n"
"v_mfma_f32_16x16x32_bf16 v[152:155], a[64:67], v[36:39], v[152:155]\n"
"v_mfma_f32_16x16x32_bf16 v[156:159], a[68:71], v[36:39], v[156:159]\n"
"v_mfma_f32_16x16x32_bf16 v[160:163], a[72:75], v[36:39], v[160:163]\n"
"v_mfma_f32_16x16x32_bf16 v[164:167], a[76:79], v[36:39], v[164:167]\n"
"v_mfma_f32_16x16x32_bf16 v[168:171], a[80:83], v[36:39], v[168:171]\n"
"v_mfma_f32_16x16x32_bf16 v[172:175], a[84:87], v[36:39], v[172:175]\n"
"v_mfma_f32_16x16x32_bf16 v[176:179], a[88:91], v[36:39], v[176:179]\n"
"v_mfma_f32_16x16x32_bf16 v[180:183], a[92:95], v[36:39], v[180:183]\n"
"v_mfma_f32_16x16x32_bf16 v[48:51], a[0:3], v[88:91], 0\n"
"v_mfma_f32_16x16x32_bf16 v[48:51], a[4:7], v[92:95], v[48:51]\n"
"v_mfma_f32_16x16x32_bf16 v[48:51], a[8:11], v[96:99], v[48:51]\n"
"v_mfma_f32_16x16x32_bf16 v[48:51], a[12:15], v[100:103], v[48:51]\n"
"v_mfma_f32_16x16x32_bf16 v[52:55], a[16:19], v[88:91], 0\n"
"v_mfma_f32_16x16x32_bf16 v[52:55], a[20:23], v[92:95], v[52:55]\n"
"v_mfma_f32_16x16x32_bf16 v[52:55], a[24:27], v[96:99], v[52:55]\n"
"v_mfma_f32_16x16x32_bf16 v[52:55], a[28:31], v[100:103], v[52:55]\n"
"s_nop 7\n"
"v_max3_f32 v7, v48, v49, v50\n"
"v_max3_f32 v7, v7, v51, v52\n"
"v_max3_f32 v7, v7, v53, v54\n"
"v_max3_f32 v7, v7, v55, v34\n"
"v_mov_b32 v8, v7\n"
"s_nop 1\n"
"v_permlane16_swap_b32 v7, v8\n"
"v_max_f32 v7, v7, v8\n"
"v_mov_b32 v8, v7\n"
"s_nop 1\n"
"v_permlane32_swap_b32 v7, v8\n"
"v_max_f32 v7, v7, v8\n"
"v_sub_f32 v8, v34, v7\n"
"v_exp_f32 v8, v8\n"
"v_mov_b32 v34, v7\n"
"v_sub_f32 v48, v48, v7\n"
"v_sub_f32 v49, v49, v7\n"
"v_sub_f32 v50, v50, v7\n"
"v_sub_f32 v51, v51, v7\n"
"v_sub_f32 v52, v52, v7\n"
"v_sub_f32 v53, v53, v7\n"
"v_sub_f32 v54, v54, v7\n"
"v_sub_f32 v55, v55, v7\n"
"v_exp_f32 v48, v48\n"
"v_exp_f32 v49, v49\n"
"v_exp_f32 v50, v50\n"
"v_exp_f32 v51, v51\n"
"v_exp_f32 v52, v52\n"
"v_exp_f32 v53, v53\n"
"v_exp_f32 v54, v54\n"
"v_exp_f32 v55, v55\n"
"v_mul_f32 v44, v44, v8\n"
"v_add_f32 v44, v44, v48\n"
"v_add_f32 v44, v44, v49\n"
"v_add_f32 v44, v44, v50\n"
"v_add_f32 v44, v44, v51\n"
"v_add_f32 v44, v44, v52\n"
"v_add_f32 v44, v44, v53\n"
"v_add_f32 v44, v44, v54\n"
"v_add_f32 v44, v44, v55\n"
"v_cvt_pk_bf16_f32 v36, v48, v49\n"
"v_cvt_pk_bf16_f32 v37, v50, v51\n"
"v_cvt_pk_bf16_f32 v38, v52, v53\n"
"v_cvt_pk_bf16_f32 v39, v54, v55\n"
"v_mul_f32 v184, v184, v8\n"
"v_mul_f32 v185, v185, v8\n"
"v_mul_f32 v186, v186, v8\n"
"v_mul_f32 v187, v187, v8\n"
"v_mul_f32 v188, v188, v8\n"
"v_mul_f32 v189, v189, v8\n"
"v_mul_f32 v190, v190, v8\n"
"v_mul_f32 v191, v191, v8\n"
"v_mul_f32 v192, v192, v8\n"
"v_mul_f32 v193, v193, v8\n"
"v_mul_f32 v194, v194, v8\n"
"v_mul_f32 v195, v195, v8\n"
"v_mul_f32 v196, v196, v8\n"
"v_mul_f32 v197, v197, v8\n"
"v_mul_f32 v198, v198, v8\n"
"v_mul_f32 v199, v199, v8\n"
"v_mul_f32 v200, v200, v8\n"
"v_mul_f32 v201, v201, v8\n"
"v_mul_f32 v202, v202, v8\n"
"v_mul_f32 v203, v203, v8\n"
"v_mul_f32 v204, v204, v8\n"
"v_mul_f32 v205, v205, v8\n"
"v_mul_f32 v206, v206, v8\n"
"v_mul_f32 v207, v207, v8\n"
"v_mul_f32 v208, v208, v8\n"
"v_mul_f32 v209, v209, v8\n"
"v_mul_f32 v210, v210, v8\n"
"v_mul_f32 v211, v211, v8\n"
"v_mul_f32 v212, v212, v8\n"
"v_mul_f32 v213, v213, v8\n"
"v_mul_f32 v214, v214, v8\n"
"v_mul_f32 v215, v215, v8\n"
"s_nop 1\n"
"v_mfma_f32_16x16x32_bf16 v[184:187], a[64:67], v[36:39], v[184:187]\n"
"v_mfma_f32_16x16x32_bf16 v[188:191], a[68:71], v[36:39], v[188:191]\n"
"v_mfma_f32_16x16x32_bf16 v[192:195], a[72:75], v[36:39], v[192:195]\n"
"v_mfma_f32_16x16x32_bf16 v[196:199], a[76:79], v[36:39], v[196:199]\n"
"v_mfma_f32_16x16x32_bf16 v[200:203], a[80:83], v[36:39], v[200:203]\n"
"v_mfma_f32_16x16x32_bf16 v[204:207], a[84:87], v[36:39], v[204:207]\n"
"v_mfma_f32_16x16x32_bf16 v[208:211], a[88:91], v[36:39], v[208:211]\n"
"v_mfma_f32_16x16x32_bf16 v[212:215], a[92:95], v[36:39], v[212:215]\n"
"v_mfma_f32_16x16x32_bf16 v[48:51], a[0:3], v[104:107], 0\n"
"v_mfma_f32_16x16x32_bf16 v[48:51], a[4:7], v[108:111], v[48:51]\n"
"v_mfma_f32_16x16x32_bf16 v[48:51], a[8:11], v[112:115], v[48:51]\n"
"v_mfma_f32_16x16x32_bf16 v[48:51], a[12:15], v[116:119], v[48:51]\n"
"v_mfma_f32_16x16x32_bf16 v[52:55], a[16:19], v[104:107], 0\n"
"v_mfma_f32_16x16x32_bf16 v[52:55], a[20:23], v[108:111], v[52:55]\n"
"v_mfma_f32_16x16x32_bf16 v[52:55], a[24:27], v[112:115], v[52:55]\n"
"v_mfma_f32_16x16x32_bf16 v[52:55], a[28:31], v[116:119], v[52:55]\n"
"s_nop 7\n"
"v_max3_f32 v7, v48, v49, v50\n"
"v_max3_f32 v7, v7, v51, v52\n"
"v_max3_f32 v7, v7, v53, v54\n"
"v_max3_f32 v7, v7, v55, v35\n"
"v_mov_b32 v8, v7\n"
"s_nop 1\n"
"v_permlane16_swap_b32 v7, v8\n"
"v_max_f32 v7, v7, v8\n"
"v_mov_b32 v8, v7\n"
"s_nop 1\n"
"v_permlane32_swap_b32 v7, v8\n"
"v_max_f32 v7, v7, v8\n"
"v_sub_f32 v8, v35, v7\n"
"v_exp_f32 v8, v8\n"
"v_mov_b32 v35, v7\n"
"v_sub_f32 v48, v48, v7\n"
"v_sub_f32 v49, v49, v7\n"
"v_sub_f32 v50, v50, v7\n"
"v_sub_f32 v51, v51, v7\n"
"v_sub_f32 v52, v52, v7\n"
"v_sub_f32 v53, v53, v7\n"
"v_sub_f32 v54, v54, v7\n"
"v_sub_f32 v55, v55, v7\n"
"v_exp_f32 v48, v48\n"
"v_exp_f32 v49, v49\n"
"v_exp_f32 v50, v50\n"
"v_exp_f32 v51, v51\n"
"v_exp_f32 v52, v52\n"
"v_exp_f32 v53, v53\n"
"v_exp_f32 v54, v54\n"
"v_exp_f32 v55, v55\n"
"v_mul_f32 v28, v28, v8\n"
"v_add_f32 v28, v28, v48\n"
"v_add_f32 v28, v28, v49\n"
"v_add_f32 v28, v28, v50\n"
"v_add_f32 v28, v28, v51\n"
"v_add_f32 v28, v28, v52\n"
"v_add_f32 v28, v28, v53\n"
"v_add_f32 v28, v28, v54\n"
"v_add_f32 v28, v28, v55\n"
"v_cvt_pk_bf16_f32 v36, v48, v49\n"
"v_cvt_pk_bf16_f32 v37, v50, v51\n"
"v_cvt_pk_bf16_f32 v38, v52, v53\n"
"v_cvt_pk_bf16_f32 v39, v54, v55\n"
"v_mul_f32 v216, v216, v8\n"
"v_mul_f32 v217, v217, v8\n"
"v_mul_f32 v218, v218, v8\n"
"v_mul_f32 v219, v219, v8\n"
"v_mul_f32 v220, v220, v8\n"
"v_mul_f32 v221, v221, v8\n"
"v_mul_f32 v222, v222, v8\n"
"v_mul_f32 v223, v223, v8\n"
"v_mul_f32 v224, v224, v8\n"
"v_mul_f32 v225, v225, v8\n"
"v_mul_f32 v226, v226, v8\n"
"v_mul_f32 v227, v227, v8\n"
"v_mul_f32 v228, v228, v8\n"
"v_mul_f32 v229, v229, v8\n"
"v_mul_f32 v230, v230, v8\n"
"v_mul_f32 v231, v231, v8\n"
"s_nop 1\n"
"v_mfma_f32_16x16x32_bf16 v[216:219], a[64:67], v[36:39], v[216:219]\n"
"v_mfma_f32_16x16x32_bf16 v[220:223], a[68:71], v[36:39], v[220:223]\n"
"v_mfma_f32_16x16x32_bf16 v[224:227], a[72:75], v[36:39], v[224:227]\n"
"v_mfma_f32_16x16x32_bf16 v[228:231], a[76:79], v[36:39], v[228:231]\n"
"s_add_u32 s62, s62, 8192\n"
"s_cmp_lt_u32 s62, 262144\n"
"s_nop 7\n"
"s_cbranch_scc1 L_sl_%=\n"
"v_mfma_f32_16x16x4_f32 a[0:3], v252, v24, 0\n"
"v_mfma_f32_16x16x4_f32 a[4:7], v252, v40, 0\n"
"v_mfma_f32_16x16x4_f32 a[8:11], v252, v44, 0\n"
"v_mfma_f32_16x16x4_f32 a[12:15], v252, v28, 0\n"
"s_nop 7\n"
"s_nop 7\n"
"v_accvgpr_read_b32 v24, a0\n"
"v_accvgpr_read_b32 v40, a4\n"
"v_accvgpr_read_b32 v44, a8\n"
"v_accvgpr_read_b32 v28, a12\n"
"v_rcp_f32 v32, v24\n"
"v_rcp_f32 v34, v40\n"
"v_rcp_f32 v35, v44\n"
"v_rcp_f32 v33, v28\n"
"s_add_u32 s62, s35, 2\n"
"s_and_b32 s62, s62, 7\n"
"s_lshl_b32 s62, s62, 17\n"
"s_add_u32 s63, s81, s62\n"
"s_add_u32 m0, s83, 49152\n"
"s_add_u32 s76, s63, 0\n"
"buffer_load_dwordx4 v12, s[40:43], s76 offen lds\n"
"s_add_u32 m0, s83, 50176\n"
"s_add_u32 s77, s63, 8192\n"
"buffer_load_dwordx4 v12, s[40:43], s77 offen lds\n"
"s_add_u32 m0, s83, 51200\n"
"s_add_u32 s76, s63, 16384\n"
"buffer_load_dwordx4 v12, s[40:43], s76 offen lds\n"
"s_add_u32 m0, s83, 52224\n"
"s_add_u32 s77, s63, 24576\n"
"buffer_load_dwordx4 v12, s[40:43], s77 offen lds\n"
"s_waitcnt vmcnt(0)\n"
"ds_read_b64_tr_b16 a[0:1], v1 offset:49152\n"
"ds_read_b64_tr_b16 a[2:3], v1 offset:51200\n"
"ds_read_b64_tr_b16 a[4:5], v2 offset:49152\n"
"ds_read_b64_tr_b16 a[6:7], v2 offset:51200\n"
"ds_read_b64_tr_b16 a[8:9], v1 offset:49664\n"
"ds_read_b64_tr_b16 a[10:11], v1 offset:51712\n"
"ds_read_b64_tr_b16 a[12:13], v2 offset:49664\n"
"ds_read_b64_tr_b16 a[14:15], v2 offset:51712\n"
"s_waitcnt lgkmcnt(0)\n"
"s_add_u32 s62, s35, 2\n"
"s_and_b32 s62, s62, 7\n"
"s_lshl_b32 s62, s62, 17\n"
"s_add_u32 s63, s81, s62\n"
"s_add_u32 m0, s83, 49152\n"
"s_add_u32 s76, s63, 32768\n"
"buffer_load_dwordx4 v12, s[40:43], s76 offen lds\n"
"s_add_u32 m0, s83, 50176\n"
"s_add_u32 s77, s63, 40960\n"
"buffer_load_dwordx4 v12, s[40:43], s77 offen lds\n"
"s_add_u32 m0, s83, 51200\n"
"s_add_u32 s76, s63, 49152\n"
"buffer_load_dwordx4 v12, s[40:43], s76 offen lds\n"
"s_add_u32 m0, s83, 52224\n"
"s_add_u32 s77, s63, 57344\n"
"buffer_load_dwordx4 v12, s[40:43], s77 offen lds\n"
"s_waitcnt vmcnt(0)\n"
"ds_read_b64_tr_b16 a[16:17], v1 offset:49152\n"
"ds_read_b64_tr_b16 a[18:19], v1 offset:51200\n"
"ds_read_b64_tr_b16 a[20:21], v2 offset:49152\n"
"ds_read_b64_tr_b16 a[22:23], v2 offset:51200\n"
"ds_read_b64_tr_b16 a[24:25], v1 offset:49664\n"
"ds_read_b64_tr_b16 a[26:27], v1 offset:51712\n"
"ds_read_b64_tr_b16 a[28:29], v2 offset:49664\n"
"ds_read_b64_tr_b16 a[30:31], v2 offset:51712\n"
"s_waitcnt lgkmcnt(0)\n"
"s_add_u32 s62, s35, 2\n"
"s_and_b32 s62, s62, 7\n"
"s_lshl_b32 s62, s62, 17\n"
"s_add_u32 s63, s81, s62\n"
"s_add_u32 m0, s83, 49152\n"
"s_add_u32 s76, s63, 65536\n"
"buffer_load_dwordx4 v12, s[40:43], s76 offen lds\n"
"s_add_u32 m0, s83, 50176\n"
"s_add_u32 s77, s63, 73728\n"
"buffer_load_dwordx4 v12, s[40:43], s77 offen lds\n"
"s_add_u32 m0, s83, 51200\n"
"s_add_u32 s76, s63, 81920\n"
"buffer_load_dwordx4 v12, s[40:43], s76 offen lds\n"
"s_add_u32 m0, s83, 52224\n"
"s_add_u32 s77, s63, 90112\n"
"buffer_load_dwordx4 v12, s[40:43], s77 offen lds\n"
"s_waitcnt vmcnt(0)\n"
"ds_read_b64_tr_b16 a[64:65], v1 offset:49152\n"
"ds_read_b64_tr_b16 a[66:67], v1 offset:51200\n"
"ds_read_b64_tr_b16 a[68:69], v2 offset:49152\n"
"ds_read_b64_tr_b16 a[70:71], v2 offset:51200\n"
"ds_read_b64_tr_b16 a[72:73], v1 offset:49664\n"
"ds_read_b64_tr_b16 a[74:75], v1 offset:51712\n"
"ds_read_b64_tr_b16 a[76:77], v2 offset:49664\n"
"ds_read_b64_tr_b16 a[78:79], v2 offset:51712\n"
"s_waitcnt lgkmcnt(0)\n"
"s_add_u32 s62, s35, 2\n"
"s_and_b32 s62, s62, 7\n"
"s_lshl_b32 s62, s62, 17\n"
"s_add_u32 s63, s81, s62\n"
"s_add_u32 m0, s83, 49152\n"
"s_add_u32 s76, s63, 98304\n"
"buffer_load_dwordx4 v12, s[40:43], s76 offen lds\n"
"s_add_u32 m0, s83, 50176\n"
"s_add_u32 s77, s63, 106496\n"
"buffer_load_dwordx4 v12, s[40:43], s77 offen lds\n"
"s_add_u32 m0, s83, 51200\n"
"s_add_u32 s76, s63, 114688\n"
"buffer_load_dwordx4 v12, s[40:43], s76 offen lds\n"
"s_add_u32 m0, s83, 52224\n"
"s_add_u32 s77, s63, 122880\n"
"buffer_load_dwordx4 v12, s[40:43], s77 offen lds\n"
"s_waitcnt vmcnt(0)\n"
"ds_read_b64_tr_b16 a[80:81], v1 offset:49152\n"
"ds_read_b64_tr_b16 a[82:83], v1 offset:51200\n"
"ds_read_b64_tr_b16 a[84:85], v2 offset:49152\n"
"ds_read_b64_tr_b16 a[86:87], v2 offset:51200\n"
"ds_read_b64_tr_b16 a[88:89], v1 offset:49664\n"
"ds_read_b64_tr_b16 a[90:91], v1 offset:51712\n"
"ds_read_b64_tr_b16 a[92:93], v2 offset:49664\n"
"ds_read_b64_tr_b16 a[94:95], v2 offset:51712\n"
"s_waitcnt lgkmcnt(0)\n"
"s_add_u32 s62, s35, 3\n"
"s_and_b32 s62, s62, 7\n"
"s_lshl_b32 s62, s62, 17\n"
"s_add_u32 s63, s81, s62\n"
"s_add_u32 m0, s83, 49152\n"
"s_add_u32 s76, s63, 0\n"
"buffer_load_dwordx4 v12, s[40:43], s76 offen lds\n"
"s_add_u32 m0, s83, 50176\n"
"s_add_u32 s77, s63, 8192\n"
"buffer_load_dwordx4 v12, s[40:43], s77 offen lds\n"
"s_add_u32 m0, s83, 51200\n"
"s_add_u32 s76, s63, 16384\n"
"buffer_load_dwordx4 v12, s[40:43], s76 offen lds\n"
"s_add_u32 m0, s83, 52224\n"
"s_add_u32 s77, s63, 24576\n"
"buffer_load_dwordx4 v12, s[40:43], s77 offen lds\n"
"s_waitcnt vmcnt(0)\n"
"ds_read_b64_tr_b16 a[32:33], v1 offset:49152\n"
"ds_read_b64_tr_b16 a[34:35], v1 offset:51200\n"
"ds_read_b64_tr_b16 a[36:37], v2 offset:49152\n"
"ds_read_b64_tr_b16 a[38:39], v2 offset:51200\n"
"ds_read_b64_tr_b16 a[40:41], v1 offset:49664\n"
"ds_read_b64_tr_b16 a[42:43], v1 offset:51712\n"
"ds_read_b64_tr_b16 a[44:45], v2 offset:49664\n"
"ds_read_b64_tr_b16 a[46:47], v2 offset:51712\n"
"s_waitcnt lgkmcnt(0)\n"
"s_add_u32 s62, s35, 3\n"
"s_and_b32 s62, s62, 7\n"
"s_lshl_b32 s62, s62, 17\n"
"s_add_u32 s63, s81, s62\n"
"s_add_u32 m0, s83, 49152\n"
"s_add_u32 s76, s63, 32768\n"
"buffer_load_dwordx4 v12, s[40:43], s76 offen lds\n"
"s_add_u32 m0, s83, 50176\n"
"s_add_u32 s77, s63, 40960\n"
"buffer_load_dwordx4 v12, s[40:43], s77 offen lds\n"
"s_add_u32 m0, s83, 51200\n"
"s_add_u32 s76, s63, 49152\n"
"buffer_load_dwordx4 v12, s[40:43], s76 offen lds\n"
"s_add_u32 m0, s83, 52224\n"
"s_add_u32 s77, s63, 57344\n"
"buffer_load_dwordx4 v12, s[40:43], s77 offen lds\n"
"s_waitcnt vmcnt(0)\n"
"ds_read_b64_tr_b16 a[48:49], v1 offset:49152\n"
"ds_read_b64_tr_b16 a[50:51], v1 offset:51200\n"
"ds_read_b64_tr_b16 a[52:53], v2 offset:49152\n"
"ds_read_b64_tr_b16 a[54:55], v2 offset:51200\n"
"ds_read_b64_tr_b16 a[56:57], v1 offset:49664\n"
"ds_read_b64_tr_b16 a[58:59], v1 offset:51712\n"
"ds_read_b64_tr_b16 a[60:61], v2 offset:49664\n"
"ds_read_b64_tr_b16 a[62:63], v2 offset:51712\n"
"s_waitcnt lgkmcnt(0)\n"
"s_add_u32 s76, s78, 0\n"
"v_pk_mul_f32 v[120:121], v[120:121], v[32:33] op_sel_hi:[1,0]\n"
"v_pk_mul_f32 v[122:123], v[122:123], v[32:33] op_sel_hi:[1,0]\n"
"v_pk_mul_f32 v[124:125], v[124:125], v[32:33] op_sel_hi:[1,0]\n"
"v_pk_mul_f32 v[126:127], v[126:127], v[32:33] op_sel_hi:[1,0]\n"
"v_cvt_pk_bf16_f32 v120, v120, v121\n"
"v_cvt_pk_bf16_f32 v121, v122, v123\n"
"v_cvt_pk_bf16_f32 v122, v124, v125\n"
"v_cvt_pk_bf16_f32 v123, v126, v127\n"
"buffer_store_dwordx4 v[120:123], v11, s[36:39], s76 offen sc1\n"
"s_add_u32 s77, s78, 1024\n"
"v_pk_mul_f32 v[128:129], v[128:129], v[32:33] op_sel_hi:[1,0]\n"
"v_pk_mul_f32 v[130:131], v[130:131], v[32:33] op_sel_hi:[1,0]\n"
"v_pk_mul_f32 v[132:133], v[132:133], v[32:33] op_sel_hi:[1,0]\n"
"v_pk_mul_f32 v[134:135], v[134:135], v[32:33] op_sel_hi:[1,0]\n"
"v_cvt_pk_bf16_f32 v128, v128, v129\n"
"v_cvt_pk_bf16_f32 v129, v130, v131\n"
"v_cvt_pk_bf16_f32 v130, v132, v133\n"
"v_cvt_pk_bf16_f32 v131, v134, v135\n"
"buffer_store_dwordx4 v[128:131], v11, s[36:39], s77 offen sc1\n"
"s_add_u32 s76, s79, 0\n"
"v_pk_mul_f32 v[136:137], v[136:137], v[32:33] op_sel_hi:[1,0]\n"
"v_pk_mul_f32 v[138:139], v[138:139], v[32:33] op_sel_hi:[1,0]\n"
"v_pk_mul_f32 v[140:141], v[140:141], v[32:33] op_sel_hi:[1,0]\n"
"v_pk_mul_f32 v[142:143], v[142:143], v[32:33] op_sel_hi:[1,0]\n"
"v_cvt_pk_bf16_f32 v136, v136, v137\n"
"v_cvt_pk_bf16_f32 v137, v138, v139\n"
"v_cvt_pk_bf16_f32 v138, v140, v141\n"
"v_cvt_pk_bf16_f32 v139, v142, v143\n"
"buffer_store_dwordx4 v[136:139], v11, s[36:39], s76 offen sc1\n"
"s_add_u32 s77, s79, 1024\n"
"v_pk_mul_f32 v[144:145], v[144:145], v[32:33] op_sel_hi:[1,0]\n"
"v_pk_mul_f32 v[146:147], v[146:147], v[32:33] op_sel_hi:[1,0]\n"
"v_pk_mul_f32 v[148:149], v[148:149], v[32:33] op_sel_hi:[1,0]\n"
"v_pk_mul_f32 v[150:151], v[150:151], v[32:33] op_sel_hi:[1,0]\n"
"v_cvt_pk_bf16_f32 v144, v144, v145\n"
"v_cvt_pk_bf16_f32 v145, v146, v147\n"
"v_cvt_pk_bf16_f32 v146, v148, v149\n"
"v_cvt_pk_bf16_f32 v147, v150, v151\n"
"buffer_store_dwordx4 v[144:147], v11, s[36:39], s77 offen sc1\n"
"s_waitcnt vmcnt(0)\n"
"s_branch L_norm_%=\n"
  : "+{v0}"(tid) : [ka]"s"(__builtin_amdgcn_kernarg_segment_ptr()), [bid]"s"(blockIdx.x), [ldsp]"s"((unsigned)(unsigned long long)lds) : "v1","v2","v3","v4","v5","v6","v7","v8","v9","v10","v11","v12","v13","v14","v15","v16","v17","v18","v19","v20","v21","v22","v23","v24","v25","v26","v27","v28","v29","v30","v31","v32","v33","v34","v35","v36","v37","v38","v39","v40","v41","v42","v43","v44","v45","v46","v47","v48","v49","v50","v51","v52","v53","v54","v55","v56","v57","v58","v59","v60","v61","v62","v63","v64","v65","v66","v67","v68","v69","v70","v71","v72","v73","v74","v75","v76","v77","v78","v79","v80","v81","v82","v83","v84","v85","v86","v87","v88","v89","v90","v91","v92","v93","v94","v95","v96","v97","v98","v99","v100","v101","v102","v103","v104","v105","v106","v107","v108","v109","v110","v111","v112","v113","v114","v115","v116","v117","v118","v119","v120","v121","v122","v123","v124","v125","v126","v127","v128","v129","v130","v131","v132","v133","v134","v135","v136","v137","v138","v139","v140","v141","v142","v143","v144","v145","v146","v147","v148","v149","v150","v151","v152","v153","v154","v155","v156","v157","v158","v159","v160","v161","v162","v163","v164","v165","v166","v167","v168","v169","v170","v171","v172","v173","v174","v175","v176","v177","v178","v179","v180","v181","v182","v183","v184","v185","v186","v187","v188","v189","v190","v191","v192","v193","v194","v195","v196","v197","v198","v199","v200","v201","v202","v203","v204","v205","v206","v207","v208","v209","v210","v211","v212","v213","v214","v215","v216","v217","v218","v219","v220","v221","v222","v223","v224","v225","v226","v227","v228","v229","v230","v231","v232","v233","v234","v235","v236","v237","v238","v239","v240","v241","v242","v243","v244","v245","v246","v247","v248","v249","v250","v251","v252","v253","v254","v255","a0","a1","a2","a3","a4","a5","a6","a7","a8","a9","a10","a11","a12","a13","a14","a15","a16","a17","a18","a19","a20","a21","a22","a23","a24","a25","a26","a27","a28","a29","a30","a31","a32","a33","a34","a35","a36","a37","a38","a39","a40","a41","a42","a43","a44","a45","a46","a47","a48","a49","a50","a51","a52","a53","a54","a55","a56","a57","a58","a59","a60","a61","a62","a63","a64","a65","a66","a67","a68","a69","a70","a71","a72","a73","a74","a75","a76","a77","a78","a79","a80","a81","a82","a83","a84","a85","a86","a87","a88","a89","a90","a91","a92","a93","a94","a95","a96","a97","a98","a99","a100","a101","a102","a103","a104","a105","a106","a107","a108","a109","a110","a111","a112","a113","a114","a115","a116","a117","a118","a119","a120","a121","a122","a123","a124","a125","a126","a127","a128","a129","a130","a131","a132","a133","a134","a135","a136","a137","a138","a139","a140","a141","a142","a143","a144","a145","a146","a147","a148","a149","a150","a151","a152","a153","a154","a155","a156","a157","a158","a159","a160","a161","a162","a163","a164","a165","a166","a167","a168","a169","a170","a171","a172","a173","a174","a175","a176","a177","a178","a179","a180","a181","a182","a183","a184","a185","a186","a187","a188","a189","a190","a191","a192","a193","a194","a195","a196","a197","a198","a199","a200","a201","a202","a203","a204","a205","a206","a207","a208","a209","a210","a211","a212","a213","a214","a215","a216","a217","a218","a219","a220","a221","a222","a223","a224","a225","a226","a227","a228","a229","a230","a231","a232","a233","a234","a235","a236","a237","a238","a239","a240","a241","a242","a243","a244","a245","a246","a247","a248","a249","a250","a251","a252","a253","a254","a255","s4","s5","s6","s7","s8","s9","s10","s11","s12","s13","s14","s15","s16","s17","s18","s19","s20","s21","s22","s23","s24","s25","s26","s27","s28","s29","s30","s31","s34","s35","s36","s37","s38","s39","s40","s41","s42","s43","s44","s45","s46","s47","s48","s49","s50","s51","s52","s53","s54","s55","s56","s57","s58","s59","s60","s61","s62","s63","s64","s65","s66","s67","s68","s69","s70","s71","s72","s73","s74","s75","s76","s77","s78","s79","s80","s81","s82","s83","s84","s85","s86","s87","s88","s89","s90","s91","s92","s93","s94","s95","s96","s97","s98","s99","vcc","scc","memory");
}

extern "C" void kernel_launch(void* const* d_in, const int* in_sizes, int n_in, void* d_out, int out_size, void* d_ws, size_t ws_size, hipStream_t stream) {
  gqa_fused<<<256,256,0,stream>>>(d_in[0],d_in[1],d_in[2],d_in[3],d_in[4],d_out,d_ws);
}
```
